```python
import torch

def _softmax(x, axis=-1):
    return torch.softmax(x, dim=axis)
def _gelu(x):
    return torch.nn.functional.gelu(x, approximate="tanh")
def _silu(x):
    return torch.nn.functional.silu(x)
def _sigmoid(x):
    return torch.sigmoid(x)
def _layernorm(x, axis=-1, eps=1e-5):
    m = x.mean(dim=axis, keepdim=True)
    v = x.var(dim=axis, keepdim=True, unbiased=False)
    return (x - m) / torch.sqrt(v + eps)
def _scatter_add(tgt, idx, src, axis=0):
    return tgt.clone().index_add_(axis, idx, src)
def _segment_sum(src, seg, n):
    out = torch.zeros((n,) + src.shape[1:], dtype=src.dtype)
    return out.index_add_(0, seg, src)
def _pad(x, pw):
    flat = tuple(v for pair in reversed(pw) for v in pair)
    return torch.nn.functional.pad(x, flat)

kernel_name = 'gated_mix__ssm_conv_s4d__dense_activation_silu__prompt'

def setup_inputs():

    _w = (lambda a: (lambda t: t.to(torch.bfloat16) if t.is_floating_point() else t)(torch.as_tensor(a)))
    def _A_setup():
        Bsz, L, D, N = 1, 2048, 640, 128
        return {
            "u": _w((lambda *s: torch.randn(*s, dtype=torch.bfloat16) * 0.05)(Bsz, L, D)),
            "log_A_real": _w((lambda *s: torch.randn(*s, dtype=torch.bfloat16) * 3.2)(D, N) * 0.5),
            "A_imag": _w((lambda *s: torch.randn(*s, dtype=torch.bfloat16) * 0.05)(D, N) * 3.14),
            "C_real": _w((lambda *s: torch.randn(*s, dtype=torch.bfloat16) * 0.1)(D, N) / N ** 0.5),
            "C_imag": _w((lambda *s: torch.randn(*s, dtype=torch.bfloat16) * 0.1)(D, N) / N ** 0.5),
            "log_dt": _w((lambda *s: torch.randn(*s, dtype=torch.bfloat16) * 0.05)(D) * 0.5 - 4.0),
            "D_skip": _w((lambda *s: torch.randn(*s, dtype=torch.bfloat16) * 0.05)(D)),
        }
    _A_in = _A_setup()

    def _B_setup():
        B, S, D, F = 1, 2048, 640, 1728
        return {
            "x": _w((lambda *s: torch.randn(*s, dtype=torch.bfloat16) * 0.05)(B, S, D)),
            "w1": _w((lambda *s: torch.randn(*s, dtype=torch.bfloat16) * 3.2)(D, F) * (D ** -0.5)),
            "w2": _w((lambda *s: torch.randn(*s, dtype=torch.bfloat16) * 0.1)(F, D) * (F ** -0.5)),
        }
    _B_in = _B_setup()

    return {
        "A_u": _A_in["u"],
        "A_log_A_real": _A_in["log_A_real"],
        "A_A_imag": _A_in["A_imag"],
        "A_C_real": _A_in["C_real"],
        "A_C_imag": _A_in["C_imag"],
        "A_log_dt": _A_in["log_dt"],
        "A_D_skip": _A_in["D_skip"],
        "B_x": _B_in["x"],
        "B_w1": _B_in["w1"],
        "B_w2": _B_in["w2"],
        "gate_w": _w((lambda *s: torch.randn(*s, dtype=torch.bfloat16) * 0.05)(640) * 16.0),
    }

def reference(A_u, A_log_A_real, A_A_imag, A_C_real, A_C_imag, A_log_dt, A_D_skip, B_x, B_w1, B_w2, gate_w):
    def _A_ref(u, log_A_real, A_imag, C_real, C_imag, log_dt, D_skip):
        L = u.shape[1]
        dt = torch.exp(log_dt)
        A = -torch.exp(log_A_real) + 1j * A_imag
        C = (C_real + 1j * C_imag) * dt[:, None]
        pos = (lambda x, dt: x.to(dt))(torch.arange(L), log_dt.dtype)
        P = torch.exp((A * dt[:, None])[:, :, None] * pos[None, None, :])
        K = 2.0 * torch.einsum("dn,dnl->dl", C, P).real
        L2 = 2 * L
        u_t = (lambda x, *d: x.permute(*d))(u, 0, 2, 1)
        Kf = (lambda x, *a, **k: torch.fft.rfft(x.float(), *a, **k))(K, L2)
        Uf = (lambda x, *a, **k: torch.fft.rfft(x.float(), *a, **k))(u_t, L2)
        y = (lambda x, *a, **k: torch.fft.irfft(x, *a, **k).to(torch.bfloat16))(Uf * Kf[None, :, :], L2)[:, :, :L]
        y = (lambda x, *d: x.permute(*d))(y, 0, 2, 1)
        return y + u * D_skip[None, None, :]

    def _B_ref(x, w1, w2):
        h = torch.einsum("bsd,df->bsf", x, w1)
        h = _silu(h)
        out = torch.einsum("bsf,fd->bsd", h, w2)
        return out

    x = A_u
    g = _sigmoid((x * gate_w).sum(-1, keepdims=True))
    a = _A_ref(x, A_log_A_real, A_A_imag, A_C_real, A_C_imag, A_log_dt, A_D_skip)
    b = _B_ref(x, B_w1, B_w2)
    return g * a + (1 - g) * b
```

```cpp
#include <hip/hip_runtime.h>
#include <hip/hip_bf16.h>
#include <stdint.h>

typedef __attribute__((ext_vector_type(8))) short bf16x8;
typedef __attribute__((ext_vector_type(4))) short bf16x4;
typedef __attribute__((ext_vector_type(16))) float f32x16;
typedef __attribute__((ext_vector_type(4))) float f32x4;
typedef __attribute__((ext_vector_type(4))) unsigned int u32x4;
typedef __attribute__((ext_vector_type(2))) unsigned int u32x2;

#define LDS_AS __attribute__((address_space(3)))

constexpr int L_ = 2048, D_ = 640, NS_ = 128, F_ = 1728;

__device__ __forceinline__ float bf2f(unsigned short v) { return __uint_as_float(((unsigned)v) << 16); }
__device__ __forceinline__ unsigned short f2bf(float f) {
  __hip_bfloat16 b = __float2bfloat16(f);
  return *reinterpret_cast<unsigned short*>(&b);
}
__device__ __forceinline__ float rbf(float f) { return bf2f(f2bf(f)); }

constexpr int GBM = 128, GBN = 64, GBK = 64;
constexpr int GA_BYTES = GBM * GBK * 2;
constexpr int GB_BYTES = GBK * GBN * 2;
constexpr int GBUF_BYTES = GA_BYTES + GB_BYTES;
constexpr int GEMM_LDS = 2 * GBUF_BYTES;

__device__ __forceinline__ void gemm_tile(const unsigned short* __restrict__ A, const unsigned short* __restrict__ B,
                                          int K, int N, int m0, int n0, char* smem, f32x16 acc[2]) {
  const int tid = threadIdx.x, lane = tid & 63, wid = tid >> 6;
  const int wm = wid >> 1, wn = wid & 1;
  const int r = lane & 31, h = lane >> 5;
  const int nk = K / GBK;

  u32x4 ra[4], rb[2];
  int arow[4], ach[4];
#pragma unroll
  for (int i = 0; i < 4; ++i) { int id = tid + 256 * i; arow[i] = id >> 3; ach[i] = id & 7; }
  int brow[2], bch[2];
#pragma unroll
  for (int i = 0; i < 2; ++i) { int id = tid + 256 * i; brow[i] = id >> 3; bch[i] = id & 7; }

  auto gload = [&](int kt) {
#pragma unroll
    for (int i = 0; i < 4; ++i)
      ra[i] = *reinterpret_cast<const u32x4*>(A + (size_t)(m0 + arow[i]) * K + kt * GBK + ach[i] * 8);
#pragma unroll
    for (int i = 0; i < 2; ++i)
      rb[i] = *reinterpret_cast<const u32x4*>(B + (size_t)(kt * GBK + brow[i]) * N + n0 + bch[i] * 8);
  };
  auto swrite = [&](int buf) {
    char* sa = smem + buf * GBUF_BYTES;
    char* sb = sa + GA_BYTES;
#pragma unroll
    for (int i = 0; i < 4; ++i)
      *reinterpret_cast<u32x4*>(sa + arow[i] * 128 + ((ach[i] ^ (arow[i] & 7)) << 4)) = ra[i];
#pragma unroll
    for (int i = 0; i < 2; ++i)
      *reinterpret_cast<u32x4*>(sb + brow[i] * 128 + ((bch[i] ^ (((brow[i] >> 1) & 1) << 2)) << 4)) = rb[i];
  };

#pragma unroll
  for (int i = 0; i < 16; ++i) { acc[0][i] = 0.f; acc[1][i] = 0.f; }

  gload(0);
  swrite(0);
  __syncthreads();

  const int q = (lane & 15) >> 2, p = lane & 3, blk = (lane >> 4) & 1;
  for (int kt = 0; kt < nk; ++kt) {
    if (kt + 1 < nk) gload(kt + 1);
    const char* sa = smem + (kt & 1) * GBUF_BYTES;
    const char* sb = sa + GA_BYTES;
#pragma unroll
    for (int s = 0; s < 4; ++s) {
      bf16x8 af0, af1, bf;
      {
        int row = wm * 64 + r;
        af0 = *reinterpret_cast<const bf16x8*>(sa + row * 128 + (((2 * s + h) ^ (row & 7)) << 4));
        row += 32;
        af1 = *reinterpret_cast<const bf16x8*>(sa + row * 128 + (((2 * s + h) ^ (row & 7)) << 4));
      }
#pragma unroll
      for (int t = 0; t < 2; ++t) {
        int row = 16 * s + 8 * h + 4 * t + q;
        int chunk = wn * 4 + 2 * blk + (p >> 1);
        const char* ad = sb + row * 128 + ((chunk ^ (((row >> 1) & 1) << 2)) << 4) + (p & 1) * 8;
        bf16x4 v = __builtin_amdgcn_ds_read_tr16_b64_v4i16((LDS_AS bf16x4*)(ad));
        bf[4 * t + 0] = v[0]; bf[4 * t + 1] = v[1]; bf[4 * t + 2] = v[2]; bf[4 * t + 3] = v[3];
      }
      acc[0] = __builtin_amdgcn_mfma_f32_32x32x16_bf16(af0, bf, acc[0], 0, 0, 0);
      acc[1] = __builtin_amdgcn_mfma_f32_32x32x16_bf16(af1, bf, acc[1], 0, 0, 0);
    }
    if (kt + 1 < nk) swrite((kt + 1) & 1);
    __syncthreads();
  }
}

constexpr int UC_STRIDE = 4384;
constexpr int UC_OFF = 0;
constexpr int KROW = 80;
constexpr int KH_OFF = 8 * UC_STRIDE;
constexpr int KL_OFF = KH_OFF + 64 * KROW + 16;
constexpr int KZ_OFF = KH_OFF + 64 * KROW;
constexpr int PR_OFF = KL_OFF + 64 * KROW;
constexpr int OS_OFF = PR_OFF + 128 * 16;
constexpr int CONV_LDS = OS_OFF + 4 * 48 * 16 * 4;
constexpr int LDS_BYTES = (CONV_LDS > GEMM_LDS ? CONV_LDS : GEMM_LDS);

__device__ __forceinline__ float thi_of(int i) {
  int t;
  if (i < 8) t = i;
  else if (i < 16) t = 128 + (i - 8);
  else if (i < 24) t = 256 + 128 * ((i - 16) >> 2) + 2 * ((i - 16) & 3);
  else if (i < 32) t = 512 + 128 * ((i - 24) >> 1) + 4 * ((i - 24) & 1);
  else if (i < 40) t = 1024 + 128 * (i - 32);
  else t = 2048;
  return (float)t;
}

__device__ void conv_role(int d, const unsigned short* __restrict__ u, const unsigned short* __restrict__ logAr,
                          const unsigned short* __restrict__ Aim, const unsigned short* __restrict__ Cre,
                          const unsigned short* __restrict__ Cim, const unsigned short* __restrict__ logdt,
                          const unsigned short* __restrict__ Dsk, unsigned short* __restrict__ aT, char* smem) {
  const int tid = threadIdx.x, lane = tid & 63, wid = tid >> 6;

  unsigned short uv[8];
#pragma unroll
  for (int k = 0; k < 8; ++k) uv[k] = u[(size_t)(8 * tid + k) * D_ + d];

  const float dt = rbf(expf(bf2f(logdt[d])));
  if (tid < NS_) {
    float are = -rbf(expf(bf2f(logAr[d * NS_ + tid])));
    float aim = bf2f(Aim[d * NS_ + tid]);
    float lr = are * dt, li = aim * dt;
    float cr = bf2f(Cre[d * NS_ + tid]) * dt, ci = bf2f(Cim[d * NS_ + tid]) * dt;
    float mag = sqrtf(cr * cr + ci * ci);
    f32x4 pp;
    pp[0] = lr * 1.4426950408889634f;
    pp[1] = li * 0.15915494309189535f;
    pp[2] = log2f(mag);
    pp[3] = atan2f(ci, cr) * 0.15915494309189535f;
    *reinterpret_cast<f32x4*>(smem + PR_OFF + tid * 16) = pp;
  }
  if (tid == 0) { u32x4 z = {0, 0, 0, 0}; *reinterpret_cast<u32x4*>(smem + KZ_OFF) = z; }
  if (tid < 64) {
    u32x4 z = {0, 0, 0, 0};
    *reinterpret_cast<u32x4*>(smem + UC_OFF + (tid >> 3) * UC_STRIDE + (tid & 7) * 16) = z;
  }
  {
    u32x4 v;
    v[0] = uv[0] | ((unsigned)uv[1] << 16); v[1] = uv[2] | ((unsigned)uv[3] << 16);
    v[2] = uv[4] | ((unsigned)uv[5] << 16); v[3] = uv[6] | ((unsigned)uv[7] << 16);
    *reinterpret_cast<u32x4*>(smem + UC_OFF + 128 + tid * 16) = v;
  }
  __syncthreads();

  {
    u32x4 v0 = *reinterpret_cast<const u32x4*>(smem + UC_OFF + 128 + tid * 16);
    u32x4 v1 = {0, 0, 0, 0};
    if (tid < 255) v1 = *reinterpret_cast<const u32x4*>(smem + UC_OFF + 128 + (tid + 1) * 16);
    unsigned w[8] = {v0[0], v0[1], v0[2], v0[3], v1[0], v1[1], v1[2], v1[3]};
#pragma unroll
    for (int e = 1; e < 8; ++e) {
      u32x4 o;
#pragma unroll
      for (int j = 0; j < 4; ++j) {
        int base = j + (e >> 1);
        if (e & 1) o[j] = (w[base] >> 16) | (w[base + 1] << 16);
        else o[j] = w[base];
      }
      *reinterpret_cast<u32x4*>(smem + UC_OFF + e * UC_STRIDE + 128 + tid * 16) = o;
    }
    if (tid == 0) {
#pragma unroll
      for (int e = 1; e < 8; ++e)
#pragma unroll
        for (int k = 0; k < e; ++k)
          *reinterpret_cast<unsigned short*>(smem + UC_OFF + e * UC_STRIDE + 2 * (64 - e + k)) = uv[k];
    }
  }

  {
    const int x = lane & 15, kk = lane >> 4;
    float th[3];
#pragma unroll
    for (int T = 0; T < 3; ++T) th[T] = thi_of(16 * T + x);
    const float tlo = (float)(8 * x);
    f32x4 acc[3];
#pragma unroll
    for (int T = 0; T < 3; ++T) acc[T] = (f32x4){0.f, 0.f, 0.f, 0.f};
#pragma unroll 2
    for (int g = 0; g < 8; ++g) {
      int n = 32 * wid + 4 * g + kk;
      f32x4 pp = *reinterpret_cast<const f32x4*>(smem + PR_OFF + n * 16);
      float qe = __builtin_amdgcn_exp2f(pp[0] * tlo);
      float qa = pp[1] * tlo;
      float qr = qe * __builtin_amdgcn_cosf(qa), qi = qe * __builtin_amdgcn_sinf(qa);
#pragma unroll
      for (int T = 0; T < 3; ++T) {
        float pe = __builtin_amdgcn_exp2f(pp[0] * th[T] + pp[2]);
        float pa = pp[1] * th[T] + pp[3];
        float pr = pe * __builtin_amdgcn_cosf(pa), pi = pe * __builtin_amdgcn_sinf(pa);
        acc[T] = __builtin_amdgcn_mfma_f32_16x16x4f32(qr, pr, acc[T], 0, 0, 0);
        acc[T] = __builtin_amdgcn_mfma_f32_16x16x4f32(-qi, pi, acc[T], 0, 0, 0);
      }
    }
#pragma unroll
    for (int T = 0; T < 3; ++T)
      *reinterpret_cast<f32x4*>(smem + OS_OFF + wid * 3072 + (16 * T + x) * 64 + kk * 16) = acc[T];
  }
  __syncthreads();

  {
    unsigned short kh[8], kl[8];
#pragma unroll
    for (int k = 0; k < 8; ++k) {
      int j = 8 * tid + k;
      int rr = (int)rbf((float)j);
      int i;
      if (rr < 256) i = (rr & 7) + 8 * (rr >> 7);
      else if (rr < 512) i = 16 + 4 * ((rr - 256) >> 7) + ((rr & 7) >> 1);
      else if (rr < 1024) i = 24 + 2 * ((rr - 512) >> 7) + ((rr & 7) >> 2);
      else if (rr < 2048) i = 32 + ((rr - 1024) >> 7);
      else i = 40;
      int jj = (rr >> 3) & 15;
      const float* os = reinterpret_cast<const float*>(smem + OS_OFF) + i * 16 + jj;
      float kv = 2.0f * ((os[0] + os[768]) + (os[1536] + os[2304]));
      unsigned short hi = f2bf(kv);
      kh[7 - k] = hi;
      kl[7 - k] = f2bf(kv - bf2f(hi));
    }
    u32x4 vh, vl;
#pragma unroll
    for (int j = 0; j < 4; ++j) {
      vh[j] = kh[2 * j] | ((unsigned)kh[2 * j + 1] << 16);
      vl[j] = kl[2 * j] | ((unsigned)kl[2 * j + 1] << 16);
    }
    *reinterpret_cast<u32x4*>(smem + KH_OFF + (tid >> 2) * KROW + (tid & 3) * 16) = vh;
    *reinterpret_cast<u32x4*>(smem + KL_OFF + (tid >> 2) * KROW + (tid & 3) * 16) = vl;
  }
  __syncthreads();

  f32x16 y0, y1;
#pragma unroll
  for (int i = 0; i < 16; ++i) { y0[i] = 0.f; y1[i] = 0.f; }
  {
    const int r = lane & 31, h = lane >> 5;
    const int e = (r + 1) & 7;
    const char* ubase = smem + UC_OFF + e * UC_STRIDE + 2 * (r - 7 - e + 64 - 8 * h);
    for (int dl = wid; dl < 64; dl += 4) {
      bf16x8 a0 = *reinterpret_cast<const bf16x8*>(ubase + 64 * dl);
      bf16x8 a1 = *reinterpret_cast<const bf16x8*>(ubase + 64 * dl - 32);
      {
        int m = 32 + r - dl;
        int off = (m >= 0) ? (m * KROW + 16 * h) : (KZ_OFF - KH_OFF);
        int off1 = (m >= 0) ? (off + 32) : off;
        bf16x8 bh0 = *reinterpret_cast<const bf16x8*>(smem + KH_OFF + off);
        bf16x8 bh1 = *reinterpret_cast<const bf16x8*>(smem + KH_OFF + off1);
        bf16x8 bl0 = *reinterpret_cast<const bf16x8*>(smem + ((m >= 0) ? KL_OFF : KH_OFF) + off);
        bf16x8 bl1 = *reinterpret_cast<const bf16x8*>(smem + ((m >= 0) ? KL_OFF : KH_OFF) + off1);
        y1 = __builtin_amdgcn_mfma_f32_32x32x16_bf16(a0, bh0, y1, 0, 0, 0);
        y1 = __builtin_amdgcn_mfma_f32_32x32x16_bf16(a1, bh1, y1, 0, 0, 0);
        y1 = __builtin_amdgcn_mfma_f32_32x32x16_bf16(a0, bl0, y1, 0, 0, 0);
        y1 = __builtin_amdgcn_mfma_f32_32x32x16_bf16(a1, bl1, y1, 0, 0, 0);
      }
      if (dl < 32) {
        int m = r - dl;
        int off = (m >= 0) ? (m * KROW + 16 * h) : (KZ_OFF - KH_OFF);
        int off1 = (m >= 0) ? (off + 32) : off;
        bf16x8 bh0 = *reinterpret_cast<const bf16x8*>(smem + KH_OFF + off);
        bf16x8 bh1 = *reinterpret_cast<const bf16x8*>(smem + KH_OFF + off1);
        bf16x8 bl0 = *reinterpret_cast<const bf16x8*>(smem + ((m >= 0) ? KL_OFF : KH_OFF) + off);
        bf16x8 bl1 = *reinterpret_cast<const bf16x8*>(smem + ((m >= 0) ? KL_OFF : KH_OFF) + off1);
        y0 = __builtin_amdgcn_mfma_f32_32x32x16_bf16(a0, bh0, y0, 0, 0, 0);
        y0 = __builtin_amdgcn_mfma_f32_32x32x16_bf16(a1, bh1, y0, 0, 0, 0);
        y0 = __builtin_amdgcn_mfma_f32_32x32x16_bf16(a0, bl0, y0, 0, 0, 0);
        y0 = __builtin_amdgcn_mfma_f32_32x32x16_bf16(a1, bl1, y0, 0, 0, 0);
      }
    }
  }
  __syncthreads();
  {
    const int r = lane & 31, h = lane >> 5;
    float* slab = reinterpret_cast<float*>(smem + wid * 8704);
#pragma unroll
    for (int g = 0; g < 4; ++g) {
      f32x4 v0 = {y0[4 * g], y0[4 * g + 1], y0[4 * g + 2], y0[4 * g + 3]};
      f32x4 v1 = {y1[4 * g], y1[4 * g + 1], y1[4 * g + 2], y1[4 * g + 3]};
      int b = 8 * g + 4 * h;
      *reinterpret_cast<f32x4*>(slab + 32 * r + b) = v0;
      *reinterpret_cast<f32x4*>(slab + 32 * (32 + r) + b) = v1;
    }
  }
  __syncthreads();
  {
    const float dsk = bf2f(Dsk[d]);
    unsigned short o[8];
#pragma unroll
    for (int k = 0; k < 8; ++k) {
      int l = 8 * tid + k;
      const float* s = reinterpret_cast<const float*>(smem);
      float y = (s[l] + s[2176 + l]) + (s[4352 + l] + s[6528 + l]);
      float yb = rbf(y);
      float sk = rbf(bf2f(uv[k]) * dsk);
      o[k] = f2bf(yb + sk);
    }
    u32x4 v;
#pragma unroll
    for (int j = 0; j < 4; ++j) v[j] = o[2 * j] | ((unsigned)o[2 * j + 1] << 16);
    *reinterpret_cast<u32x4*>(aT + (size_t)d * L_ + 8 * tid) = v;
  }
}

constexpr int N_CONV = D_;
constexpr int N_G1 = (L_ / GBM) * (F_ / GBN);
constexpr int N_GATE = 32;

__global__ __launch_bounds__(256) void kernelA(const unsigned short* __restrict__ u, const unsigned short* __restrict__ logAr,
                                               const unsigned short* __restrict__ Aim, const unsigned short* __restrict__ Cre,
                                               const unsigned short* __restrict__ Cim, const unsigned short* __restrict__ logdt,
                                               const unsigned short* __restrict__ Dsk, const unsigned short* __restrict__ w1,
                                               const unsigned short* __restrict__ gw, unsigned short* __restrict__ aT,
                                               unsigned short* __restrict__ hbuf, float* __restrict__ gbuf) {
  extern __shared__ __attribute__((aligned(16))) char smem[];
  int b = blockIdx.x;
  if (b < N_CONV) {
    conv_role(b, u, logAr, Aim, Cre, Cim, logdt, Dsk, aT, smem);
  } else if (b < N_CONV + N_G1) {
    int t = b - N_CONV;
    int tm = t / (F_ / GBN), tn = t % (F_ / GBN);
    f32x16 acc[2];
    gemm_tile(u, w1, D_, F_, tm * GBM, tn * GBN, smem, acc);
    const int lane = threadIdx.x & 63, wid = threadIdx.x >> 6;
    const int wm = wid >> 1, wn = wid & 1, r = lane & 31, h = lane >> 5;
#pragma unroll
    for (int mt = 0; mt < 2; ++mt)
#pragma unroll
      for (int i = 0; i < 16; ++i) {
        int row = tm * GBM + wm * 64 + mt * 32 + (i & 3) + 8 * (i >> 2) + 4 * h;
        int col = tn * GBN + wn * 32 + r;
        float hv = rbf(acc[mt][i]);
        float sv = hv / (1.0f + __expf(-hv));
        hbuf[(size_t)row * F_ + col] = f2bf(sv);
      }
  } else {
    int t = b - N_CONV - N_G1;
    const int lane = threadIdx.x & 63, wid = threadIdx.x >> 6;
    for (int rr = 0; rr < 16; ++rr) {
      int row = (t * 4 + wid) * 16 + rr;
      float s = 0.f;
      for (int c = lane; c < D_; c += 64) s += rbf(bf2f(u[(size_t)row * D_ + c]) * bf2f(gw[c]));
#pragma unroll
      for (int o = 32; o >= 1; o >>= 1) s += __shfl_xor(s, o);
      if (lane == 0) {
        float sb = rbf(s);
        gbuf[row] = rbf(1.0f / (1.0f + expf(-sb)));
      }
    }
  }
}

__global__ __launch_bounds__(256) void kernelB(const unsigned short* __restrict__ hbuf, const unsigned short* __restrict__ w2,
                                               const unsigned short* __restrict__ aT, const float* __restrict__ gbuf,
                                               unsigned short* __restrict__ out) {
  extern __shared__ __attribute__((aligned(16))) char smem[];
  int t = blockIdx.x;
  int tm = t / (D_ / GBN), tn = t % (D_ / GBN);
  f32x16 acc[2];
  gemm_tile(hbuf, w2, F_, D_, tm * GBM, tn * GBN, smem, acc);
  const int lane = threadIdx.x & 63, wid = threadIdx.x >> 6;
  const int wm = wid >> 1, wn = wid & 1, r = lane & 31, h = lane >> 5;
  const int col = tn * GBN + wn * 32 + r;
#pragma unroll
  for (int mt = 0; mt < 2; ++mt)
#pragma unroll
    for (int g4 = 0; g4 < 4; ++g4) {
      int row0 = tm * GBM + wm * 64 + mt * 32 + 8 * g4 + 4 * h;
      u32x2 av = *reinterpret_cast<const u32x2*>(aT + (size_t)col * L_ + row0);
      f32x4 gv = *reinterpret_cast<const f32x4*>(gbuf + row0);
#pragma unroll
      for (int j = 0; j < 4; ++j) {
        float a = bf2f((unsigned short)((av[j >> 1] >> (16 * (j & 1))) & 0xffff));
        float bv = rbf(acc[mt][4 * g4 + j]);
        float g = gv[j];
        float t1 = rbf(g * a);
        float t2 = rbf(1.0f - g);
        float t3 = rbf(t2 * bv);
        out[(size_t)(row0 + j) * D_ + col] = f2bf(t1 + t3);
      }
    }
}

extern "C" void kernel_launch(void* const* d_in, const int* in_sizes, int n_in, void* d_out, int out_size, void* d_ws,
                              size_t ws_size, hipStream_t stream) {
  const unsigned short* u = (const unsigned short*)d_in[0];
  const unsigned short* logAr = (const unsigned short*)d_in[1];
  const unsigned short* Aim = (const unsigned short*)d_in[2];
  const unsigned short* Cre = (const unsigned short*)d_in[3];
  const unsigned short* Cim = (const unsigned short*)d_in[4];
  const unsigned short* logdt = (const unsigned short*)d_in[5];
  const unsigned short* Dsk = (const unsigned short*)d_in[6];
  const unsigned short* w1 = (const unsigned short*)d_in[8];
  const unsigned short* w2 = (const unsigned short*)d_in[9];
  const unsigned short* gw = (const unsigned short*)d_in[10];
  unsigned short* out = (unsigned short*)d_out;

  char* ws = (char*)d_ws;
  unsigned short* hbuf = (unsigned short*)ws;
  unsigned short* aT = (unsigned short*)(ws + (size_t)L_ * F_ * 2);
  float* gbuf = (float*)(ws + (size_t)L_ * F_ * 2 + (size_t)D_ * L_ * 2);

  hipLaunchKernelGGL(kernelA, dim3(N_CONV + N_G1 + N_GATE), dim3(256), LDS_BYTES, stream, u, logAr, Aim, Cre, Cim, logdt,
                     Dsk, w1, gw, aT, hbuf, gbuf);
  hipLaunchKernelGGL(kernelB, dim3((L_ / GBM) * (D_ / GBN)), dim3(256), GEMM_LDS, stream, hbuf, w2, aT, gbuf, out);
}
```

```cpp
#include <hip/hip_runtime.h>
#include <hip/hip_bf16.h>
#include <stdint.h>

typedef __attribute__((ext_vector_type(8))) short bf16x8;
typedef __attribute__((ext_vector_type(4))) short bf16x4;
typedef __attribute__((ext_vector_type(16))) float f32x16;
typedef __attribute__((ext_vector_type(4))) float f32x4;
typedef __attribute__((ext_vector_type(4))) unsigned int u32x4;
typedef __attribute__((ext_vector_type(2))) unsigned int u32x2;

#define CONV_STOP 0
#define LDS_AS __attribute__((address_space(3)))

constexpr int L_ = 2048, D_ = 640, NS_ = 128, F_ = 1728;

__device__ __forceinline__ float bf2f(unsigned short v) { return __uint_as_float(((unsigned)v) << 16); }
__device__ __forceinline__ unsigned short f2bf(float f) {
  __hip_bfloat16 b = __float2bfloat16(f);
  return *reinterpret_cast<unsigned short*>(&b);
}
__device__ __forceinline__ float rbf(float f) { return bf2f(f2bf(f)); }

constexpr int GBN = 64, GBK = 64;
template <int BM> struct GemmCfg {
  static constexpr int A_BYTES = BM * GBK * 2;
  static constexpr int B_BYTES = GBK * GBN * 2;
  static constexpr int BUF_BYTES = A_BYTES + B_BYTES;
  static constexpr int LDS = 2 * BUF_BYTES;
  static constexpr int MT = BM / 64;
  static constexpr int AL = BM / 32;
};

template <int BM>
__device__ __forceinline__ void gemm_tile(const unsigned short* __restrict__ A, const unsigned short* __restrict__ B,
                                          int K, int N, int m0, int n0, char* smem, f32x16 (&acc)[BM / 64]) {
  using C = GemmCfg<BM>;
  const int tid = threadIdx.x, lane = tid & 63, wid = tid >> 6;
  const int wm = wid >> 1, wn = wid & 1;
  const int r = lane & 31, h = lane >> 5;
  const int nk = K / GBK;

  u32x4 ra0[C::AL], rb0[2], ra1[C::AL], rb1[2];
  const int ar = tid >> 3, ac = tid & 7;
  const unsigned short* Ap = A + (size_t)(m0 + ar) * K + ac * 8;
  const unsigned short* Bp = B + (size_t)ar * N + n0 + ac * 8;
  const int asw = (ac ^ (ar & 7)) << 4;
  const int bsw = (ac ^ (((ar >> 1) & 1) << 2)) << 4;

#define G_LOAD(RA, RB, kt)                                                                            \
  {                                                                                                   \
    _Pragma("unroll") for (int i = 0; i < C::AL; ++i)                                                 \
        RA[i] = *reinterpret_cast<const u32x4*>(Ap + (size_t)(32 * i) * K + (kt) * GBK);              \
    _Pragma("unroll") for (int i = 0; i < 2; ++i)                                                     \
        RB[i] = *reinterpret_cast<const u32x4*>(Bp + (size_t)((kt) * GBK + 32 * i) * N);              \
  }
#define G_STORE(RA, RB, buf)                                                                          \
  {                                                                                                   \
    char* sa_ = smem + (buf) * C::BUF_BYTES;                                                          \
    char* sb_ = sa_ + C::A_BYTES;                                                                     \
    _Pragma("unroll") for (int i = 0; i < C::AL; ++i)                                                 \
        *reinterpret_cast<u32x4*>(sa_ + (ar + 32 * i) * 128 + asw) = RA[i];                           \
    _Pragma("unroll") for (int i = 0; i < 2; ++i)                                                     \
        *reinterpret_cast<u32x4*>(sb_ + (ar + 32 * i) * 128 + bsw) = RB[i];                           \
  }
  const int q = (lane & 15) >> 2, p = lane & 3, blk = (lane >> 4) & 1;
#define G_COMPUTE(buf)                                                                                \
  {                                                                                                   \
    const char* sa_ = smem + (buf) * C::BUF_BYTES;                                                    \
    const char* sb_ = sa_ + C::A_BYTES;                                                               \
    _Pragma("unroll") for (int s = 0; s < 4; ++s) {                                                   \
      bf16x8 bfr;                                                                                     \
      _Pragma("unroll") for (int t = 0; t < 2; ++t) {                                                 \
        int row = 16 * s + 8 * h + 4 * t + q;                                                         \
        int chunk = wn * 4 + 2 * blk + (p >> 1);                                                      \
        const char* ad = sb_ + row * 128 + ((chunk ^ (((row >> 1) & 1) << 2)) << 4) + (p & 1) * 8;    \
        bf16x4 v = __builtin_amdgcn_ds_read_tr16_b64_v4i16((LDS_AS bf16x4*)(ad));                     \
        bfr[4 * t + 0] = v[0]; bfr[4 * t + 1] = v[1]; bfr[4 * t + 2] = v[2]; bfr[4 * t + 3] = v[3];   \
      }                                                                                               \
      _Pragma("unroll") for (int mt = 0; mt < C::MT; ++mt) {                                          \
        int row = wm * (BM / 2) + mt * 32 + r;                                                        \
        bf16x8 af = *reinterpret_cast<const bf16x8*>(sa_ + row * 128 + (((2 * s + h) ^ (row & 7)) << 4)); \
        acc[mt] = __builtin_amdgcn_mfma_f32_32x32x16_bf16(af, bfr, acc[mt], 0, 0, 0);                 \
      }                                                                                               \
    }                                                                                                 \
  }

#pragma unroll
  for (int mt = 0; mt < C::MT; ++mt)
#pragma unroll
    for (int i = 0; i < 16; ++i) acc[mt][i] = 0.f;

  G_LOAD(ra0, rb0, 0);
  if (nk > 1) G_LOAD(ra1, rb1, 1);
  G_STORE(ra0, rb0, 0);
  __syncthreads();
  for (int kt = 0; kt < nk; kt += 2) {
    if (kt + 2 < nk) G_LOAD(ra0, rb0, kt + 2);
    G_COMPUTE(0);
    if (kt + 1 < nk) G_STORE(ra1, rb1, 1);
    __syncthreads();
    if (kt + 1 >= nk) break;
    if (kt + 3 < nk) G_LOAD(ra1, rb1, kt + 3);
    G_COMPUTE(1);
    if (kt + 2 < nk) G_STORE(ra0, rb0, 0);
    __syncthreads();
  }
#undef G_LOAD
#undef G_STORE
#undef G_COMPUTE
}

constexpr int UC_STRIDE = 4384;
constexpr int UC_OFF = 0;
constexpr int KROW = 80;
constexpr int KH_OFF = 8 * UC_STRIDE;
constexpr int KL_OFF = KH_OFF + 64 * KROW + 16;
constexpr int KZ_OFF = KH_OFF + 64 * KROW;
constexpr int PR_OFF = KL_OFF + 64 * KROW;
constexpr int OS_OFF = PR_OFF + 128 * 16;
constexpr int CONV_LDS = OS_OFF + 48 * 16 * 4;
constexpr int G1_BM = 128, G2_BM = 64;
constexpr int LDS_A_BYTES = (CONV_LDS > GemmCfg<G1_BM>::LDS ? CONV_LDS : GemmCfg<G1_BM>::LDS);

__device__ __forceinline__ float thi_of(int i) {
  int t;
  if (i < 8) t = i;
  else if (i < 16) t = 128 + (i - 8);
  else if (i < 24) t = 256 + 128 * ((i - 16) >> 2) + 2 * ((i - 16) & 3);
  else if (i < 32) t = 512 + 128 * ((i - 24) >> 1) + 4 * ((i - 24) & 1);
  else if (i < 40) t = 1024 + 128 * (i - 32);
  else t = 2048;
  return (float)t;
}

__device__ void conv_role(int d, const unsigned short* __restrict__ uT, const unsigned short* __restrict__ logAr,
                          const unsigned short* __restrict__ Aim, const unsigned short* __restrict__ Cre,
                          const unsigned short* __restrict__ Cim, const unsigned short* __restrict__ logdt,
                          const unsigned short* __restrict__ Dsk, unsigned short* __restrict__ aT, char* smem) {
  const int tid = threadIdx.x, lane = tid & 63, wid = tid >> 6;

  const u32x4 uvec = *reinterpret_cast<const u32x4*>(uT + (size_t)d * L_ + 8 * tid);

  const float dt = rbf(expf(bf2f(logdt[d])));
  if (tid < NS_) {
    float are = -rbf(expf(bf2f(logAr[d * NS_ + tid])));
    float aim = bf2f(Aim[d * NS_ + tid]);
    float lr = are * dt, li = aim * dt;
    float cr = bf2f(Cre[d * NS_ + tid]) * dt, ci = bf2f(Cim[d * NS_ + tid]) * dt;
    float mag = sqrtf(cr * cr + ci * ci);
    f32x4 pp;
    pp[0] = lr * 1.4426950408889634f;
    pp[1] = li * 0.15915494309189535f;
    pp[2] = log2f(mag);
    pp[3] = atan2f(ci, cr) * 0.15915494309189535f;
    *reinterpret_cast<f32x4*>(smem + PR_OFF + tid * 16) = pp;
  } else {
    f32x4 z = {0.f, 0.f, 0.f, 0.f};
    *reinterpret_cast<f32x4*>(smem + OS_OFF + (tid - NS_) * 16) = z;
    if (tid - NS_ < 64) *reinterpret_cast<f32x4*>(smem + OS_OFF + (128 + tid - NS_) * 16) = z;
  }
  if (tid == 0) { u32x4 z = {0, 0, 0, 0}; *reinterpret_cast<u32x4*>(smem + KZ_OFF) = z; }
  if (tid < 64) {
    u32x4 z = {0, 0, 0, 0};
    *reinterpret_cast<u32x4*>(smem + UC_OFF + (tid >> 3) * UC_STRIDE + (tid & 7) * 16) = z;
  }
  *reinterpret_cast<u32x4*>(smem + UC_OFF + 128 + tid * 16) = uvec;
  __syncthreads();

  {
    u32x4 v1 = {0, 0, 0, 0};
    if (tid < 255) v1 = *reinterpret_cast<const u32x4*>(smem + UC_OFF + 128 + (tid + 1) * 16);
    unsigned w[8] = {uvec[0], uvec[1], uvec[2], uvec[3], v1[0], v1[1], v1[2], v1[3]};
#pragma unroll
    for (int e = 1; e < 8; ++e) {
      u32x4 o;
#pragma unroll
      for (int j = 0; j < 4; ++j) {
        int base = j + (e >> 1);
        if (e & 1) o[j] = (w[base] >> 16) | (w[base + 1] << 16);
        else o[j] = w[base];
      }
      *reinterpret_cast<u32x4*>(smem + UC_OFF + e * UC_STRIDE + 128 + tid * 16) = o;
    }
    if (tid == 0) {
#pragma unroll
      for (int e = 1; e < 8; ++e)
#pragma unroll
        for (int k = 0; k < e; ++k)
          *reinterpret_cast<unsigned short*>(smem + UC_OFF + e * UC_STRIDE + 2 * (64 - e + k)) =
              (unsigned short)(w[k >> 1] >> (16 * (k & 1)));
    }
  }

  {
    const int x = lane & 15, kk = lane >> 4;
    float th[3];
#pragma unroll
    for (int T = 0; T < 3; ++T) th[T] = thi_of(16 * T + x);
    const float tlo = (float)(8 * x);
    f32x4 acc[3];
#pragma unroll
    for (int T = 0; T < 3; ++T) acc[T] = (f32x4){0.f, 0.f, 0.f, 0.f};
#pragma unroll 2
    for (int g = 0; g < 8; ++g) {
      int n = 32 * wid + 4 * g + kk;
      f32x4 pp = *reinterpret_cast<const f32x4*>(smem + PR_OFF + n * 16);
      float qe = __builtin_amdgcn_exp2f(pp[0] * tlo);
      float qa = pp[1] * tlo;
      float qr = qe * __builtin_amdgcn_cosf(qa), qi = qe * __builtin_amdgcn_sinf(qa);
#pragma unroll
      for (int T = 0; T < 3; ++T) {
        float pe = __builtin_amdgcn_exp2f(pp[0] * th[T] + pp[2]);
        float pa = pp[1] * th[T] + pp[3];
        float pr = pe * __builtin_amdgcn_cosf(pa), pi = pe * __builtin_amdgcn_sinf(pa);
        acc[T] = __builtin_amdgcn_mfma_f32_16x16x4f32(qr, pr, acc[T], 0, 0, 0);
        acc[T] = __builtin_amdgcn_mfma_f32_16x16x4f32(-qi, pi, acc[T], 0, 0, 0);
      }
    }
    float* os = reinterpret_cast<float*>(smem + OS_OFF);
#pragma unroll
    for (int T = 0; T < 3; ++T)
#pragma unroll
      for (int j = 0; j < 4; ++j)
        __hip_atomic_fetch_add(os + (16 * T + x) * 16 + 4 * kk + j, acc[T][j], __ATOMIC_RELAXED, __HIP_MEMORY_SCOPE_WORKGROUP);
  }
  __syncthreads();

  {
    unsigned short kh[8], kl[8];
#pragma unroll
    for (int k = 0; k < 8; ++k) {
      int j = 8 * tid + k;
      int rr = (int)rbf((float)j);
      int i;
      if (rr < 256) i = (rr & 7) + 8 * (rr >> 7);
      else if (rr < 512) i = 16 + 4 * ((rr - 256) >> 7) + ((rr & 7) >> 1);
      else if (rr < 1024) i = 24 + 2 * ((rr - 512) >> 7) + ((rr & 7) >> 2);
      else if (rr < 2048) i = 32 + ((rr - 1024) >> 7);
      else i = 40;
      int jj = (rr >> 3) & 15;
      float kv = 2.0f * reinterpret_cast<const float*>(smem + OS_OFF)[i * 16 + jj];
      unsigned short hi = f2bf(kv);
      kh[7 - k] = hi;
      kl[7 - k] = f2bf(kv - bf2f(hi));
    }
    u32x4 vh, vl;
#pragma unroll
    for (int j = 0; j < 4; ++j) {
      vh[j] = kh[2 * j] | ((unsigned)kh[2 * j + 1] << 16);
      vl[j] = kl[2 * j] | ((unsigned)kl[2 * j + 1] << 16);
    }
    *reinterpret_cast<u32x4*>(smem + KH_OFF + (tid >> 2) * KROW + (tid & 3) * 16) = vh;
    *reinterpret_cast<u32x4*>(smem + KL_OFF + (tid >> 2) * KROW + (tid & 3) * 16) = vl;
  }
  __syncthreads();

  f32x16 y0, y1;
#pragma unroll
  for (int i = 0; i < 16; ++i) { y0[i] = 0.f; y1[i] = 0.f; }
  {
    const int r = lane & 31, h = lane >> 5;
    const int e = (r + 1) & 7;
    const char* ubase = smem + UC_OFF + e * UC_STRIDE + 2 * (r - 7 - e + 64 - 8 * h);
    for (int dl = wid; dl < 64; dl += 4) {
      bf16x8 a0 = *reinterpret_cast<const bf16x8*>(ubase + 64 * dl);
      bf16x8 a1 = *reinterpret_cast<const bf16x8*>(ubase + 64 * dl - 32);
      {
        int m = 32 + r - dl;
        int off = (m >= 0) ? (m * KROW + 16 * h) : (KZ_OFF - KH_OFF);
        int off1 = (m >= 0) ? (off + 32) : off;
        bf16x8 bh0 = *reinterpret_cast<const bf16x8*>(smem + KH_OFF + off);
        bf16x8 bh1 = *reinterpret_cast<const bf16x8*>(smem + KH_OFF + off1);
        bf16x8 bl0 = *reinterpret_cast<const bf16x8*>(smem + ((m >= 0) ? KL_OFF : KH_OFF) + off);
        bf16x8 bl1 = *reinterpret_cast<const bf16x8*>(smem + ((m >= 0) ? KL_OFF : KH_OFF) + off1);
        y1 = __builtin_amdgcn_mfma_f32_32x32x16_bf16(a0, bh0, y1, 0, 0, 0);
        y1 = __builtin_amdgcn_mfma_f32_32x32x16_bf16(a1, bh1, y1, 0, 0, 0);
        y1 = __builtin_amdgcn_mfma_f32_32x32x16_bf16(a0, bl0, y1, 0, 0, 0);
        y1 = __builtin_amdgcn_mfma_f32_32x32x16_bf16(a1, bl1, y1, 0, 0, 0);
      }
      if (dl < 32) {
        int m = r - dl;
        int off = (m >= 0) ? (m * KROW + 16 * h) : (KZ_OFF - KH_OFF);
        int off1 = (m >= 0) ? (off + 32) : off;
        bf16x8 bh0 = *reinterpret_cast<const bf16x8*>(smem + KH_OFF + off);
        bf16x8 bh1 = *reinterpret_cast<const bf16x8*>(smem + KH_OFF + off1);
        bf16x8 bl0 = *reinterpret_cast<const bf16x8*>(smem + ((m >= 0) ? KL_OFF : KH_OFF) + off);
        bf16x8 bl1 = *reinterpret_cast<const bf16x8*>(smem + ((m >= 0) ? KL_OFF : KH_OFF) + off1);
        y0 = __builtin_amdgcn_mfma_f32_32x32x16_bf16(a0, bh0, y0, 0, 0, 0);
        y0 = __builtin_amdgcn_mfma_f32_32x32x16_bf16(a1, bh1, y0, 0, 0, 0);
        y0 = __builtin_amdgcn_mfma_f32_32x32x16_bf16(a0, bl0, y0, 0, 0, 0);
        y0 = __builtin_amdgcn_mfma_f32_32x32x16_bf16(a1, bl1, y0, 0, 0, 0);
      }
    }
  }
  __syncthreads();
  {
    const int r = lane & 31, h = lane >> 5;
    float* slab = reinterpret_cast<float*>(smem + wid * 8704);
#pragma unroll
    for (int g = 0; g < 4; ++g) {
      f32x4 v0 = {y0[4 * g], y0[4 * g + 1], y0[4 * g + 2], y0[4 * g + 3]};
      f32x4 v1 = {y1[4 * g], y1[4 * g + 1], y1[4 * g + 2], y1[4 * g + 3]};
      int b = 8 * g + 4 * h;
      *reinterpret_cast<f32x4*>(slab + 32 * r + b) = v0;
      *reinterpret_cast<f32x4*>(slab + 32 * (32 + r) + b) = v1;
    }
  }
  __syncthreads();
  {
    const float dsk = bf2f(Dsk[d]);
    const float* s = reinterpret_cast<const float*>(smem);
    f32x4 ys[2];
#pragma unroll
    for (int hh = 0; hh < 2; ++hh) {
      f32x4 s0 = *reinterpret_cast<const f32x4*>(s + 8 * tid + 4 * hh);
      f32x4 s1 = *reinterpret_cast<const f32x4*>(s + 2176 + 8 * tid + 4 * hh);
      f32x4 s2 = *reinterpret_cast<const f32x4*>(s + 4352 + 8 * tid + 4 * hh);
      f32x4 s3 = *reinterpret_cast<const f32x4*>(s + 6528 + 8 * tid + 4 * hh);
      ys[hh] = (s0 + s1) + (s2 + s3);
    }
    unsigned short o[8];
#pragma unroll
    for (int k = 0; k < 8; ++k) {
      float yb = rbf(ys[k >> 2][k & 3]);
      float uu = bf2f((unsigned short)(uvec[k >> 1] >> (16 * (k & 1))));
      float sk = rbf(uu * dsk);
      o[k] = f2bf(yb + sk);
    }
    u32x4 v;
#pragma unroll
    for (int j = 0; j < 4; ++j) v[j] = o[2 * j] | ((unsigned)o[2 * j + 1] << 16);
    *reinterpret_cast<u32x4*>(aT + (size_t)d * L_ + 8 * tid) = v;
  }
}

constexpr int N_TR = (L_ / 64) * (D_ / 64);
constexpr int N_GATE = L_ / 8;

__global__ __launch_bounds__(256) void kernel0(const unsigned short* __restrict__ u, const unsigned short* __restrict__ gw,
                                               unsigned short* __restrict__ uT, float* __restrict__ gbuf) {
  __shared__ unsigned short tile[64][72];
  const int tid = threadIdx.x;
  if (blockIdx.x < N_TR) {
    const int tl = blockIdx.x / (D_ / 64), td = blockIdx.x % (D_ / 64);
#pragma unroll
    for (int i = 0; i < 2; ++i) {
      int id = tid + 256 * i, row = id >> 3, ch = id & 7;
      u32x4 v = *reinterpret_cast<const u32x4*>(u + (size_t)(tl * 64 + row) * D_ + td * 64 + ch * 8);
      *reinterpret_cast<u32x4*>(&tile[row][ch * 8]) = v;
    }
    __syncthreads();
#pragma unroll
    for (int i = 0; i < 2; ++i) {
      int id = tid + 256 * i, col = id >> 3, ch = id & 7;
      unsigned short o[8];
#pragma unroll
      for (int k = 0; k < 8; ++k) o[k] = tile[ch * 8 + k][col];
      u32x4 v;
#pragma unroll
      for (int j = 0; j < 4; ++j) v[j] = o[2 * j] | ((unsigned)o[2 * j + 1] << 16);
      *reinterpret_cast<u32x4*>(uT + (size_t)(td * 64 + col) * L_ + tl * 64 + ch * 8) = v;
    }
  } else {
    const int lane = tid & 63, wid = tid >> 6;
    const int row0 = ((blockIdx.x - N_TR) * 4 + wid) * 2;
    u32x4 g0 = *reinterpret_cast<const u32x4*>(gw + lane * 8);
    u32x4 g1 = {0, 0, 0, 0};
    if (lane < 16) g1 = *reinterpret_cast<const u32x4*>(gw + 512 + lane * 8);
#pragma unroll
    for (int rr = 0; rr < 2; ++rr) {
      const unsigned short* xr = u + (size_t)(row0 + rr) * D_;
      u32x4 x0 = *reinterpret_cast<const u32x4*>(xr + lane * 8);
      u32x4 x1 = {0, 0, 0, 0};
      if (lane < 16) x1 = *reinterpret_cast<const u32x4*>(xr + 512 + lane * 8);
      float s = 0.f;
#pragma unroll
      for (int j = 0; j < 4; ++j) {
        s += rbf(__uint_as_float(x0[j] << 16) * __uint_as_float(g0[j] << 16));
        s += rbf(__uint_as_float(x0[j] & 0xffff0000u) * __uint_as_float(g0[j] & 0xffff0000u));
        s += rbf(__uint_as_float(x1[j] << 16) * __uint_as_float(g1[j] << 16));
        s += rbf(__uint_as_float(x1[j] & 0xffff0000u) * __uint_as_float(g1[j] & 0xffff0000u));
      }
#pragma unroll
      for (int o = 32; o >= 1; o >>= 1) s += __shfl_xor(s, o);
      if (lane == 0) gbuf[row0 + rr] = rbf(1.0f / (1.0f + expf(-rbf(s))));
    }
  }
}

constexpr int N_CONV = D_;
constexpr int N_G1 = (L_ / G1_BM) * (F_ / GBN);

__global__ __launch_bounds__(256) void kernelA(const unsigned short* __restrict__ u, const unsigned short* __restrict__ uT,
                                               const unsigned short* __restrict__ logAr,
                                               const unsigned short* __restrict__ Aim, const unsigned short* __restrict__ Cre,
                                               const unsigned short* __restrict__ Cim, const unsigned short* __restrict__ logdt,
                                               const unsigned short* __restrict__ Dsk, const unsigned short* __restrict__ w1,
                                               unsigned short* __restrict__ aT, unsigned short* __restrict__ hbuf) {
  extern __shared__ __attribute__((aligned(16))) char smem[];
  int b = blockIdx.x;
  if (b < N_CONV) {
    conv_role(b, uT, logAr, Aim, Cre, Cim, logdt, Dsk, aT, smem);
  } else {
    int t = b - N_CONV;
    t = (t & 7) * (N_G1 / 8) + (t >> 3);
    int tm = t / (F_ / GBN), tn = t % (F_ / GBN);
    f32x16 acc[2];
    gemm_tile<G1_BM>(u, w1, D_, F_, tm * G1_BM, tn * GBN, smem, acc);
    const int lane = threadIdx.x & 63, wid = threadIdx.x >> 6;
    const int wm = wid >> 1, wn = wid & 1, r = lane & 31, h = lane >> 5;
#pragma unroll
    for (int mt = 0; mt < 2; ++mt)
#pragma unroll
      for (int i = 0; i < 16; ++i) {
        int row = tm * G1_BM + wm * 64 + mt * 32 + (i & 3) + 8 * (i >> 2) + 4 * h;
        int col = tn * GBN + wn * 32 + r;
        float hv = rbf(acc[mt][i]);
        float sv = hv / (1.0f + __expf(-hv));
        hbuf[(size_t)row * F_ + col] = f2bf(sv);
      }
  }
}

constexpr int N_G2 = (L_ / G2_BM) * (D_ / GBN);

__global__ __launch_bounds__(256) void kernelB(const unsigned short* __restrict__ hbuf, const unsigned short* __restrict__ w2,
                                               const unsigned short* __restrict__ aT, const float* __restrict__ gbuf,
                                               unsigned short* __restrict__ out) {
  extern __shared__ __attribute__((aligned(16))) char smem[];
  int t = blockIdx.x;
  t = (t & 7) * (N_G2 / 8) + (t >> 3);
  int tm = t / (D_ / GBN), tn = t % (D_ / GBN);
  f32x16 acc[1];
  gemm_tile<G2_BM>(hbuf, w2, F_, D_, tm * G2_BM, tn * GBN, smem, acc);
  const int lane = threadIdx.x & 63, wid = threadIdx.x >> 6;
  const int wm = wid >> 1, wn = wid & 1, r = lane & 31, h = lane >> 5;
  const int col = tn * GBN + wn * 32 + r;
#pragma unroll
  for (int g4 = 0; g4 < 4; ++g4) {
    int row0 = tm * G2_BM + wm * 32 + 8 * g4 + 4 * h;
    u32x2 av = *reinterpret_cast<const u32x2*>(aT + (size_t)col * L_ + row0);
    f32x4 gv = *reinterpret_cast<const f32x4*>(gbuf + row0);
#pragma unroll
    for (int j = 0; j < 4; ++j) {
      float a = bf2f((unsigned short)((av[j >> 1] >> (16 * (j & 1))) & 0xffff));
      float bv = rbf(acc[0][4 * g4 + j]);
      float g = gv[j];
      float t1 = rbf(g * a);
      float t2 = rbf(1.0f - g);
      float t3 = rbf(t2 * bv);
      out[(size_t)(row0 + j) * D_ + col] = f2bf(t1 + t3);
    }
  }
}

extern "C" void kernel_launch(void* const* d_in, const int* in_sizes, int n_in, void* d_out, int out_size, void* d_ws,
                              size_t ws_size, hipStream_t stream) {
  const unsigned short* u = (const unsigned short*)d_in[0];
  const unsigned short* logAr = (const unsigned short*)d_in[1];
  const unsigned short* Aim = (const unsigned short*)d_in[2];
  const unsigned short* Cre = (const unsigned short*)d_in[3];
  const unsigned short* Cim = (const unsigned short*)d_in[4];
  const unsigned short* logdt = (const unsigned short*)d_in[5];
  const unsigned short* Dsk = (const unsigned short*)d_in[6];
  const unsigned short* w1 = (const unsigned short*)d_in[8];
  const unsigned short* w2 = (const unsigned short*)d_in[9];
  const unsigned short* gw = (const unsigned short*)d_in[10];
  unsigned short* out = (unsigned short*)d_out;

  char* ws = (char*)d_ws;
  unsigned short* hbuf = (unsigned short*)ws;
  unsigned short* aT = (unsigned short*)(ws + (size_t)L_ * F_ * 2);
  unsigned short* uT = (unsigned short*)(ws + (size_t)L_ * F_ * 2 + (size_t)D_ * L_ * 2);
  float* gbuf = (float*)(ws + (size_t)L_ * F_ * 2 + 2 * (size_t)D_ * L_ * 2);

  hipLaunchKernelGGL(kernel0, dim3(N_TR + N_GATE), dim3(256), 0, stream, u, gw, uT, gbuf);
  hipLaunchKernelGGL(kernelA, dim3(N_CONV + N_G1), dim3(256), LDS_A_BYTES, stream, u, uT, logAr, Aim, Cre, Cim, logdt,
                     Dsk, w1, aT, hbuf);
  hipLaunchKernelGGL(kernelB, dim3(N_G2), dim3(256), GemmCfg<G2_BM>::LDS, stream, hbuf, w2, aT, gbuf, out);
}
```

```cpp
#include <hip/hip_runtime.h>
#include <hip/hip_bf16.h>
#include <stdint.h>

typedef __attribute__((ext_vector_type(8))) short bf16x8;
typedef __attribute__((ext_vector_type(4))) short bf16x4;
typedef __attribute__((ext_vector_type(16))) float f32x16;
typedef __attribute__((ext_vector_type(4))) float f32x4;
typedef __attribute__((ext_vector_type(4))) unsigned int u32x4;
typedef __attribute__((ext_vector_type(2))) unsigned int u32x2;

#define LDS_AS __attribute__((address_space(3)))
#define RAW_BARRIER() do { asm volatile("s_waitcnt lgkmcnt(0)" ::: "memory"); __builtin_amdgcn_s_barrier(); } while (0)

constexpr int L_ = 2048, D_ = 640, NS_ = 128, F_ = 1728;

__device__ __forceinline__ float bf2f(unsigned short v) { return __uint_as_float(((unsigned)v) << 16); }
__device__ __forceinline__ unsigned short f2bf(float f) {
  __hip_bfloat16 b = __float2bfloat16(f);
  return *reinterpret_cast<unsigned short*>(&b);
}
__device__ __forceinline__ float rbf(float f) { return bf2f(f2bf(f)); }

constexpr int G1_TM = L_ / 128, G1_TN = (F_ + 127) / 128;
constexpr int N_G1 = G1_TM * G1_TN;
constexpr int G1_STAGE = 32768, G1_LDS = 3 * G1_STAGE;
constexpr int N_HU = 16, N_HW = 16;
constexpr int K1_GRID = N_G1 + N_HU + N_HW;

__device__ __forceinline__ void tr_tiles_load(const unsigned short* __restrict__ src, int ss, int r0, int c0,
                                              u32x4 (&v)[2]) {
  const int tid = threadIdx.x;
#pragma unroll
  for (int i = 0; i < 2; ++i) {
    int id = tid + 256 * i, row = id >> 3, ch = id & 7;
    v[i] = *reinterpret_cast<const u32x4*>(src + (size_t)(r0 + row) * ss + c0 + ch * 8);
  }
}
__device__ __forceinline__ void tr_tiles_put(unsigned short (*tile)[72], const u32x4 (&v)[2]) {
  const int tid = threadIdx.x;
#pragma unroll
  for (int i = 0; i < 2; ++i) {
    int id = tid + 256 * i, row = id >> 3, ch = id & 7;
    *reinterpret_cast<u32x4*>(&tile[row][ch * 8]) = v[i];
  }
}
__device__ __forceinline__ void tr_tiles_store(unsigned short (*tile)[72], unsigned short* __restrict__ dst, int ds,
                                               int r0, int c0) {
  const int tid = threadIdx.x;
#pragma unroll
  for (int i = 0; i < 2; ++i) {
    int id = tid + 256 * i, col = id >> 3, ch = id & 7;
    unsigned short o[8];
#pragma unroll
    for (int k = 0; k < 8; ++k) o[k] = tile[ch * 8 + k][col];
    u32x4 w;
#pragma unroll
    for (int j = 0; j < 4; ++j) w[j] = o[2 * j] | ((unsigned)o[2 * j + 1] << 16);
    *reinterpret_cast<u32x4*>(dst + (size_t)(c0 + col) * ds + r0 + ch * 8) = w;
  }
}

__global__ __launch_bounds__(256, 1) void k1_gemm1(const unsigned short* __restrict__ x, const unsigned short* __restrict__ w1,
                                                   const unsigned short* __restrict__ w2, const unsigned short* __restrict__ gw,
                                                   unsigned short* __restrict__ hbuf, unsigned short* __restrict__ uT,
                                                   unsigned short* __restrict__ w2T, float* __restrict__ gbuf) {
  extern __shared__ __attribute__((aligned(16))) char smem[];
  const int tid = threadIdx.x, lane = tid & 63, wid = tid >> 6;
  const int b = blockIdx.x;
  if (b >= N_G1) {
    typedef unsigned short tile_t[64][72];
    tile_t* tiles = reinterpret_cast<tile_t*>(smem);
    if (b < N_G1 + N_HU) {
      const int hb = b - N_G1;
      for (int rb = 0; rb < 2; ++rb) {
        const int r0 = (hb * 2 + rb) * 64;
        u32x4 v[10][2];
#pragma unroll
        for (int t = 0; t < 10; ++t) tr_tiles_load(x, D_, r0, t * 64, v[t]);
        float s[2] = {0.f, 0.f};
#pragma unroll
        for (int t = 0; t < 10; ++t) {
          u32x4 g = *reinterpret_cast<const u32x4*>(gw + t * 64 + (tid & 7) * 8);
#pragma unroll
          for (int i = 0; i < 2; ++i)
#pragma unroll
            for (int j = 0; j < 4; ++j) {
              s[i] += rbf(__uint_as_float(v[t][i][j] << 16) * __uint_as_float(g[j] << 16));
              s[i] += rbf(__uint_as_float(v[t][i][j] & 0xffff0000u) * __uint_as_float(g[j] & 0xffff0000u));
            }
          tr_tiles_put(tiles[t], v[t]);
        }
#pragma unroll
        for (int i = 0; i < 2; ++i) {
          s[i] += __shfl_xor(s[i], 1); s[i] += __shfl_xor(s[i], 2); s[i] += __shfl_xor(s[i], 4);
          if ((tid & 7) == 0) gbuf[r0 + (tid >> 3) + 32 * i] = rbf(1.0f / (1.0f + expf(-rbf(s[i]))));
        }
        __syncthreads();
#pragma unroll
        for (int t = 0; t < 10; ++t) tr_tiles_store(tiles[t], uT, L_, r0, t * 64);
        __syncthreads();
      }
    } else {
      const int hb = b - N_G1 - N_HU;
      for (int t0 = hb; t0 < 270; t0 += N_HW * 9) {
        u32x4 v[9][2];
#pragma unroll
        for (int j = 0; j < 9; ++j) {
          int t = t0 + j * N_HW;
          if (t < 270) tr_tiles_load(w2, D_, (t / 10) * 64, (t % 10) * 64, v[j]);
        }
#pragma unroll
        for (int j = 0; j < 9; ++j) {
          int t = t0 + j * N_HW;
          if (t < 270) tr_tiles_put(tiles[j], v[j]);
        }
        __syncthreads();
#pragma unroll
        for (int j = 0; j < 9; ++j) {
          int t = t0 + j * N_HW;
          if (t < 270) tr_tiles_store(tiles[j], w2T, F_, (t / 10) * 64, (t % 10) * 64);
        }
        __syncthreads();
      }
    }
    return;
  }

  int t = (b & 7) * (N_G1 / 8) + (b >> 3);
  const int tm = t / G1_TN, tn = t % G1_TN;
  const int m0 = tm * 128, n0 = tn * 128;
  const int wm = wid >> 1, wn = wid & 1, r = lane & 31, h = lane >> 5;

  const unsigned short* asrc[4];
  const unsigned short* bsrc[4];
#pragma unroll
  for (int i = 0; i < 4; ++i) {
    int row = (i * 4 + wid) * 8 + (lane >> 3);
    int c = (lane & 7) ^ (row & 7);
    asrc[i] = x + (size_t)(m0 + row) * D_ + c * 8;
    int k = (i * 4 + wid) * 4 + (lane >> 4);
    int cb = (lane & 15) ^ ((k & 3) << 1);
    int col = n0 + cb * 8;
    if (col > F_ - 8) col = F_ - 8;
    bsrc[i] = w1 + (size_t)k * F_ + col;
  }
  auto stage = [&](int kt, int buf) {
    char* sa = smem + buf * G1_STAGE;
    char* sb = sa + 16384;
#pragma unroll
    for (int i = 0; i < 4; ++i)
      __builtin_amdgcn_global_load_lds((const unsigned*)(asrc[i] + kt * 64), (LDS_AS unsigned*)(sa + (i * 4 + wid) * 1024), 16, 0, 0);
#pragma unroll
    for (int i = 0; i < 4; ++i)
      __builtin_amdgcn_global_load_lds((const unsigned*)(bsrc[i] + (size_t)kt * 64 * F_), (LDS_AS unsigned*)(sb + (i * 4 + wid) * 1024), 16, 0, 0);
  };

  f32x16 acc[2][2];
#pragma unroll
  for (int a = 0; a < 2; ++a)
#pragma unroll
    for (int c = 0; c < 2; ++c)
#pragma unroll
      for (int i = 0; i < 16; ++i) acc[a][c][i] = 0.f;

  const int q = (lane & 15) >> 2, p = lane & 3, blk = (lane >> 4) & 1;
  auto compute = [&](int buf) {
    const char* sa = smem + buf * G1_STAGE;
    const char* sb = sa + 16384;
#pragma unroll
    for (int s = 0; s < 4; ++s) {
      bf16x8 af[2], bfr[2];
#pragma unroll
      for (int mt = 0; mt < 2; ++mt) {
        int row = wm * 64 + mt * 32 + r;
        af[mt] = *reinterpret_cast<const bf16x8*>(sa + row * 128 + (((2 * s + h) ^ (row & 7)) << 4));
      }
#pragma unroll
      for (int nt = 0; nt < 2; ++nt)
#pragma unroll
        for (int tt = 0; tt < 2; ++tt) {
          int k = 16 * s + 8 * h + 4 * tt + q;
          int chunk = wn * 8 + nt * 4 + 2 * blk + (p >> 1);
          const char* ad = sb + k * 256 + ((chunk ^ (q << 1)) << 4) + (p & 1) * 8;
          bf16x4 v = __builtin_amdgcn_ds_read_tr16_b64_v4i16((LDS_AS bf16x4*)(ad));
          bfr[nt][4 * tt + 0] = v[0]; bfr[nt][4 * tt + 1] = v[1]; bfr[nt][4 * tt + 2] = v[2]; bfr[nt][4 * tt + 3] = v[3];
        }
#pragma unroll
      for (int mt = 0; mt < 2; ++mt)
#pragma unroll
        for (int nt = 0; nt < 2; ++nt)
          acc[mt][nt] = __builtin_amdgcn_mfma_f32_32x32x16_bf16(af[mt], bfr[nt], acc[mt][nt], 0, 0, 0);
    }
  };

  constexpr int NK = D_ / 64;
  stage(0, 0);
  stage(1, 1);
  for (int kt = 0; kt < NK - 1; ++kt) {
    asm volatile("s_waitcnt vmcnt(8)" ::: "memory");
    RAW_BARRIER();
    if (kt + 2 < NK) stage(kt + 2, (kt + 2) % 3);
    compute(kt % 3);
  }
  asm volatile("s_waitcnt vmcnt(0)" ::: "memory");
  RAW_BARRIER();
  compute((NK - 1) % 3);

#pragma unroll
  for (int nt = 0; nt < 2; ++nt) {
    const int col = n0 + wn * 64 + nt * 32 + r;
    if (col < F_) {
#pragma unroll
      for (int mt = 0; mt < 2; ++mt)
#pragma unroll
        for (int i = 0; i < 16; ++i) {
          int row = m0 + wm * 64 + mt * 32 + (i & 3) + 8 * (i >> 2) + 4 * h;
          float hv = rbf(acc[mt][nt][i]);
          float sv = hv / (1.0f + __expf(-hv));
          hbuf[(size_t)row * F_ + col] = f2bf(sv);
        }
    }
  }
}

constexpr int UC_STRIDE = 4384;
constexpr int UC_OFF = 0;
constexpr int KROW = 80;
constexpr int KH_OFF = 8 * UC_STRIDE;
constexpr int KL_OFF = KH_OFF + 64 * KROW + 16;
constexpr int KZ_OFF = KH_OFF + 64 * KROW;
constexpr int PR_OFF = KL_OFF + 64 * KROW;
constexpr int OS_OFF = PR_OFF + 128 * 16;
constexpr int CONV_LDS = OS_OFF + 4 * 48 * 16 * 4;

__device__ __forceinline__ float thi_of(int i) {
  int t;
  if (i < 8) t = i;
  else if (i < 16) t = 128 + (i - 8);
  else if (i < 24) t = 256 + 128 * ((i - 16) >> 2) + 2 * ((i - 16) & 3);
  else if (i < 32) t = 512 + 128 * ((i - 24) >> 1) + 4 * ((i - 24) & 1);
  else if (i < 40) t = 1024 + 128 * (i - 32);
  else t = 2048;
  return (float)t;
}

__device__ void conv_role(int d, const unsigned short* __restrict__ uT, const unsigned short* __restrict__ logAr,
                          const unsigned short* __restrict__ Aim, const unsigned short* __restrict__ Cre,
                          const unsigned short* __restrict__ Cim, const unsigned short* __restrict__ logdt,
                          const unsigned short* __restrict__ Dsk, unsigned short* __restrict__ aT, char* smem) {
  const int tid = threadIdx.x, lane = tid & 63, wid = tid >> 6;

  const u32x4 uvec = *reinterpret_cast<const u32x4*>(uT + (size_t)d * L_ + 8 * tid);

  const float dt = rbf(expf(bf2f(logdt[d])));
  if (tid < NS_) {
    float are = -rbf(expf(bf2f(logAr[d * NS_ + tid])));
    float aim = bf2f(Aim[d * NS_ + tid]);
    float lr = are * dt, li = aim * dt;
    float cr = bf2f(Cre[d * NS_ + tid]) * dt, ci = bf2f(Cim[d * NS_ + tid]) * dt;
    float mag = sqrtf(cr * cr + ci * ci);
    f32x4 pp;
    pp[0] = lr * 1.4426950408889634f;
    pp[1] = li * 0.15915494309189535f;
    pp[2] = log2f(mag);
    pp[3] = atan2f(ci, cr) * 0.15915494309189535f;
    *reinterpret_cast<f32x4*>(smem + PR_OFF + tid * 16) = pp;
  }
  if (tid == 0) { u32x4 z = {0, 0, 0, 0}; *reinterpret_cast<u32x4*>(smem + KZ_OFF) = z; }
  if (tid < 64) {
    u32x4 z = {0, 0, 0, 0};
    *reinterpret_cast<u32x4*>(smem + UC_OFF + (tid >> 3) * UC_STRIDE + (tid & 7) * 16) = z;
  }
  *reinterpret_cast<u32x4*>(smem + UC_OFF + 128 + tid * 16) = uvec;
  __syncthreads();

  {
    u32x4 v1 = {0, 0, 0, 0};
    if (tid < 255) v1 = *reinterpret_cast<const u32x4*>(smem + UC_OFF + 128 + (tid + 1) * 16);
    unsigned w[8] = {uvec[0], uvec[1], uvec[2], uvec[3], v1[0], v1[1], v1[2], v1[3]};
#pragma unroll
    for (int e = 1; e < 8; ++e) {
      u32x4 o;
#pragma unroll
      for (int j = 0; j < 4; ++j) {
        int base = j + (e >> 1);
        if (e & 1) o[j] = (w[base] >> 16) | (w[base + 1] << 16);
        else o[j] = w[base];
      }
      *reinterpret_cast<u32x4*>(smem + UC_OFF + e * UC_STRIDE + 128 + tid * 16) = o;
    }
    if (tid == 0) {
#pragma unroll
      for (int e = 1; e < 8; ++e)
#pragma unroll
        for (int k = 0; k < e; ++k)
          *reinterpret_cast<unsigned short*>(smem + UC_OFF + e * UC_STRIDE + 2 * (64 - e + k)) =
              (unsigned short)(w[k >> 1] >> (16 * (k & 1)));
    }
  }

  {
    const int x = lane & 15, kk = lane >> 4;
    float th[3];
#pragma unroll
    for (int T = 0; T < 3; ++T) th[T] = thi_of(16 * T + x);
    const float tlo = (float)(8 * x);
    f32x4 acc[3];
#pragma unroll
    for (int T = 0; T < 3; ++T) acc[T] = (f32x4){0.f, 0.f, 0.f, 0.f};
#pragma unroll 2
    for (int g = 0; g < 8; ++g) {
      int n = 32 * wid + 4 * g + kk;
      f32x4 pp = *reinterpret_cast<const f32x4*>(smem + PR_OFF + n * 16);
      float qe = __builtin_amdgcn_exp2f(pp[0] * tlo);
      float qa = pp[1] * tlo;
      float qr = qe * __builtin_amdgcn_cosf(qa), qi = qe * __builtin_amdgcn_sinf(qa);
#pragma unroll
      for (int T = 0; T < 3; ++T) {
        float pe = __builtin_amdgcn_exp2f(pp[0] * th[T] + pp[2]);
        float pa = pp[1] * th[T] + pp[3];
        float pr = pe * __builtin_amdgcn_cosf(pa), pi = pe * __builtin_amdgcn_sinf(pa);
        acc[T] = __builtin_amdgcn_mfma_f32_16x16x4f32(qr, pr, acc[T], 0, 0, 0);
        acc[T] = __builtin_amdgcn_mfma_f32_16x16x4f32(-qi, pi, acc[T], 0, 0, 0);
      }
    }
    float* os = reinterpret_cast<float*>(smem + OS_OFF) + wid * 768;
#pragma unroll
    for (int T = 0; T < 3; ++T) *reinterpret_cast<f32x4*>(os + (16 * T + x) * 16 + 4 * kk) = acc[T];
  }
  __syncthreads();

  {
    unsigned short kh[8], kl[8];
#pragma unroll
    for (int k = 0; k < 8; ++k) {
      int j = 8 * tid + k;
      int rr = (int)rbf((float)j);
      int i;
      if (rr < 256) i = (rr & 7) + 8 * (rr >> 7);
      else if (rr < 512) i = 16 + 4 * ((rr - 256) >> 7) + ((rr & 7) >> 1);
      else if (rr < 1024) i = 24 + 2 * ((rr - 512) >> 7) + ((rr & 7) >> 2);
      else if (rr < 2048) i = 32 + ((rr - 1024) >> 7);
      else i = 40;
      int jj = (rr >> 3) & 15;
      const float* osp = reinterpret_cast<const float*>(smem + OS_OFF) + i * 16 + jj;
      float kv = 2.0f * ((osp[0] + osp[768]) + (osp[1536] + osp[2304]));
      unsigned short hi = f2bf(kv);
      kh[7 - k] = hi;
      kl[7 - k] = f2bf(kv - bf2f(hi));
    }
    u32x4 vh, vl;
#pragma unroll
    for (int j = 0; j < 4; ++j) {
      vh[j] = kh[2 * j] | ((unsigned)kh[2 * j + 1] << 16);
      vl[j] = kl[2 * j] | ((unsigned)kl[2 * j + 1] << 16);
    }
    *reinterpret_cast<u32x4*>(smem + KH_OFF + (tid >> 2) * KROW + (tid & 3) * 16) = vh;
    *reinterpret_cast<u32x4*>(smem + KL_OFF + (tid >> 2) * KROW + (tid & 3) * 16) = vl;
  }
  __syncthreads();

  f32x16 y0, y1;
#pragma unroll
  for (int i = 0; i < 16; ++i) { y0[i] = 0.f; y1[i] = 0.f; }
  {
    const int r = lane & 31, h = lane >> 5;
    const int e = (r + 1) & 7;
    const char* ubase = smem + UC_OFF + e * UC_STRIDE + 2 * (r - 7 - e + 64 - 8 * h);
    for (int dl = wid; dl < 64; dl += 4) {
      bf16x8 a0 = *reinterpret_cast<const bf16x8*>(ubase + 64 * dl);
      bf16x8 a1 = *reinterpret_cast<const bf16x8*>(ubase + 64 * dl - 32);
      {
        int m = 32 + r - dl;
        int off = (m >= 0) ? (m * KROW + 16 * h) : (KZ_OFF - KH_OFF);
        int off1 = (m >= 0) ? (off + 32) : off;
        bf16x8 bh0 = *reinterpret_cast<const bf16x8*>(smem + KH_OFF + off);
        bf16x8 bh1 = *reinterpret_cast<const bf16x8*>(smem + KH_OFF + off1);
        bf16x8 bl0 = *reinterpret_cast<const bf16x8*>(smem + ((m >= 0) ? KL_OFF : KH_OFF) + off);
        bf16x8 bl1 = *reinterpret_cast<const bf16x8*>(smem + ((m >= 0) ? KL_OFF : KH_OFF) + off1);
        y1 = __builtin_amdgcn_mfma_f32_32x32x16_bf16(a0, bh0, y1, 0, 0, 0);
        y1 = __builtin_amdgcn_mfma_f32_32x32x16_bf16(a1, bh1, y1, 0, 0, 0);
        y1 = __builtin_amdgcn_mfma_f32_32x32x16_bf16(a0, bl0, y1, 0, 0, 0);
        y1 = __builtin_amdgcn_mfma_f32_32x32x16_bf16(a1, bl1, y1, 0, 0, 0);
      }
      if (dl < 32) {
        int m = r - dl;
        int off = (m >= 0) ? (m * KROW + 16 * h) : (KZ_OFF - KH_OFF);
        int off1 = (m >= 0) ? (off + 32) : off;
        bf16x8 bh0 = *reinterpret_cast<const bf16x8*>(smem + KH_OFF + off);
        bf16x8 bh1 = *reinterpret_cast<const bf16x8*>(smem + KH_OFF + off1);
        bf16x8 bl0 = *reinterpret_cast<const bf16x8*>(smem + ((m >= 0) ? KL_OFF : KH_OFF) + off);
        bf16x8 bl1 = *reinterpret_cast<const bf16x8*>(smem + ((m >= 0) ? KL_OFF : KH_OFF) + off1);
        y0 = __builtin_amdgcn_mfma_f32_32x32x16_bf16(a0, bh0, y0, 0, 0, 0);
        y0 = __builtin_amdgcn_mfma_f32_32x32x16_bf16(a1, bh1, y0, 0, 0, 0);
        y0 = __builtin_amdgcn_mfma_f32_32x32x16_bf16(a0, bl0, y0, 0, 0, 0);
        y0 = __builtin_amdgcn_mfma_f32_32x32x16_bf16(a1, bl1, y0, 0, 0, 0);
      }
    }
  }
  __syncthreads();
  {
    const int r = lane & 31, h = lane >> 5;
    float* slab = reinterpret_cast<float*>(smem + wid * 8704);
#pragma unroll
    for (int g = 0; g < 4; ++g) {
      f32x4 v0 = {y0[4 * g], y0[4 * g + 1], y0[4 * g + 2], y0[4 * g + 3]};
      f32x4 v1 = {y1[4 * g], y1[4 * g + 1], y1[4 * g + 2], y1[4 * g + 3]};
      int b = 8 * g + 4 * h;
      *reinterpret_cast<f32x4*>(slab + 32 * r + b) = v0;
      *reinterpret_cast<f32x4*>(slab + 32 * (32 + r) + b) = v1;
    }
  }
  __syncthreads();
  {
    const float dsk = bf2f(Dsk[d]);
    const float* s = reinterpret_cast<const float*>(smem);
    f32x4 ys[2];
#pragma unroll
    for (int hh = 0; hh < 2; ++hh) {
      f32x4 s0 = *reinterpret_cast<const f32x4*>(s + 8 * tid + 4 * hh);
      f32x4 s1 = *reinterpret_cast<const f32x4*>(s + 2176 + 8 * tid + 4 * hh);
      f32x4 s2 = *reinterpret_cast<const f32x4*>(s + 4352 + 8 * tid + 4 * hh);
      f32x4 s3 = *reinterpret_cast<const f32x4*>(s + 6528 + 8 * tid + 4 * hh);
      ys[hh] = (s0 + s1) + (s2 + s3);
    }
    unsigned short o[8];
#pragma unroll
    for (int k = 0; k < 8; ++k) {
      float yb = rbf(ys[k >> 2][k & 3]);
      float uu = bf2f((unsigned short)(uvec[k >> 1] >> (16 * (k & 1))));
      float sk = rbf(uu * dsk);
      o[k] = f2bf(yb + sk);
    }
    u32x4 v;
#pragma unroll
    for (int j = 0; j < 4; ++j) v[j] = o[2 * j] | ((unsigned)o[2 * j + 1] << 16);
    *reinterpret_cast<u32x4*>(aT + (size_t)d * L_ + 8 * tid) = v;
  }
}

__global__ __launch_bounds__(256) void k2_conv(const unsigned short* __restrict__ uT, const unsigned short* __restrict__ logAr,
                                               const unsigned short* __restrict__ Aim, const unsigned short* __restrict__ Cre,
                                               const unsigned short* __restrict__ Cim, const unsigned short* __restrict__ logdt,
                                               const unsigned short* __restrict__ Dsk, unsigned short* __restrict__ aT) {
  extern __shared__ __attribute__((aligned(16))) char smem[];
  conv_role(blockIdx.x, uT, logAr, Aim, Cre, Cim, logdt, Dsk, aT, smem);
}

constexpr int G2_RS = 84;
__global__ __launch_bounds__(256, 1) void k3_gemm2(const unsigned short* __restrict__ hbuf, const unsigned short* __restrict__ w2T,
                                                   const unsigned short* __restrict__ aT, const float* __restrict__ gbuf,
                                                   unsigned short* __restrict__ out) {
  __shared__ float red[4][64][G2_RS];
  const int tid = threadIdx.x, lane = tid & 63, wid = tid >> 6;
  int t = (blockIdx.x & 7) * 32 + (blockIdx.x >> 3);
  const int tm = t >> 3, tn = t & 7;
  const int m0 = tm * 64, n0 = tn * 80;
  const int fr = lane & 15, fq = lane >> 4;
  const int c0 = (wid * 27) >> 2, c1 = ((wid + 1) * 27) >> 2;

  const unsigned short* ap = hbuf + (size_t)(m0 + fr) * F_ + 16 * fq;
  const unsigned short* bp = w2T + (size_t)(n0 + fr) * F_ + 16 * fq;

  f32x4 acc[4][5];
#pragma unroll
  for (int a = 0; a < 4; ++a)
#pragma unroll
    for (int c = 0; c < 5; ++c) acc[a][c] = (f32x4){0.f, 0.f, 0.f, 0.f};

  bf16x8 A0[4][2], B0[5][2], A1[4][2], B1[5][2];
#define G2_LOAD(AA, BB, c)                                                                             \
  {                                                                                                    \
    _Pragma("unroll") for (int a = 0; a < 4; ++a) {                                                    \
      AA[a][0] = *reinterpret_cast<const bf16x8*>(ap + (size_t)a * 16 * F_ + (c) * 64);                \
      AA[a][1] = *reinterpret_cast<const bf16x8*>(ap + (size_t)a * 16 * F_ + (c) * 64 + 8);            \
    }                                                                                                  \
    _Pragma("unroll") for (int a = 0; a < 5; ++a) {                                                    \
      BB[a][0] = *reinterpret_cast<const bf16x8*>(bp + (size_t)a * 16 * F_ + (c) * 64);                \
      BB[a][1] = *reinterpret_cast<const bf16x8*>(bp + (size_t)a * 16 * F_ + (c) * 64 + 8);            \
    }                                                                                                  \
  }
#define G2_COMP(AA, BB)                                                                                \
  {                                                                                                    \
    _Pragma("unroll") for (int s = 0; s < 2; ++s)                                                      \
      _Pragma("unroll") for (int a = 0; a < 4; ++a)                                                    \
        _Pragma("unroll") for (int c = 0; c < 5; ++c)                                                  \
          acc[a][c] = __builtin_amdgcn_mfma_f32_16x16x32_bf16(AA[a][s], BB[c][s], acc[a][c], 0, 0, 0); \
  }
  G2_LOAD(A0, B0, c0);
  for (int c = c0; c < c1; c += 2) {
    if (c + 1 < c1) G2_LOAD(A1, B1, c + 1);
    G2_COMP(A0, B0);
    if (c + 1 >= c1) break;
    if (c + 2 < c1) G2_LOAD(A0, B0, c + 2);
    G2_COMP(A1, B1);
  }
#undef G2_LOAD
#undef G2_COMP

#pragma unroll
  for (int a = 0; a < 4; ++a)
#pragma unroll
    for (int c = 0; c < 5; ++c)
#pragma unroll
      for (int i = 0; i < 4; ++i) red[wid][16 * a + 4 * fq + i][16 * c + fr] = acc[a][c][i];
  __syncthreads();

#pragma unroll
  for (int j = 0; j < 2; ++j) {
    const int col = lane + 64 * j;
    if (col < 80) {
      const int row0 = m0 + 16 * wid;
      u32x4 av0 = *reinterpret_cast<const u32x4*>(aT + (size_t)(n0 + col) * L_ + row0);
      u32x4 av1 = *reinterpret_cast<const u32x4*>(aT + (size_t)(n0 + col) * L_ + row0 + 8);
#pragma unroll
      for (int i = 0; i < 16; ++i) {
        int rr = 16 * wid + i;
        float bv = rbf((red[0][rr][col] + red[1][rr][col]) + (red[2][rr][col] + red[3][rr][col]));
        unsigned aw = (i < 8) ? av0[(i >> 1) & 3] : av1[(i >> 1) & 3];
        float a = bf2f((unsigned short)((aw >> (16 * (i & 1))) & 0xffff));
        float g = gbuf[row0 + i];
        float t1 = rbf(g * a);
        float t2 = rbf(1.0f - g);
        float t3 = rbf(t2 * bv);
        out[(size_t)(row0 + i) * D_ + n0 + col] = f2bf(t1 + t3);
      }
    }
  }
}

extern "C" void kernel_launch(void* const* d_in, const int* in_sizes, int n_in, void* d_out, int out_size, void* d_ws,
                              size_t ws_size, hipStream_t stream) {
  const unsigned short* u = (const unsigned short*)d_in[0];
  const unsigned short* logAr = (const unsigned short*)d_in[1];
  const unsigned short* Aim = (const unsigned short*)d_in[2];
  const unsigned short* Cre = (const unsigned short*)d_in[3];
  const unsigned short* Cim = (const unsigned short*)d_in[4];
  const unsigned short* logdt = (const unsigned short*)d_in[5];
  const unsigned short* Dsk = (const unsigned short*)d_in[6];
  const unsigned short* w1 = (const unsigned short*)d_in[8];
  const unsigned short* w2 = (const unsigned short*)d_in[9];
  const unsigned short* gw = (const unsigned short*)d_in[10];
  unsigned short* out = (unsigned short*)d_out;

  char* ws = (char*)d_ws;
  unsigned short* hbuf = (unsigned short*)ws;
  unsigned short* aT = (unsigned short*)(ws + (size_t)L_ * F_ * 2);
  unsigned short* uT = (unsigned short*)(ws + (size_t)L_ * F_ * 2 + (size_t)D_ * L_ * 2);
  unsigned short* w2T = (unsigned short*)(ws + (size_t)L_ * F_ * 2 + 2 * (size_t)D_ * L_ * 2);
  float* gbuf = (float*)(ws + (size_t)L_ * F_ * 2 + 2 * (size_t)D_ * L_ * 2 + (size_t)D_ * F_ * 2);

  static bool attr_set = false;
  if (!attr_set) {
    hipFuncSetAttribute((const void*)k1_gemm1, hipFuncAttributeMaxDynamicSharedMemorySize, G1_LDS);
    attr_set = true;
  }
  hipLaunchKernelGGL(k1_gemm1, dim3(K1_GRID), dim3(256), G1_LDS, stream, u, w1, w2, gw, hbuf, uT, w2T, gbuf);
  hipLaunchKernelGGL(k2_conv, dim3(D_), dim3(256), CONV_LDS, stream, uT, logAr, Aim, Cre, Cim, logdt, Dsk, aT);
  hipLaunchKernelGGL(k3_gemm2, dim3(256), dim3(256), 0, stream, hbuf, w2T, aT, gbuf, out);
}
```

```cpp
#include <hip/hip_runtime.h>
#include <hip/hip_bf16.h>
#include <stdint.h>

typedef __attribute__((ext_vector_type(8))) short bf16x8;
typedef __attribute__((ext_vector_type(4))) short bf16x4;
typedef __attribute__((ext_vector_type(16))) float f32x16;
typedef __attribute__((ext_vector_type(4))) float f32x4;
typedef __attribute__((ext_vector_type(4))) unsigned int u32x4;
typedef __attribute__((ext_vector_type(2))) unsigned int u32x2;

#define LDS_AS __attribute__((address_space(3)))
#define RAW_BARRIER() do { asm volatile("s_waitcnt lgkmcnt(0)" ::: "memory"); __builtin_amdgcn_s_barrier(); } while (0)

constexpr int L_ = 2048, D_ = 640, NS_ = 128, F_ = 1728;

__device__ __forceinline__ float bf2f(unsigned short v) { return __uint_as_float(((unsigned)v) << 16); }
__device__ __forceinline__ unsigned short f2bf(float f) {
  __hip_bfloat16 b = __float2bfloat16(f);
  return *reinterpret_cast<unsigned short*>(&b);
}
__device__ __forceinline__ float rbf(float f) { return bf2f(f2bf(f)); }

constexpr int G1_TM = L_ / 128, G1_TN = (F_ + 127) / 128;
constexpr int N_G1 = G1_TM * G1_TN;
constexpr int G1_STAGE = 32768, G1_LDS = 3 * G1_STAGE;
constexpr int K1_GRID = N_G1 + 32;

__device__ __forceinline__ void tr_tiles_load(const unsigned short* __restrict__ src, int ss, int r0, int c0,
                                              u32x4 (&v)[2]) {
  const int tid = threadIdx.x;
#pragma unroll
  for (int i = 0; i < 2; ++i) {
    int id = tid + 256 * i, row = id >> 3, ch = id & 7;
    v[i] = *reinterpret_cast<const u32x4*>(src + (size_t)(r0 + row) * ss + c0 + ch * 8);
  }
}
__device__ __forceinline__ void tr_tiles_put(unsigned short (*tile)[72], const u32x4 (&v)[2]) {
  const int tid = threadIdx.x;
#pragma unroll
  for (int i = 0; i < 2; ++i) {
    int id = tid + 256 * i, row = id >> 3, ch = id & 7;
    *reinterpret_cast<u32x4*>(&tile[row][ch * 8]) = v[i];
  }
}
__device__ __forceinline__ void tr_tiles_store(unsigned short (*tile)[72], unsigned short* __restrict__ dst, int ds,
                                               int r0, int c0) {
  const int tid = threadIdx.x;
#pragma unroll
  for (int i = 0; i < 2; ++i) {
    int id = tid + 256 * i, col = id >> 3, ch = id & 7;
    unsigned short o[8];
#pragma unroll
    for (int k = 0; k < 8; ++k) o[k] = tile[ch * 8 + k][col];
    u32x4 w;
#pragma unroll
    for (int j = 0; j < 4; ++j) w[j] = o[2 * j] | ((unsigned)o[2 * j + 1] << 16);
    *reinterpret_cast<u32x4*>(dst + (size_t)(c0 + col) * ds + r0 + ch * 8) = w;
  }
}

__global__ __launch_bounds__(256, 1) void k1_gemm1(const unsigned short* __restrict__ x, const unsigned short* __restrict__ w1,
                                                   const unsigned short* __restrict__ w2, const unsigned short* __restrict__ gw,
                                                   unsigned short* __restrict__ hbuf, unsigned short* __restrict__ uT,
                                                   unsigned short* __restrict__ w2T, float* __restrict__ gbuf) {
  extern __shared__ __attribute__((aligned(16))) char smem[];
  const int tid = threadIdx.x, lane = tid & 63, wid = tid >> 6;
  const int b = blockIdx.x;
  if (b >= N_G1) {
    const int r0 = (b - N_G1) * 64;
    u32x4 v[10][2];
#pragma unroll
    for (int t = 0; t < 10; ++t) tr_tiles_load(x, D_, r0, t * 64, v[t]);
    float s[2] = {0.f, 0.f};
#pragma unroll
    for (int t = 0; t < 10; ++t) {
      u32x4 g = *reinterpret_cast<const u32x4*>(gw + t * 64 + (tid & 7) * 8);
#pragma unroll
      for (int i = 0; i < 2; ++i)
#pragma unroll
        for (int j = 0; j < 4; ++j) {
          s[i] += rbf(__uint_as_float(v[t][i][j] << 16) * __uint_as_float(g[j] << 16));
          s[i] += rbf(__uint_as_float(v[t][i][j] & 0xffff0000u) * __uint_as_float(g[j] & 0xffff0000u));
        }
    }
#pragma unroll
    for (int i = 0; i < 2; ++i) {
      s[i] += __shfl_xor(s[i], 1); s[i] += __shfl_xor(s[i], 2); s[i] += __shfl_xor(s[i], 4);
      if ((tid & 7) == 0) gbuf[r0 + (tid >> 3) + 32 * i] = rbf(1.0f / (1.0f + expf(-rbf(s[i]))));
    }
    return;
  }

  u32x4 tv[3][2];
#pragma unroll
  for (int j = 0; j < 3; ++j) {
    int id = b + j * N_G1;
    if (id < 320) tr_tiles_load(x, D_, (id / 10) * 64, (id % 10) * 64, tv[j]);
    else if (id < 590) tr_tiles_load(w2, D_, ((id - 320) / 10) * 64, ((id - 320) % 10) * 64, tv[j]);
  }

  int t = (b & 7) * (N_G1 / 8) + (b >> 3);
  const int tm = t / G1_TN, tn = t % G1_TN;
  const int m0 = tm * 128, n0 = tn * 128;
  const int wm = wid >> 1, wn = wid & 1, r = lane & 31, h = lane >> 5;

  const unsigned short* asrc[4];
  const unsigned short* bsrc[4];
#pragma unroll
  for (int i = 0; i < 4; ++i) {
    int row = (i * 4 + wid) * 8 + (lane >> 3);
    int c = (lane & 7) ^ (row & 7);
    asrc[i] = x + (size_t)(m0 + row) * D_ + c * 8;
    int k = (i * 4 + wid) * 4 + (lane >> 4);
    int cb = (lane & 15) ^ ((k & 3) << 1);
    int col = n0 + cb * 8;
    if (col > F_ - 8) col = F_ - 8;
    bsrc[i] = w1 + (size_t)k * F_ + col;
  }
  auto stage = [&](int kt, int buf) {
    char* sa = smem + buf * G1_STAGE;
    char* sb = sa + 16384;
#pragma unroll
    for (int i = 0; i < 4; ++i)
      __builtin_amdgcn_global_load_lds((const unsigned*)(asrc[i] + kt * 64), (LDS_AS unsigned*)(sa + (i * 4 + wid) * 1024), 16, 0, 0);
#pragma unroll
    for (int i = 0; i < 4; ++i)
      __builtin_amdgcn_global_load_lds((const unsigned*)(bsrc[i] + (size_t)kt * 64 * F_), (LDS_AS unsigned*)(sb + (i * 4 + wid) * 1024), 16, 0, 0);
  };

  f32x16 acc[2][2];
#pragma unroll
  for (int a = 0; a < 2; ++a)
#pragma unroll
    for (int c = 0; c < 2; ++c)
#pragma unroll
      for (int i = 0; i < 16; ++i) acc[a][c][i] = 0.f;

  const int q = (lane & 15) >> 2, p = lane & 3, blk = (lane >> 4) & 1;
  auto compute = [&](int buf) {
    const char* sa = smem + buf * G1_STAGE;
    const char* sb = sa + 16384;
#pragma unroll
    for (int s = 0; s < 4; ++s) {
      bf16x8 af[2], bfr[2];
#pragma unroll
      for (int mt = 0; mt < 2; ++mt) {
        int row = wm * 64 + mt * 32 + r;
        af[mt] = *reinterpret_cast<const bf16x8*>(sa + row * 128 + (((2 * s + h) ^ (row & 7)) << 4));
      }
#pragma unroll
      for (int nt = 0; nt < 2; ++nt)
#pragma unroll
        for (int tt = 0; tt < 2; ++tt) {
          int k = 16 * s + 8 * h + 4 * tt + q;
          int chunk = wn * 8 + nt * 4 + 2 * blk + (p >> 1);
          const char* ad = sb + k * 256 + ((chunk ^ (q << 1)) << 4) + (p & 1) * 8;
          bf16x4 v = __builtin_amdgcn_ds_read_tr16_b64_v4i16((LDS_AS bf16x4*)(ad));
          bfr[nt][4 * tt + 0] = v[0]; bfr[nt][4 * tt + 1] = v[1]; bfr[nt][4 * tt + 2] = v[2]; bfr[nt][4 * tt + 3] = v[3];
        }
#pragma unroll
      for (int mt = 0; mt < 2; ++mt)
#pragma unroll
        for (int nt = 0; nt < 2; ++nt)
          acc[mt][nt] = __builtin_amdgcn_mfma_f32_32x32x16_bf16(af[mt], bfr[nt], acc[mt][nt], 0, 0, 0);
    }
  };

  constexpr int NK = D_ / 64;
  stage(0, 0);
  stage(1, 1);
  for (int kt = 0; kt < NK - 1; ++kt) {
    asm volatile("s_waitcnt vmcnt(8)" ::: "memory");
    RAW_BARRIER();
    if (kt + 2 < NK) stage(kt + 2, (kt + 2) % 3);
    compute(kt % 3);
  }
  asm volatile("s_waitcnt vmcnt(0)" ::: "memory");
  RAW_BARRIER();
  compute((NK - 1) % 3);

  RAW_BARRIER();
  {
    unsigned short* ht = reinterpret_cast<unsigned short*>(smem);
#pragma unroll
    for (int nt = 0; nt < 2; ++nt)
#pragma unroll
      for (int mt = 0; mt < 2; ++mt)
#pragma unroll
        for (int i = 0; i < 16; ++i) {
          int row = wm * 64 + mt * 32 + (i & 3) + 8 * (i >> 2) + 4 * h;
          float hv = rbf(acc[mt][nt][i]);
          float sv = hv / (1.0f + __expf(-hv));
          ht[row * 136 + wn * 64 + nt * 32 + r] = f2bf(sv);
        }
    typedef unsigned short tile_t[64][72];
    tile_t* tiles = reinterpret_cast<tile_t*>(smem + 36864);
#pragma unroll
    for (int j = 0; j < 3; ++j)
      if (b + j * N_G1 < 590) tr_tiles_put(tiles[j], tv[j]);
    __syncthreads();
#pragma unroll
    for (int i = 0; i < 8; ++i) {
      int id = tid + 256 * i, row = id >> 4, ch = id & 15;
      if (n0 + ch * 8 < F_)
        *reinterpret_cast<u32x4*>(hbuf + (size_t)(m0 + row) * F_ + n0 + ch * 8) =
            *reinterpret_cast<const u32x4*>(ht + row * 136 + ch * 8);
    }
#pragma unroll
    for (int j = 0; j < 3; ++j) {
      int id = b + j * N_G1;
      if (id < 320) tr_tiles_store(tiles[j], uT, L_, (id / 10) * 64, (id % 10) * 64);
      else if (id < 590) tr_tiles_store(tiles[j], w2T, F_, ((id - 320) / 10) * 64, ((id - 320) % 10) * 64);
    }
  }
}

constexpr int UC_STRIDE = 4384;
constexpr int UC_OFF = 0;
constexpr int KROW = 80;
constexpr int KH_OFF = 8 * UC_STRIDE;
constexpr int KL_OFF = KH_OFF + 64 * KROW + 16;
constexpr int KZ_OFF = KH_OFF + 64 * KROW;
constexpr int PR_OFF = KL_OFF + 64 * KROW;
constexpr int OS_OFF = PR_OFF + 128 * 16;
constexpr int CONV_LDS = OS_OFF + 4 * 48 * 16 * 4;

__device__ __forceinline__ float thi_of(int i) {
  int t;
  if (i < 8) t = i;
  else if (i < 16) t = 128 + (i - 8);
  else if (i < 24) t = 256 + 128 * ((i - 16) >> 2) + 2 * ((i - 16) & 3);
  else if (i < 32) t = 512 + 128 * ((i - 24) >> 1) + 4 * ((i - 24) & 1);
  else if (i < 40) t = 1024 + 128 * (i - 32);
  else t = 2048;
  return (float)t;
}

__device__ void conv_role(int d, const unsigned short* __restrict__ uT, const unsigned short* __restrict__ logAr,
                          const unsigned short* __restrict__ Aim, const unsigned short* __restrict__ Cre,
                          const unsigned short* __restrict__ Cim, const unsigned short* __restrict__ logdt,
                          const unsigned short* __restrict__ Dsk, unsigned short* __restrict__ aT, char* smem) {
  const int tid = threadIdx.x, lane = tid & 63, wid = tid >> 6;

  const u32x4 uvec = *reinterpret_cast<const u32x4*>(uT + (size_t)d * L_ + 8 * tid);

  const float dt = rbf(expf(bf2f(logdt[d])));
  if (tid < NS_) {
    float are = -rbf(expf(bf2f(logAr[d * NS_ + tid])));
    float aim = bf2f(Aim[d * NS_ + tid]);
    float lr = are * dt, li = aim * dt;
    float cr = bf2f(Cre[d * NS_ + tid]) * dt, ci = bf2f(Cim[d * NS_ + tid]) * dt;
    float mag = sqrtf(cr * cr + ci * ci);
    f32x4 pp;
    pp[0] = lr * 1.4426950408889634f;
    pp[1] = li * 0.15915494309189535f;
    pp[2] = log2f(mag);
    pp[3] = atan2f(ci, cr) * 0.15915494309189535f;
    *reinterpret_cast<f32x4*>(smem + PR_OFF + tid * 16) = pp;
  }
  if (tid == 0) { u32x4 z = {0, 0, 0, 0}; *reinterpret_cast<u32x4*>(smem + KZ_OFF) = z; }
  if (tid < 64) {
    u32x4 z = {0, 0, 0, 0};
    *reinterpret_cast<u32x4*>(smem + UC_OFF + (tid >> 3) * UC_STRIDE + (tid & 7) * 16) = z;
  }
  *reinterpret_cast<u32x4*>(smem + UC_OFF + 128 + tid * 16) = uvec;
  __syncthreads();

  {
    u32x4 v1 = {0, 0, 0, 0};
    if (tid < 255) v1 = *reinterpret_cast<const u32x4*>(smem + UC_OFF + 128 + (tid + 1) * 16);
    unsigned w[8] = {uvec[0], uvec[1], uvec[2], uvec[3], v1[0], v1[1], v1[2], v1[3]};
#pragma unroll
    for (int e = 1; e < 8; ++e) {
      u32x4 o;
#pragma unroll
      for (int j = 0; j < 4; ++j) {
        int base = j + (e >> 1);
        if (e & 1) o[j] = (w[base] >> 16) | (w[base + 1] << 16);
        else o[j] = w[base];
      }
      *reinterpret_cast<u32x4*>(smem + UC_OFF + e * UC_STRIDE + 128 + tid * 16) = o;
    }
    if (tid == 0) {
#pragma unroll
      for (int e = 1; e < 8; ++e)
#pragma unroll
        for (int k = 0; k < e; ++k)
          *reinterpret_cast<unsigned short*>(smem + UC_OFF + e * UC_STRIDE + 2 * (64 - e + k)) =
              (unsigned short)(w[k >> 1] >> (16 * (k & 1)));
    }
  }

  {
    const int x = lane & 15, kk = lane >> 4;
    float th[3];
#pragma unroll
    for (int T = 0; T < 3; ++T) th[T] = thi_of(16 * T + x);
    const float tlo = (float)(8 * x);
    f32x4 acc[3];
#pragma unroll
    for (int T = 0; T < 3; ++T) acc[T] = (f32x4){0.f, 0.f, 0.f, 0.f};
#pragma unroll 2
    for (int g = 0; g < 8; ++g) {
      int n = 32 * wid + 4 * g + kk;
      f32x4 pp = *reinterpret_cast<const f32x4*>(smem + PR_OFF + n * 16);
      float qe = __builtin_amdgcn_exp2f(pp[0] * tlo);
      float qa = pp[1] * tlo;
      float qr = qe * __builtin_amdgcn_cosf(qa), qi = qe * __builtin_amdgcn_sinf(qa);
#pragma unroll
      for (int T = 0; T < 3; ++T) {
        float pe = __builtin_amdgcn_exp2f(pp[0] * th[T] + pp[2]);
        float pa = pp[1] * th[T] + pp[3];
        float pr = pe * __builtin_amdgcn_cosf(pa), pi = pe * __builtin_amdgcn_sinf(pa);
        acc[T] = __builtin_amdgcn_mfma_f32_16x16x4f32(qr, pr, acc[T], 0, 0, 0);
        acc[T] = __builtin_amdgcn_mfma_f32_16x16x4f32(-qi, pi, acc[T], 0, 0, 0);
      }
    }
    float* os = reinterpret_cast<float*>(smem + OS_OFF) + wid * 768;
#pragma unroll
    for (int T = 0; T < 3; ++T) *reinterpret_cast<f32x4*>(os + (16 * T + x) * 16 + 4 * kk) = acc[T];
  }
  __syncthreads();

  {
    unsigned short kh[8], kl[8];
#pragma unroll
    for (int k = 0; k < 8; ++k) {
      int j = 8 * tid + k;
      int rr = (int)rbf((float)j);
      int i;
      if (rr < 256) i = (rr & 7) + 8 * (rr >> 7);
      else if (rr < 512) i = 16 + 4 * ((rr - 256) >> 7) + ((rr & 7) >> 1);
      else if (rr < 1024) i = 24 + 2 * ((rr - 512) >> 7) + ((rr & 7) >> 2);
      else if (rr < 2048) i = 32 + ((rr - 1024) >> 7);
      else i = 40;
      int jj = (rr >> 3) & 15;
      const float* osp = reinterpret_cast<const float*>(smem + OS_OFF) + i * 16 + jj;
      float kv = 2.0f * ((osp[0] + osp[768]) + (osp[1536] + osp[2304]));
      unsigned short hi = f2bf(kv);
      kh[7 - k] = hi;
      kl[7 - k] = f2bf(kv - bf2f(hi));
    }
    u32x4 vh, vl;
#pragma unroll
    for (int j = 0; j < 4; ++j) {
      vh[j] = kh[2 * j] | ((unsigned)kh[2 * j + 1] << 16);
      vl[j] = kl[2 * j] | ((unsigned)kl[2 * j + 1] << 16);
    }
    *reinterpret_cast<u32x4*>(smem + KH_OFF + (tid >> 2) * KROW + (tid & 3) * 16) = vh;
    *reinterpret_cast<u32x4*>(smem + KL_OFF + (tid >> 2) * KROW + (tid & 3) * 16) = vl;
  }
  __syncthreads();

  f32x16 y0, y1;
#pragma unroll
  for (int i = 0; i < 16; ++i) { y0[i] = 0.f; y1[i] = 0.f; }
  {
    const int r = lane & 31, h = lane >> 5;
    const int e = (r + 1) & 7;
    const char* ubase = smem + UC_OFF + e * UC_STRIDE + 2 * (r - 7 - e + 64 - 8 * h);
    for (int dl = wid; dl < 64; dl += 4) {
      bf16x8 a0 = *reinterpret_cast<const bf16x8*>(ubase + 64 * dl);
      bf16x8 a1 = *reinterpret_cast<const bf16x8*>(ubase + 64 * dl - 32);
      {
        int m = 32 + r - dl;
        int off = (m >= 0) ? (m * KROW + 16 * h) : (KZ_OFF - KH_OFF);
        int off1 = (m >= 0) ? (off + 32) : off;
        bf16x8 bh0 = *reinterpret_cast<const bf16x8*>(smem + KH_OFF + off);
        bf16x8 bh1 = *reinterpret_cast<const bf16x8*>(smem + KH_OFF + off1);
        bf16x8 bl0 = *reinterpret_cast<const bf16x8*>(smem + ((m >= 0) ? KL_OFF : KH_OFF) + off);
        bf16x8 bl1 = *reinterpret_cast<const bf16x8*>(smem + ((m >= 0) ? KL_OFF : KH_OFF) + off1);
        y1 = __builtin_amdgcn_mfma_f32_32x32x16_bf16(a0, bh0, y1, 0, 0, 0);
        y1 = __builtin_amdgcn_mfma_f32_32x32x16_bf16(a1, bh1, y1, 0, 0, 0);
        y1 = __builtin_amdgcn_mfma_f32_32x32x16_bf16(a0, bl0, y1, 0, 0, 0);
        y1 = __builtin_amdgcn_mfma_f32_32x32x16_bf16(a1, bl1, y1, 0, 0, 0);
      }
      if (dl < 32) {
        int m = r - dl;
        int off = (m >= 0) ? (m * KROW + 16 * h) : (KZ_OFF - KH_OFF);
        int off1 = (m >= 0) ? (off + 32) : off;
        bf16x8 bh0 = *reinterpret_cast<const bf16x8*>(smem + KH_OFF + off);
        bf16x8 bh1 = *reinterpret_cast<const bf16x8*>(smem + KH_OFF + off1);
        bf16x8 bl0 = *reinterpret_cast<const bf16x8*>(smem + ((m >= 0) ? KL_OFF : KH_OFF) + off);
        bf16x8 bl1 = *reinterpret_cast<const bf16x8*>(smem + ((m >= 0) ? KL_OFF : KH_OFF) + off1);
        y0 = __builtin_amdgcn_mfma_f32_32x32x16_bf16(a0, bh0, y0, 0, 0, 0);
        y0 = __builtin_amdgcn_mfma_f32_32x32x16_bf16(a1, bh1, y0, 0, 0, 0);
        y0 = __builtin_amdgcn_mfma_f32_32x32x16_bf16(a0, bl0, y0, 0, 0, 0);
        y0 = __builtin_amdgcn_mfma_f32_32x32x16_bf16(a1, bl1, y0, 0, 0, 0);
      }
    }
  }
  __syncthreads();
  {
    const int r = lane & 31, h = lane >> 5;
    float* slab = reinterpret_cast<float*>(smem + wid * 8704);
#pragma unroll
    for (int g = 0; g < 4; ++g) {
      f32x4 v0 = {y0[4 * g], y0[4 * g + 1], y0[4 * g + 2], y0[4 * g + 3]};
      f32x4 v1 = {y1[4 * g], y1[4 * g + 1], y1[4 * g + 2], y1[4 * g + 3]};
      int b = 8 * g + 4 * h;
      *reinterpret_cast<f32x4*>(slab + 32 * r + b) = v0;
      *reinterpret_cast<f32x4*>(slab + 32 * (32 + r) + b) = v1;
    }
  }
  __syncthreads();
  {
    const float dsk = bf2f(Dsk[d]);
    const float* s = reinterpret_cast<const float*>(smem);
    f32x4 ys[2];
#pragma unroll
    for (int hh = 0; hh < 2; ++hh) {
      f32x4 s0 = *reinterpret_cast<const f32x4*>(s + 8 * tid + 4 * hh);
      f32x4 s1 = *reinterpret_cast<const f32x4*>(s + 2176 + 8 * tid + 4 * hh);
      f32x4 s2 = *reinterpret_cast<const f32x4*>(s + 4352 + 8 * tid + 4 * hh);
      f32x4 s3 = *reinterpret_cast<const f32x4*>(s + 6528 + 8 * tid + 4 * hh);
      ys[hh] = (s0 + s1) + (s2 + s3);
    }
    unsigned short o[8];
#pragma unroll
    for (int k = 0; k < 8; ++k) {
      float yb = rbf(ys[k >> 2][k & 3]);
      float uu = bf2f((unsigned short)(uvec[k >> 1] >> (16 * (k & 1))));
      float sk = rbf(uu * dsk);
      o[k] = f2bf(yb + sk);
    }
    u32x4 v;
#pragma unroll
    for (int j = 0; j < 4; ++j) v[j] = o[2 * j] | ((unsigned)o[2 * j + 1] << 16);
    *reinterpret_cast<u32x4*>(aT + (size_t)d * L_ + 8 * tid) = v;
  }
}

__global__ __launch_bounds__(256) void k2_conv(const unsigned short* __restrict__ uT, const unsigned short* __restrict__ logAr,
                                               const unsigned short* __restrict__ Aim, const unsigned short* __restrict__ Cre,
                                               const unsigned short* __restrict__ Cim, const unsigned short* __restrict__ logdt,
                                               const unsigned short* __restrict__ Dsk, unsigned short* __restrict__ aT) {
  extern __shared__ __attribute__((aligned(16))) char smem[];
  conv_role(blockIdx.x, uT, logAr, Aim, Cre, Cim, logdt, Dsk, aT, smem);
}

constexpr int G2_RS = 84;
__global__ __launch_bounds__(256, 1) void k3_gemm2(const unsigned short* __restrict__ hbuf, const unsigned short* __restrict__ w2T,
                                                   const unsigned short* __restrict__ aT, const float* __restrict__ gbuf,
                                                   unsigned short* __restrict__ out) {
  __shared__ float red[4][64][G2_RS];
  const int tid = threadIdx.x, lane = tid & 63, wid = tid >> 6;
  int t = (blockIdx.x & 7) * 32 + (blockIdx.x >> 3);
  const int tm = t >> 3, tn = t & 7;
  const int m0 = tm * 64, n0 = tn * 80;
  const int fr = lane & 15, fq = lane >> 4;
  const int c0 = (wid * 27) >> 2, c1 = ((wid + 1) * 27) >> 2;

  const unsigned short* ap = hbuf + (size_t)(m0 + fr) * F_ + 8 * fq;
  const unsigned short* bp = w2T + (size_t)(n0 + fr) * F_ + 8 * fq;

  f32x4 acc[4][5];
#pragma unroll
  for (int a = 0; a < 4; ++a)
#pragma unroll
    for (int c = 0; c < 5; ++c) acc[a][c] = (f32x4){0.f, 0.f, 0.f, 0.f};

  bf16x8 A0[4][2], B0[5][2], A1[4][2], B1[5][2];
#define G2_LOAD(AA, BB, c)                                                                             \
  {                                                                                                    \
    _Pragma("unroll") for (int a = 0; a < 4; ++a) {                                                    \
      AA[a][0] = *reinterpret_cast<const bf16x8*>(ap + (size_t)a * 16 * F_ + (c) * 64);                \
      AA[a][1] = *reinterpret_cast<const bf16x8*>(ap + (size_t)a * 16 * F_ + (c) * 64 + 32);            \
    }                                                                                                  \
    _Pragma("unroll") for (int a = 0; a < 5; ++a) {                                                    \
      BB[a][0] = *reinterpret_cast<const bf16x8*>(bp + (size_t)a * 16 * F_ + (c) * 64);                \
      BB[a][1] = *reinterpret_cast<const bf16x8*>(bp + (size_t)a * 16 * F_ + (c) * 64 + 32);            \
    }                                                                                                  \
  }
#define G2_COMP(AA, BB)                                                                                \
  {                                                                                                    \
    _Pragma("unroll") for (int s = 0; s < 2; ++s)                                                      \
      _Pragma("unroll") for (int a = 0; a < 4; ++a)                                                    \
        _Pragma("unroll") for (int c = 0; c < 5; ++c)                                                  \
          acc[a][c] = __builtin_amdgcn_mfma_f32_16x16x32_bf16(AA[a][s], BB[c][s], acc[a][c], 0, 0, 0); \
  }
  G2_LOAD(A0, B0, c0);
  for (int c = c0; c < c1; c += 2) {
    if (c + 1 < c1) G2_LOAD(A1, B1, c + 1);
    G2_COMP(A0, B0);
    if (c + 1 >= c1) break;
    if (c + 2 < c1) G2_LOAD(A0, B0, c + 2);
    G2_COMP(A1, B1);
  }
#undef G2_LOAD
#undef G2_COMP

#pragma unroll
  for (int a = 0; a < 4; ++a)
#pragma unroll
    for (int c = 0; c < 5; ++c)
#pragma unroll
      for (int i = 0; i < 4; ++i) red[wid][16 * a + 4 * fq + i][16 * c + fr] = acc[a][c][i];
  __syncthreads();

#pragma unroll
  for (int j = 0; j < 2; ++j) {
    const int col = lane + 64 * j;
    if (col < 80) {
      const int row0 = m0 + 16 * wid;
      u32x4 av0 = *reinterpret_cast<const u32x4*>(aT + (size_t)(n0 + col) * L_ + row0);
      u32x4 av1 = *reinterpret_cast<const u32x4*>(aT + (size_t)(n0 + col) * L_ + row0 + 8);
#pragma unroll
      for (int i = 0; i < 16; ++i) {
        int rr = 16 * wid + i;
        float bv = rbf((red[0][rr][col] + red[1][rr][col]) + (red[2][rr][col] + red[3][rr][col]));
        unsigned aw = (i < 8) ? av0[(i >> 1) & 3] : av1[(i >> 1) & 3];
        float a = bf2f((unsigned short)((aw >> (16 * (i & 1))) & 0xffff));
        float g = gbuf[row0 + i];
        float t1 = rbf(g * a);
        float t2 = rbf(1.0f - g);
        float t3 = rbf(t2 * bv);
        out[(size_t)(row0 + i) * D_ + n0 + col] = f2bf(t1 + t3);
      }
    }
  }
}

extern "C" void kernel_launch(void* const* d_in, const int* in_sizes, int n_in, void* d_out, int out_size, void* d_ws,
                              size_t ws_size, hipStream_t stream) {
  const unsigned short* u = (const unsigned short*)d_in[0];
  const unsigned short* logAr = (const unsigned short*)d_in[1];
  const unsigned short* Aim = (const unsigned short*)d_in[2];
  const unsigned short* Cre = (const unsigned short*)d_in[3];
  const unsigned short* Cim = (const unsigned short*)d_in[4];
  const unsigned short* logdt = (const unsigned short*)d_in[5];
  const unsigned short* Dsk = (const unsigned short*)d_in[6];
  const unsigned short* w1 = (const unsigned short*)d_in[8];
  const unsigned short* w2 = (const unsigned short*)d_in[9];
  const unsigned short* gw = (const unsigned short*)d_in[10];
  unsigned short* out = (unsigned short*)d_out;

  char* ws = (char*)d_ws;
  unsigned short* hbuf = (unsigned short*)ws;
  unsigned short* aT = (unsigned short*)(ws + (size_t)L_ * F_ * 2);
  unsigned short* uT = (unsigned short*)(ws + (size_t)L_ * F_ * 2 + (size_t)D_ * L_ * 2);
  unsigned short* w2T = (unsigned short*)(ws + (size_t)L_ * F_ * 2 + 2 * (size_t)D_ * L_ * 2);
  float* gbuf = (float*)(ws + (size_t)L_ * F_ * 2 + 2 * (size_t)D_ * L_ * 2 + (size_t)D_ * F_ * 2);

  static bool attr_set = false;
  if (!attr_set) {
    hipFuncSetAttribute((const void*)k1_gemm1, hipFuncAttributeMaxDynamicSharedMemorySize, G1_LDS);
    attr_set = true;
  }
  hipLaunchKernelGGL(k1_gemm1, dim3(K1_GRID), dim3(256), G1_LDS, stream, u, w1, w2, gw, hbuf, uT, w2T, gbuf);
  hipLaunchKernelGGL(k2_conv, dim3(D_), dim3(256), CONV_LDS, stream, uT, logAr, Aim, Cre, Cim, logdt, Dsk, aT);
  hipLaunchKernelGGL(k3_gemm2, dim3(256), dim3(256), 0, stream, hbuf, w2T, aT, gbuf, out);
}
```

```cpp
#include <hip/hip_runtime.h>
#include <hip/hip_bf16.h>
#include <stdint.h>

typedef __attribute__((ext_vector_type(8))) short bf16x8;
typedef __attribute__((ext_vector_type(4))) short bf16x4;
typedef __attribute__((ext_vector_type(16))) float f32x16;
typedef __attribute__((ext_vector_type(4))) float f32x4;
typedef __attribute__((ext_vector_type(4))) unsigned int u32x4;
typedef __attribute__((ext_vector_type(2))) unsigned int u32x2;

#define LDS_AS __attribute__((address_space(3)))
#define RAW_BARRIER() do { asm volatile("s_waitcnt lgkmcnt(0)" ::: "memory"); __builtin_amdgcn_s_barrier(); } while (0)

constexpr int L_ = 2048, D_ = 640, NS_ = 128, F_ = 1728;

__device__ __forceinline__ float bf2f(unsigned short v) { return __uint_as_float(((unsigned)v) << 16); }
__device__ __forceinline__ unsigned short f2bf(float f) {
  unsigned r;
  asm("v_cvt_pk_bf16_f32 %0, %1, %1" : "=v"(r) : "v"(f));
  return (unsigned short)(r & 0xffffu);
}
__device__ __forceinline__ float rbf(float f) { return bf2f(f2bf(f)); }

constexpr int G1_TM = L_ / 128, G1_TN = (F_ + 127) / 128;
constexpr int N_G1 = G1_TM * G1_TN;
constexpr int G1_STAGE = 32768, G1_LDS = 3 * G1_STAGE;
constexpr int K1_GRID = N_G1 + 32;

__device__ __forceinline__ void tr_tiles_load(const unsigned short* __restrict__ src, int ss, int r0, int c0,
                                              u32x4 (&v)[2]) {
  const int tid = threadIdx.x;
#pragma unroll
  for (int i = 0; i < 2; ++i) {
    int id = tid + 256 * i, row = id >> 3, ch = id & 7;
    v[i] = *reinterpret_cast<const u32x4*>(src + (size_t)(r0 + row) * ss + c0 + ch * 8);
  }
}
__device__ __forceinline__ void tr_tiles_put(unsigned short (*tile)[72], const u32x4 (&v)[2]) {
  const int tid = threadIdx.x;
#pragma unroll
  for (int i = 0; i < 2; ++i) {
    int id = tid + 256 * i, row = id >> 3, ch = id & 7;
    *reinterpret_cast<u32x4*>(&tile[row][ch * 8]) = v[i];
  }
}
__device__ __forceinline__ void tr_tiles_store(unsigned short (*tile)[72], unsigned short* __restrict__ dst, int ds,
                                               int r0, int c0) {
  const int tid = threadIdx.x;
#pragma unroll
  for (int i = 0; i < 2; ++i) {
    int id = tid + 256 * i, col = id >> 3, ch = id & 7;
    unsigned short o[8];
#pragma unroll
    for (int k = 0; k < 8; ++k) o[k] = tile[ch * 8 + k][col];
    u32x4 w;
#pragma unroll
    for (int j = 0; j < 4; ++j) w[j] = o[2 * j] | ((unsigned)o[2 * j + 1] << 16);
    __builtin_nontemporal_store(w, reinterpret_cast<u32x4*>(dst + (size_t)(c0 + col) * ds + r0 + ch * 8));
  }
}

__global__ __launch_bounds__(256, 1) void k1_gemm1(const unsigned short* __restrict__ x, const unsigned short* __restrict__ w1,
                                                   const unsigned short* __restrict__ w2, const unsigned short* __restrict__ gw,
                                                   unsigned short* __restrict__ hbuf, unsigned short* __restrict__ uT,
                                                   unsigned short* __restrict__ w2T, float* __restrict__ gbuf) {
  extern __shared__ __attribute__((aligned(16))) char smem[];
  const int tid = threadIdx.x, lane = tid & 63, wid = tid >> 6;
  const int b = blockIdx.x;
  if (b >= N_G1) {
    const int r0 = (b - N_G1) * 64;
    u32x4 v[10][2];
#pragma unroll
    for (int t = 0; t < 10; ++t) tr_tiles_load(x, D_, r0, t * 64, v[t]);
    float s[2] = {0.f, 0.f};
#pragma unroll
    for (int t = 0; t < 10; ++t) {
      u32x4 g = *reinterpret_cast<const u32x4*>(gw + t * 64 + (tid & 7) * 8);
#pragma unroll
      for (int i = 0; i < 2; ++i)
#pragma unroll
        for (int j = 0; j < 4; ++j) {
          s[i] += rbf(__uint_as_float(v[t][i][j] << 16) * __uint_as_float(g[j] << 16));
          s[i] += rbf(__uint_as_float(v[t][i][j] & 0xffff0000u) * __uint_as_float(g[j] & 0xffff0000u));
        }
    }
#pragma unroll
    for (int i = 0; i < 2; ++i) {
      s[i] += __shfl_xor(s[i], 1); s[i] += __shfl_xor(s[i], 2); s[i] += __shfl_xor(s[i], 4);
      if ((tid & 7) == 0) gbuf[r0 + (tid >> 3) + 32 * i] = rbf(1.0f / (1.0f + expf(-rbf(s[i]))));
    }
    return;
  }

  u32x4 tv[3][2];
#pragma unroll
  for (int j = 0; j < 3; ++j) {
    int id = b + j * N_G1;
    if (id < 320) tr_tiles_load(x, D_, (id / 10) * 64, (id % 10) * 64, tv[j]);
    else if (id < 590) tr_tiles_load(w2, D_, ((id - 320) / 10) * 64, ((id - 320) % 10) * 64, tv[j]);
  }

  int t = (b & 7) * (N_G1 / 8) + (b >> 3);
  const int tm = t / G1_TN, tn = t % G1_TN;
  const int m0 = tm * 128, n0 = tn * 128;
  const int wm = wid >> 1, wn = wid & 1, r = lane & 31, h = lane >> 5;

  const unsigned short* asrc[4];
  const unsigned short* bsrc[4];
#pragma unroll
  for (int i = 0; i < 4; ++i) {
    int row = (i * 4 + wid) * 8 + (lane >> 3);
    int c = (lane & 7) ^ (row & 7);
    asrc[i] = x + (size_t)(m0 + row) * D_ + c * 8;
    int k = (i * 4 + wid) * 4 + (lane >> 4);
    int cb = (lane & 15) ^ ((k & 3) << 1);
    int col = n0 + cb * 8;
    if (col > F_ - 8) col = F_ - 8;
    bsrc[i] = w1 + (size_t)k * F_ + col;
  }
  auto stage = [&](int kt, int buf) {
    char* sa = smem + buf * G1_STAGE;
    char* sb = sa + 16384;
#pragma unroll
    for (int i = 0; i < 4; ++i)
      __builtin_amdgcn_global_load_lds((const unsigned*)(asrc[i] + kt * 64), (LDS_AS unsigned*)(sa + (i * 4 + wid) * 1024), 16, 0, 0);
#pragma unroll
    for (int i = 0; i < 4; ++i)
      __builtin_amdgcn_global_load_lds((const unsigned*)(bsrc[i] + (size_t)kt * 64 * F_), (LDS_AS unsigned*)(sb + (i * 4 + wid) * 1024), 16, 0, 0);
  };

  f32x16 acc[2][2];
#pragma unroll
  for (int a = 0; a < 2; ++a)
#pragma unroll
    for (int c = 0; c < 2; ++c)
#pragma unroll
      for (int i = 0; i < 16; ++i) acc[a][c][i] = 0.f;

  const int q = (lane & 15) >> 2, p = lane & 3, blk = (lane >> 4) & 1;
  auto compute = [&](int buf) {
    const char* sa = smem + buf * G1_STAGE;
    const char* sb = sa + 16384;
#pragma unroll
    for (int s = 0; s < 4; ++s) {
      bf16x8 af[2], bfr[2];
#pragma unroll
      for (int mt = 0; mt < 2; ++mt) {
        int row = wm * 64 + mt * 32 + r;
        af[mt] = *reinterpret_cast<const bf16x8*>(sa + row * 128 + (((2 * s + h) ^ (row & 7)) << 4));
      }
#pragma unroll
      for (int nt = 0; nt < 2; ++nt)
#pragma unroll
        for (int tt = 0; tt < 2; ++tt) {
          int k = 16 * s + 8 * h + 4 * tt + q;
          int chunk = wn * 8 + nt * 4 + 2 * blk + (p >> 1);
          const char* ad = sb + k * 256 + ((chunk ^ (q << 1)) << 4) + (p & 1) * 8;
          bf16x4 v = __builtin_amdgcn_ds_read_tr16_b64_v4i16((LDS_AS bf16x4*)(ad));
          bfr[nt][4 * tt + 0] = v[0]; bfr[nt][4 * tt + 1] = v[1]; bfr[nt][4 * tt + 2] = v[2]; bfr[nt][4 * tt + 3] = v[3];
        }
#pragma unroll
      for (int mt = 0; mt < 2; ++mt)
#pragma unroll
        for (int nt = 0; nt < 2; ++nt)
          acc[mt][nt] = __builtin_amdgcn_mfma_f32_32x32x16_bf16(af[mt], bfr[nt], acc[mt][nt], 0, 0, 0);
    }
  };

  constexpr int NK = D_ / 64;
  stage(0, 0);
  stage(1, 1);
  for (int kt = 0; kt < NK - 1; ++kt) {
    asm volatile("s_waitcnt vmcnt(8)" ::: "memory");
    RAW_BARRIER();
    if (kt + 2 < NK) stage(kt + 2, (kt + 2) % 3);
    compute(kt % 3);
  }
  asm volatile("s_waitcnt vmcnt(0)" ::: "memory");
  RAW_BARRIER();
  compute((NK - 1) % 3);

  RAW_BARRIER();
  {
    unsigned short* ht = reinterpret_cast<unsigned short*>(smem);
#pragma unroll
    for (int nt = 0; nt < 2; ++nt)
#pragma unroll
      for (int mt = 0; mt < 2; ++mt)
#pragma unroll
        for (int i = 0; i < 16; ++i) {
          int row = wm * 64 + mt * 32 + (i & 3) + 8 * (i >> 2) + 4 * h;
          float hv = rbf(acc[mt][nt][i]);
          float sv = hv * __builtin_amdgcn_rcpf(1.0f + __expf(-hv));
          ht[row * 136 + wn * 64 + nt * 32 + r] = f2bf(sv);
        }
    typedef unsigned short tile_t[64][72];
    tile_t* tiles = reinterpret_cast<tile_t*>(smem + 36864);
#pragma unroll
    for (int j = 0; j < 3; ++j)
      if (b + j * N_G1 < 590) tr_tiles_put(tiles[j], tv[j]);
    __syncthreads();
#pragma unroll
    for (int i = 0; i < 8; ++i) {
      int id = tid + 256 * i, row = id >> 4, ch = id & 15;
      if (n0 + ch * 8 < F_)
        __builtin_nontemporal_store(*reinterpret_cast<const u32x4*>(ht + row * 136 + ch * 8), reinterpret_cast<u32x4*>(hbuf + (size_t)(m0 + row) * F_ + n0 + ch * 8));
    }
#pragma unroll
    for (int j = 0; j < 3; ++j) {
      int id = b + j * N_G1;
      if (id < 320) tr_tiles_store(tiles[j], uT, L_, (id / 10) * 64, (id % 10) * 64);
      else if (id < 590) tr_tiles_store(tiles[j], w2T, F_, ((id - 320) / 10) * 64, ((id - 320) % 10) * 64);
    }
  }
}

constexpr int UC_STRIDE = 4384;
constexpr int UC_OFF = 0;
constexpr int KROW = 80;
constexpr int KH_OFF = 8 * UC_STRIDE;
constexpr int KL_OFF = KH_OFF + 64 * KROW + 16;
constexpr int KZ_OFF = KH_OFF + 64 * KROW;
constexpr int PR_OFF = KL_OFF + 64 * KROW;
constexpr int OS_OFF = PR_OFF + 128 * 16;
constexpr int CONV_LDS = OS_OFF + 4 * 48 * 16 * 4;

__device__ __forceinline__ float thi_of(int i) {
  int t;
  if (i < 8) t = i;
  else if (i < 16) t = 128 + (i - 8);
  else if (i < 24) t = 256 + 128 * ((i - 16) >> 2) + 2 * ((i - 16) & 3);
  else if (i < 32) t = 512 + 128 * ((i - 24) >> 1) + 4 * ((i - 24) & 1);
  else if (i < 40) t = 1024 + 128 * (i - 32);
  else t = 2048;
  return (float)t;
}

__device__ void conv_role(int d, const unsigned short* __restrict__ uT, const unsigned short* __restrict__ logAr,
                          const unsigned short* __restrict__ Aim, const unsigned short* __restrict__ Cre,
                          const unsigned short* __restrict__ Cim, const unsigned short* __restrict__ logdt,
                          const unsigned short* __restrict__ Dsk, unsigned short* __restrict__ aT, char* smem) {
  const int tid = threadIdx.x, lane = tid & 63, wid = tid >> 6;

  const u32x4 uvec = *reinterpret_cast<const u32x4*>(uT + (size_t)d * L_ + 8 * tid);

  const float dt = rbf(expf(bf2f(logdt[d])));
  if (tid < NS_) {
    float are = -rbf(expf(bf2f(logAr[d * NS_ + tid])));
    float aim = bf2f(Aim[d * NS_ + tid]);
    float lr = are * dt, li = aim * dt;
    float cr = bf2f(Cre[d * NS_ + tid]) * dt, ci = bf2f(Cim[d * NS_ + tid]) * dt;
    float mag = sqrtf(cr * cr + ci * ci);
    f32x4 pp;
    pp[0] = lr * 1.4426950408889634f;
    pp[1] = li * 0.15915494309189535f;
    pp[2] = log2f(mag);
    pp[3] = atan2f(ci, cr) * 0.15915494309189535f;
    *reinterpret_cast<f32x4*>(smem + PR_OFF + tid * 16) = pp;
  }
  if (tid == 0) { u32x4 z = {0, 0, 0, 0}; *reinterpret_cast<u32x4*>(smem + KZ_OFF) = z; }
  if (tid < 64) {
    u32x4 z = {0, 0, 0, 0};
    *reinterpret_cast<u32x4*>(smem + UC_OFF + (tid >> 3) * UC_STRIDE + (tid & 7) * 16) = z;
  }
  *reinterpret_cast<u32x4*>(smem + UC_OFF + 128 + tid * 16) = uvec;
  __syncthreads();

  {
    u32x4 v1 = {0, 0, 0, 0};
    if (tid < 255) v1 = *reinterpret_cast<const u32x4*>(smem + UC_OFF + 128 + (tid + 1) * 16);
    unsigned w[8] = {uvec[0], uvec[1], uvec[2], uvec[3], v1[0], v1[1], v1[2], v1[3]};
#pragma unroll
    for (int e = 1; e < 8; ++e) {
      u32x4 o;
#pragma unroll
      for (int j = 0; j < 4; ++j) {
        int base = j + (e >> 1);
        if (e & 1) o[j] = (w[base] >> 16) | (w[base + 1] << 16);
        else o[j] = w[base];
      }
      *reinterpret_cast<u32x4*>(smem + UC_OFF + e * UC_STRIDE + 128 + tid * 16) = o;
    }
    if (tid == 0) {
#pragma unroll
      for (int e = 1; e < 8; ++e)
#pragma unroll
        for (int k = 0; k < e; ++k)
          *reinterpret_cast<unsigned short*>(smem + UC_OFF + e * UC_STRIDE + 2 * (64 - e + k)) =
              (unsigned short)(w[k >> 1] >> (16 * (k & 1)));
    }
  }

  {
    const int x = lane & 15, kk = lane >> 4;
    float th[3];
#pragma unroll
    for (int T = 0; T < 3; ++T) th[T] = thi_of(16 * T + x);
    const float tlo = (float)(8 * x);
    f32x4 acc[3];
#pragma unroll
    for (int T = 0; T < 3; ++T) acc[T] = (f32x4){0.f, 0.f, 0.f, 0.f};
#pragma unroll 2
    for (int g = 0; g < 8; ++g) {
      int n = 32 * wid + 4 * g + kk;
      f32x4 pp = *reinterpret_cast<const f32x4*>(smem + PR_OFF + n * 16);
      float qe = __builtin_amdgcn_exp2f(pp[0] * tlo);
      float qa = pp[1] * tlo;
      float qr = qe * __builtin_amdgcn_cosf(qa), qi = qe * __builtin_amdgcn_sinf(qa);
#pragma unroll
      for (int T = 0; T < 3; ++T) {
        float pe = __builtin_amdgcn_exp2f(pp[0] * th[T] + pp[2]);
        float pa = pp[1] * th[T] + pp[3];
        float pr = pe * __builtin_amdgcn_cosf(pa), pi = pe * __builtin_amdgcn_sinf(pa);
        acc[T] = __builtin_amdgcn_mfma_f32_16x16x4f32(qr, pr, acc[T], 0, 0, 0);
        acc[T] = __builtin_amdgcn_mfma_f32_16x16x4f32(-qi, pi, acc[T], 0, 0, 0);
      }
    }
    float* os = reinterpret_cast<float*>(smem + OS_OFF) + wid * 768;
#pragma unroll
    for (int T = 0; T < 3; ++T) *reinterpret_cast<f32x4*>(os + (16 * T + x) * 16 + 4 * kk) = acc[T];
  }
  __syncthreads();

  {
    unsigned short kh[8], kl[8];
#pragma unroll
    for (int k = 0; k < 8; ++k) {
      int j = 8 * tid + k;
      int rr = (int)rbf((float)j);
      int i;
      if (rr < 256) i = (rr & 7) + 8 * (rr >> 7);
      else if (rr < 512) i = 16 + 4 * ((rr - 256) >> 7) + ((rr & 7) >> 1);
      else if (rr < 1024) i = 24 + 2 * ((rr - 512) >> 7) + ((rr & 7) >> 2);
      else if (rr < 2048) i = 32 + ((rr - 1024) >> 7);
      else i = 40;
      int jj = (rr >> 3) & 15;
      const float* osp = reinterpret_cast<const float*>(smem + OS_OFF) + i * 16 + jj;
      float kv = 2.0f * ((osp[0] + osp[768]) + (osp[1536] + osp[2304]));
      unsigned short hi = f2bf(kv);
      kh[7 - k] = hi;
      kl[7 - k] = f2bf(kv - bf2f(hi));
    }
    u32x4 vh, vl;
#pragma unroll
    for (int j = 0; j < 4; ++j) {
      vh[j] = kh[2 * j] | ((unsigned)kh[2 * j + 1] << 16);
      vl[j] = kl[2 * j] | ((unsigned)kl[2 * j + 1] << 16);
    }
    *reinterpret_cast<u32x4*>(smem + KH_OFF + (tid >> 2) * KROW + (tid & 3) * 16) = vh;
    *reinterpret_cast<u32x4*>(smem + KL_OFF + (tid >> 2) * KROW + (tid & 3) * 16) = vl;
  }
  __syncthreads();

  f32x16 y0, y1;
#pragma unroll
  for (int i = 0; i < 16; ++i) { y0[i] = 0.f; y1[i] = 0.f; }
  {
    const int r = lane & 31, h = lane >> 5;
    const int e = (r + 1) & 7;
    const char* ubase = smem + UC_OFF + e * UC_STRIDE + 2 * (r - 7 - e + 64 - 8 * h);
    for (int dl = wid; dl < 64; dl += 4) {
      bf16x8 a0 = *reinterpret_cast<const bf16x8*>(ubase + 64 * dl);
      bf16x8 a1 = *reinterpret_cast<const bf16x8*>(ubase + 64 * dl - 32);
      {
        int m = 32 + r - dl;
        int off = (m >= 0) ? (m * KROW + 16 * h) : (KZ_OFF - KH_OFF);
        int off1 = (m >= 0) ? (off + 32) : off;
        bf16x8 bh0 = *reinterpret_cast<const bf16x8*>(smem + KH_OFF + off);
        bf16x8 bh1 = *reinterpret_cast<const bf16x8*>(smem + KH_OFF + off1);
        bf16x8 bl0 = *reinterpret_cast<const bf16x8*>(smem + ((m >= 0) ? KL_OFF : KH_OFF) + off);
        bf16x8 bl1 = *reinterpret_cast<const bf16x8*>(smem + ((m >= 0) ? KL_OFF : KH_OFF) + off1);
        y1 = __builtin_amdgcn_mfma_f32_32x32x16_bf16(a0, bh0, y1, 0, 0, 0);
        y1 = __builtin_amdgcn_mfma_f32_32x32x16_bf16(a1, bh1, y1, 0, 0, 0);
        y1 = __builtin_amdgcn_mfma_f32_32x32x16_bf16(a0, bl0, y1, 0, 0, 0);
        y1 = __builtin_amdgcn_mfma_f32_32x32x16_bf16(a1, bl1, y1, 0, 0, 0);
      }
      if (dl < 32) {
        int m = r - dl;
        int off = (m >= 0) ? (m * KROW + 16 * h) : (KZ_OFF - KH_OFF);
        int off1 = (m >= 0) ? (off + 32) : off;
        bf16x8 bh0 = *reinterpret_cast<const bf16x8*>(smem + KH_OFF + off);
        bf16x8 bh1 = *reinterpret_cast<const bf16x8*>(smem + KH_OFF + off1);
        bf16x8 bl0 = *reinterpret_cast<const bf16x8*>(smem + ((m >= 0) ? KL_OFF : KH_OFF) + off);
        bf16x8 bl1 = *reinterpret_cast<const bf16x8*>(smem + ((m >= 0) ? KL_OFF : KH_OFF) + off1);
        y0 = __builtin_amdgcn_mfma_f32_32x32x16_bf16(a0, bh0, y0, 0, 0, 0);
        y0 = __builtin_amdgcn_mfma_f32_32x32x16_bf16(a1, bh1, y0, 0, 0, 0);
        y0 = __builtin_amdgcn_mfma_f32_32x32x16_bf16(a0, bl0, y0, 0, 0, 0);
        y0 = __builtin_amdgcn_mfma_f32_32x32x16_bf16(a1, bl1, y0, 0, 0, 0);
      }
    }
  }
  __syncthreads();
  {
    const int r = lane & 31, h = lane >> 5;
    float* slab = reinterpret_cast<float*>(smem + wid * 8704);
#pragma unroll
    for (int g = 0; g < 4; ++g) {
      f32x4 v0 = {y0[4 * g], y0[4 * g + 1], y0[4 * g + 2], y0[4 * g + 3]};
      f32x4 v1 = {y1[4 * g], y1[4 * g + 1], y1[4 * g + 2], y1[4 * g + 3]};
      int b = 8 * g + 4 * h;
      *reinterpret_cast<f32x4*>(slab + 32 * r + b) = v0;
      *reinterpret_cast<f32x4*>(slab + 32 * (32 + r) + b) = v1;
    }
  }
  __syncthreads();
  {
    const float dsk = bf2f(Dsk[d]);
    const float* s = reinterpret_cast<const float*>(smem);
    f32x4 ys[2];
#pragma unroll
    for (int hh = 0; hh < 2; ++hh) {
      f32x4 s0 = *reinterpret_cast<const f32x4*>(s + 8 * tid + 4 * hh);
      f32x4 s1 = *reinterpret_cast<const f32x4*>(s + 2176 + 8 * tid + 4 * hh);
      f32x4 s2 = *reinterpret_cast<const f32x4*>(s + 4352 + 8 * tid + 4 * hh);
      f32x4 s3 = *reinterpret_cast<const f32x4*>(s + 6528 + 8 * tid + 4 * hh);
      ys[hh] = (s0 + s1) + (s2 + s3);
    }
    unsigned short o[8];
#pragma unroll
    for (int k = 0; k < 8; ++k) {
      float yb = rbf(ys[k >> 2][k & 3]);
      float uu = bf2f((unsigned short)(uvec[k >> 1] >> (16 * (k & 1))));
      float sk = rbf(uu * dsk);
      o[k] = f2bf(yb + sk);
    }
    u32x4 v;
#pragma unroll
    for (int j = 0; j < 4; ++j) v[j] = o[2 * j] | ((unsigned)o[2 * j + 1] << 16);
    *reinterpret_cast<u32x4*>(aT + (size_t)d * L_ + 8 * tid) = v;
  }
}

constexpr int G2_RS = 84;
__device__ __forceinline__ void gemm2_role(int bid, const unsigned short* __restrict__ hbuf, const unsigned short* __restrict__ w2T,
                                           unsigned short* __restrict__ bbuf, char* smem) {
  float (*red)[64][G2_RS] = reinterpret_cast<float (*)[64][G2_RS]>(smem);
  const int tid = threadIdx.x, lane = tid & 63, wid = tid >> 6;
  int t = (bid & 7) * 32 + (bid >> 3);
  const int tm = t >> 3, tn = t & 7;
  const int m0 = tm * 64, n0 = tn * 80;
  const int fr = lane & 15, fq = lane >> 4;
  const int c0 = (wid * 27) >> 2, c1 = ((wid + 1) * 27) >> 2;

  const unsigned short* ap = hbuf + (size_t)(m0 + fr) * F_ + 8 * fq;
  const unsigned short* bp = w2T + (size_t)(n0 + fr) * F_ + 8 * fq;

  f32x4 acc[4][5];
#pragma unroll
  for (int a = 0; a < 4; ++a)
#pragma unroll
    for (int c = 0; c < 5; ++c) acc[a][c] = (f32x4){0.f, 0.f, 0.f, 0.f};

  bf16x8 A0[4][2], B0[5][2], A1[4][2], B1[5][2];
#define G2_LOAD(AA, BB, c)                                                                             \
  {                                                                                                    \
    _Pragma("unroll") for (int a = 0; a < 4; ++a) {                                                    \
      AA[a][0] = *reinterpret_cast<const bf16x8*>(ap + (size_t)a * 16 * F_ + (c) * 64);                \
      AA[a][1] = *reinterpret_cast<const bf16x8*>(ap + (size_t)a * 16 * F_ + (c) * 64 + 32);           \
    }                                                                                                  \
    _Pragma("unroll") for (int a = 0; a < 5; ++a) {                                                    \
      BB[a][0] = *reinterpret_cast<const bf16x8*>(bp + (size_t)a * 16 * F_ + (c) * 64);                \
      BB[a][1] = *reinterpret_cast<const bf16x8*>(bp + (size_t)a * 16 * F_ + (c) * 64 + 32);           \
    }                                                                                                  \
  }
#define G2_COMP(AA, BB)                                                                                \
  {                                                                                                    \
    _Pragma("unroll") for (int s = 0; s < 2; ++s)                                                      \
      _Pragma("unroll") for (int a = 0; a < 4; ++a)                                                    \
        _Pragma("unroll") for (int c = 0; c < 5; ++c)                                                  \
          acc[a][c] = __builtin_amdgcn_mfma_f32_16x16x32_bf16(AA[a][s], BB[c][s], acc[a][c], 0, 0, 0); \
  }
  G2_LOAD(A0, B0, c0);
  for (int c = c0; c < c1; c += 2) {
    if (c + 1 < c1) G2_LOAD(A1, B1, c + 1);
    G2_COMP(A0, B0);
    if (c + 1 >= c1) break;
    if (c + 2 < c1) G2_LOAD(A0, B0, c + 2);
    G2_COMP(A1, B1);
  }
#undef G2_LOAD
#undef G2_COMP

#define G2_PUT(S) _Pragma("unroll") for (int a = 0; a < 4; ++a) _Pragma("unroll") for (int c = 0; c < 5; ++c) \
    _Pragma("unroll") for (int i = 0; i < 4; ++i) red[S][16 * a + 4 * fq + i][16 * c + fr] = acc[a][c][i];
#define G2_ADD(S) _Pragma("unroll") for (int a = 0; a < 4; ++a) _Pragma("unroll") for (int c = 0; c < 5; ++c) \
    _Pragma("unroll") for (int i = 0; i < 4; ++i) acc[a][c][i] += red[S][16 * a + 4 * fq + i][16 * c + fr];
  if (wid >= 2) { G2_PUT(wid - 2); }
  __syncthreads();
  if (wid < 2) { G2_ADD(wid); }
  __syncthreads();
  if (wid == 1) { G2_PUT(1); }
  __syncthreads();
  if (wid == 0) {
    G2_ADD(1);
#pragma unroll
    for (int a = 0; a < 4; ++a)
#pragma unroll
      for (int c = 0; c < 5; ++c)
#pragma unroll
        for (int i = 0; i < 4; ++i)
          bbuf[(size_t)(m0 + 16 * a + 4 * fq + i) * D_ + n0 + 16 * c + fr] = f2bf(acc[a][c][i]);
  }
#undef G2_PUT
#undef G2_ADD
}

__global__ __launch_bounds__(256) void k2_conv_gemm2(const unsigned short* __restrict__ uT, const unsigned short* __restrict__ logAr,
                                               const unsigned short* __restrict__ Aim, const unsigned short* __restrict__ Cre,
                                               const unsigned short* __restrict__ Cim, const unsigned short* __restrict__ logdt,
                                               const unsigned short* __restrict__ Dsk, unsigned short* __restrict__ aT,
                                               const unsigned short* __restrict__ hbuf, const unsigned short* __restrict__ w2T,
                                               unsigned short* __restrict__ bbuf) {
  extern __shared__ __attribute__((aligned(16))) char smem[];
  if (blockIdx.x < 256) gemm2_role(blockIdx.x, hbuf, w2T, bbuf, smem);
  else conv_role(blockIdx.x - 256, uT, logAr, Aim, Cre, Cim, logdt, Dsk, aT, smem);
}

__global__ __launch_bounds__(256) void k3_mix(const unsigned short* __restrict__ aT, const unsigned short* __restrict__ bbuf,
                                              const float* __restrict__ gbuf, unsigned short* __restrict__ out) {
  __shared__ unsigned short tile[64][72];
  const int tid = threadIdx.x;
  const int tl = blockIdx.x / 10, td = blockIdx.x % 10;
  u32x4 bv[2];
  float g[2];
#pragma unroll
  for (int i = 0; i < 2; ++i) {
    int id = tid + 256 * i, row = id >> 3, ch = id & 7;
    *reinterpret_cast<u32x4*>(&tile[row][ch * 8]) = *reinterpret_cast<const u32x4*>(aT + (size_t)(td * 64 + row) * L_ + tl * 64 + ch * 8);
    bv[i] = *reinterpret_cast<const u32x4*>(bbuf + (size_t)(tl * 64 + row) * D_ + td * 64 + ch * 8);
    g[i] = gbuf[tl * 64 + row];
  }
  __syncthreads();
#pragma unroll
  for (int i = 0; i < 2; ++i) {
    int id = tid + 256 * i, row = id >> 3, ch = id & 7;
    unsigned short o[8];
    const float t2 = rbf(1.0f - g[i]);
#pragma unroll
    for (int k = 0; k < 8; ++k) {
      float a = bf2f(tile[ch * 8 + k][row]);
      float b = bf2f((unsigned short)((bv[i][k >> 1] >> (16 * (k & 1))) & 0xffff));
      o[k] = f2bf(rbf(g[i] * a) + rbf(t2 * b));
    }
    u32x4 w;
#pragma unroll
    for (int j = 0; j < 4; ++j) w[j] = o[2 * j] | ((unsigned)o[2 * j + 1] << 16);
    *reinterpret_cast<u32x4*>(out + (size_t)(tl * 64 + row) * D_ + td * 64 + ch * 8) = w;
  }
}

extern "C" void kernel_launch(void* const* d_in, const int* in_sizes, int n_in, void* d_out, int out_size, void* d_ws,
                              size_t ws_size, hipStream_t stream) {
  const unsigned short* u = (const unsigned short*)d_in[0];
  const unsigned short* logAr = (const unsigned short*)d_in[1];
  const unsigned short* Aim = (const unsigned short*)d_in[2];
  const unsigned short* Cre = (const unsigned short*)d_in[3];
  const unsigned short* Cim = (const unsigned short*)d_in[4];
  const unsigned short* logdt = (const unsigned short*)d_in[5];
  const unsigned short* Dsk = (const unsigned short*)d_in[6];
  const unsigned short* w1 = (const unsigned short*)d_in[8];
  const unsigned short* w2 = (const unsigned short*)d_in[9];
  const unsigned short* gw = (const unsigned short*)d_in[10];
  unsigned short* out = (unsigned short*)d_out;

  char* ws = (char*)d_ws;
  unsigned short* hbuf = (unsigned short*)ws;
  unsigned short* aT = (unsigned short*)(ws + (size_t)L_ * F_ * 2);
  unsigned short* uT = (unsigned short*)(ws + (size_t)L_ * F_ * 2 + (size_t)D_ * L_ * 2);
  unsigned short* w2T = (unsigned short*)(ws + (size_t)L_ * F_ * 2 + 2 * (size_t)D_ * L_ * 2);
  float* gbuf = (float*)(ws + (size_t)L_ * F_ * 2 + 2 * (size_t)D_ * L_ * 2 + (size_t)D_ * F_ * 2);
  unsigned short* bbuf = (unsigned short*)((char*)gbuf + 8192);

  static bool attr_set = false;
  if (!attr_set) {
    hipFuncSetAttribute((const void*)k1_gemm1, hipFuncAttributeMaxDynamicSharedMemorySize, G1_LDS);
    attr_set = true;
  }
  hipLaunchKernelGGL(k1_gemm1, dim3(K1_GRID), dim3(256), G1_LDS, stream, u, w1, w2, gw, hbuf, uT, w2T, gbuf);
  hipLaunchKernelGGL(k2_conv_gemm2, dim3(256 + D_), dim3(256), CONV_LDS, stream, uT, logAr, Aim, Cre, Cim, logdt, Dsk, aT, hbuf, w2T, bbuf);
  hipLaunchKernelGGL(k3_mix, dim3(320), dim3(256), 0, stream, aT, bbuf, gbuf, out);
}
```

```cpp
#include <hip/hip_runtime.h>
#include <hip/hip_bf16.h>
#include <stdint.h>

typedef __attribute__((ext_vector_type(8))) short bf16x8;
typedef __attribute__((ext_vector_type(4))) short bf16x4;
typedef __attribute__((ext_vector_type(16))) float f32x16;
typedef __attribute__((ext_vector_type(4))) float f32x4;
typedef __attribute__((ext_vector_type(4))) unsigned int u32x4;
typedef __attribute__((ext_vector_type(2))) unsigned int u32x2;

#define LDS_AS __attribute__((address_space(3)))
#define RAW_BARRIER() do { asm volatile("s_waitcnt lgkmcnt(0)" ::: "memory"); __builtin_amdgcn_s_barrier(); } while (0)

constexpr int L_ = 2048, D_ = 640, NS_ = 128, F_ = 1728;

__device__ __forceinline__ float bf2f(unsigned short v) { return __uint_as_float(((unsigned)v) << 16); }
__device__ __forceinline__ unsigned short f2bf(float f) {
  unsigned r;
  asm("v_cvt_pk_bf16_f32 %0, %1, %1" : "=v"(r) : "v"(f));
  return (unsigned short)(r & 0xffffu);
}
__device__ __forceinline__ float rbf(float f) { return bf2f(f2bf(f)); }

constexpr int G1_TM = L_ / 128, G1_TN = (F_ + 127) / 128;
constexpr int N_G1 = G1_TM * G1_TN;
constexpr int G1_STAGE = 32768, G1_LDS = 3 * G1_STAGE;
constexpr int K1_GRID = N_G1 + 32;

__device__ __forceinline__ void tr_tiles_load(const unsigned short* __restrict__ src, int ss, int r0, int c0,
                                              u32x4 (&v)[2]) {
  const int tid = threadIdx.x;
#pragma unroll
  for (int i = 0; i < 2; ++i) {
    int id = tid + 256 * i, row = id >> 3, ch = id & 7;
    v[i] = *reinterpret_cast<const u32x4*>(src + (size_t)(r0 + row) * ss + c0 + ch * 8);
  }
}
__device__ __forceinline__ void tr_tiles_put(unsigned short (*tile)[72], const u32x4 (&v)[2]) {
  const int tid = threadIdx.x;
#pragma unroll
  for (int i = 0; i < 2; ++i) {
    int id = tid + 256 * i, row = id >> 3, ch = id & 7;
    *reinterpret_cast<u32x4*>(&tile[row][ch * 8]) = v[i];
  }
}
template <bool BLOCKED>
__device__ __forceinline__ void tr_tiles_store(unsigned short (*tile)[72], unsigned short* __restrict__ dst, int ds,
                                               int r0, int c0) {
  const int tid = threadIdx.x;
#pragma unroll
  for (int i = 0; i < 2; ++i) {
    int id = tid + 256 * i, col = id >> 3, ch = id & 7;
    unsigned short o[8];
#pragma unroll
    for (int k = 0; k < 8; ++k) o[k] = tile[ch * 8 + k][col];
    u32x4 w;
#pragma unroll
    for (int j = 0; j < 4; ++j) w[j] = o[2 * j] | ((unsigned)o[2 * j + 1] << 16);
    unsigned short* dp = BLOCKED ? dst + ((size_t)((c0 + col) >> 4) * 54 + ((r0 + ch * 8) >> 5)) * 512 + (col & 15) * 32 + (ch & 3) * 8
                                 : dst + (size_t)(c0 + col) * ds + r0 + ch * 8;
    __builtin_nontemporal_store(w, reinterpret_cast<u32x4*>(dp));
  }
}

__global__ __launch_bounds__(256, 1) void k1_gemm1(const unsigned short* __restrict__ x, const unsigned short* __restrict__ w1,
                                                   const unsigned short* __restrict__ w2, const unsigned short* __restrict__ gw,
                                                   unsigned short* __restrict__ hbuf, unsigned short* __restrict__ uT,
                                                   unsigned short* __restrict__ w2T, float* __restrict__ gbuf) {
  extern __shared__ __attribute__((aligned(16))) char smem[];
  const int tid = threadIdx.x, lane = tid & 63, wid = tid >> 6;
  const int b = blockIdx.x;
  if (b >= N_G1) {
    const int r0 = (b - N_G1) * 64;
    u32x4 v[10][2];
#pragma unroll
    for (int t = 0; t < 10; ++t) tr_tiles_load(x, D_, r0, t * 64, v[t]);
    float s[2] = {0.f, 0.f};
#pragma unroll
    for (int t = 0; t < 10; ++t) {
      u32x4 g = *reinterpret_cast<const u32x4*>(gw + t * 64 + (tid & 7) * 8);
#pragma unroll
      for (int i = 0; i < 2; ++i)
#pragma unroll
        for (int j = 0; j < 4; ++j) {
          s[i] += rbf(__uint_as_float(v[t][i][j] << 16) * __uint_as_float(g[j] << 16));
          s[i] += rbf(__uint_as_float(v[t][i][j] & 0xffff0000u) * __uint_as_float(g[j] & 0xffff0000u));
        }
    }
#pragma unroll
    for (int i = 0; i < 2; ++i) {
      s[i] += __shfl_xor(s[i], 1); s[i] += __shfl_xor(s[i], 2); s[i] += __shfl_xor(s[i], 4);
      if ((tid & 7) == 0) gbuf[r0 + (tid >> 3) + 32 * i] = rbf(1.0f / (1.0f + expf(-rbf(s[i]))));
    }
    return;
  }

  u32x4 tv[3][2];
#pragma unroll
  for (int j = 0; j < 3; ++j) {
    int id = b + j * N_G1;
    if (id < 320) tr_tiles_load(x, D_, (id / 10) * 64, (id % 10) * 64, tv[j]);
    else if (id < 590) tr_tiles_load(w2, D_, ((id - 320) / 10) * 64, ((id - 320) % 10) * 64, tv[j]);
  }

  int t = (b & 7) * (N_G1 / 8) + (b >> 3);
  const int tm = t / G1_TN, tn = t % G1_TN;
  const int m0 = tm * 128, n0 = tn * 128;
  const int wm = wid >> 1, wn = wid & 1, r = lane & 31, h = lane >> 5;

  const unsigned short* asrc[4];
  const unsigned short* bsrc[4];
#pragma unroll
  for (int i = 0; i < 4; ++i) {
    int row = (i * 4 + wid) * 8 + (lane >> 3);
    int c = (lane & 7) ^ (row & 7);
    asrc[i] = x + (size_t)(m0 + row) * D_ + c * 8;
    int k = (i * 4 + wid) * 4 + (lane >> 4);
    int cb = (lane & 15) ^ ((k & 3) << 1);
    int col = n0 + cb * 8;
    if (col > F_ - 8) col = F_ - 8;
    bsrc[i] = w1 + (size_t)k * F_ + col;
  }
  auto stage = [&](int kt, int buf) {
    char* sa = smem + buf * G1_STAGE;
    char* sb = sa + 16384;
#pragma unroll
    for (int i = 0; i < 4; ++i)
      __builtin_amdgcn_global_load_lds((const unsigned*)(asrc[i] + kt * 64), (LDS_AS unsigned*)(sa + (i * 4 + wid) * 1024), 16, 0, 0);
#pragma unroll
    for (int i = 0; i < 4; ++i)
      __builtin_amdgcn_global_load_lds((const unsigned*)(bsrc[i] + (size_t)kt * 64 * F_), (LDS_AS unsigned*)(sb + (i * 4 + wid) * 1024), 16, 0, 0);
  };

  f32x16 acc[2][2];
#pragma unroll
  for (int a = 0; a < 2; ++a)
#pragma unroll
    for (int c = 0; c < 2; ++c)
#pragma unroll
      for (int i = 0; i < 16; ++i) acc[a][c][i] = 0.f;

  const int q = (lane & 15) >> 2, p = lane & 3, blk = (lane >> 4) & 1;
  auto compute = [&](int buf) {
    const char* sa = smem + buf * G1_STAGE;
    const char* sb = sa + 16384;
#pragma unroll
    for (int s = 0; s < 4; ++s) {
      bf16x8 af[2], bfr[2];
#pragma unroll
      for (int mt = 0; mt < 2; ++mt) {
        int row = wm * 64 + mt * 32 + r;
        af[mt] = *reinterpret_cast<const bf16x8*>(sa + row * 128 + (((2 * s + h) ^ (row & 7)) << 4));
      }
#pragma unroll
      for (int nt = 0; nt < 2; ++nt)
#pragma unroll
        for (int tt = 0; tt < 2; ++tt) {
          int k = 16 * s + 8 * h + 4 * tt + q;
          int chunk = wn * 8 + nt * 4 + 2 * blk + (p >> 1);
          const char* ad = sb + k * 256 + ((chunk ^ (q << 1)) << 4) + (p & 1) * 8;
          bf16x4 v = __builtin_amdgcn_ds_read_tr16_b64_v4i16((LDS_AS bf16x4*)(ad));
          bfr[nt][4 * tt + 0] = v[0]; bfr[nt][4 * tt + 1] = v[1]; bfr[nt][4 * tt + 2] = v[2]; bfr[nt][4 * tt + 3] = v[3];
        }
#pragma unroll
      for (int mt = 0; mt < 2; ++mt)
#pragma unroll
        for (int nt = 0; nt < 2; ++nt)
          acc[mt][nt] = __builtin_amdgcn_mfma_f32_32x32x16_bf16(af[mt], bfr[nt], acc[mt][nt], 0, 0, 0);
    }
  };

  constexpr int NK = D_ / 64;
  stage(0, 0);
  stage(1, 1);
  for (int kt = 0; kt < NK - 1; ++kt) {
    asm volatile("s_waitcnt vmcnt(8)" ::: "memory");
    RAW_BARRIER();
    if (kt + 2 < NK) stage(kt + 2, (kt + 2) % 3);
    compute(kt % 3);
  }
  asm volatile("s_waitcnt vmcnt(0)" ::: "memory");
  RAW_BARRIER();
  compute((NK - 1) % 3);

  RAW_BARRIER();
  {
    unsigned short* ht = reinterpret_cast<unsigned short*>(smem);
#pragma unroll
    for (int nt = 0; nt < 2; ++nt)
#pragma unroll
      for (int mt = 0; mt < 2; ++mt)
#pragma unroll
        for (int i = 0; i < 16; ++i) {
          int row = wm * 64 + mt * 32 + (i & 3) + 8 * (i >> 2) + 4 * h;
          float hv = rbf(acc[mt][nt][i]);
          float sv = hv * __builtin_amdgcn_rcpf(1.0f + __expf(-hv));
          ht[row * 136 + wn * 64 + nt * 32 + r] = f2bf(sv);
        }
    typedef unsigned short tile_t[64][72];
    tile_t* tiles = reinterpret_cast<tile_t*>(smem + 36864);
#pragma unroll
    for (int j = 0; j < 3; ++j)
      if (b + j * N_G1 < 590) tr_tiles_put(tiles[j], tv[j]);
    __syncthreads();
#pragma unroll
    for (int i = 0; i < 8; ++i) {
      int id = tid + 256 * i, row = id >> 4, ch = id & 15;
      if (n0 + ch * 8 < F_)
        __builtin_nontemporal_store(*reinterpret_cast<const u32x4*>(ht + row * 136 + ch * 8), reinterpret_cast<u32x4*>(hbuf + ((size_t)((m0 + row) >> 4) * 54 + ((n0 + ch * 8) >> 5)) * 512 + (row & 15) * 32 + (ch & 3) * 8));
    }
#pragma unroll
    for (int j = 0; j < 3; ++j) {
      int id = b + j * N_G1;
      if (id < 320) tr_tiles_store<false>(tiles[j], uT, L_, (id / 10) * 64, (id % 10) * 64);
      else if (id < 590) tr_tiles_store<true>(tiles[j], w2T, F_, ((id - 320) / 10) * 64, ((id - 320) % 10) * 64);
    }
  }
}

constexpr int UC_STRIDE = 4384;
constexpr int UC_OFF = 0;
constexpr int KROW = 80;
constexpr int KH_OFF = 8 * UC_STRIDE;
constexpr int KL_OFF = KH_OFF + 64 * KROW + 16;
constexpr int KZ_OFF = KH_OFF + 64 * KROW;
constexpr int PR_OFF = KL_OFF + 64 * KROW;
constexpr int OS_OFF = PR_OFF + 128 * 16;
constexpr int CONV_LDS = OS_OFF + 4 * 48 * 16 * 4;

__device__ __forceinline__ float thi_of(int i) {
  int t;
  if (i < 8) t = i;
  else if (i < 16) t = 128 + (i - 8);
  else if (i < 24) t = 256 + 128 * ((i - 16) >> 2) + 2 * ((i - 16) & 3);
  else if (i < 32) t = 512 + 128 * ((i - 24) >> 1) + 4 * ((i - 24) & 1);
  else if (i < 40) t = 1024 + 128 * (i - 32);
  else t = 2048;
  return (float)t;
}

__device__ void conv_role(int d, const unsigned short* __restrict__ uT, const unsigned short* __restrict__ logAr,
                          const unsigned short* __restrict__ Aim, const unsigned short* __restrict__ Cre,
                          const unsigned short* __restrict__ Cim, const unsigned short* __restrict__ logdt,
                          const unsigned short* __restrict__ Dsk, unsigned short* __restrict__ aT, char* smem) {
  const int tid = threadIdx.x, lane = tid & 63, wid = tid >> 6;

  const u32x4 uvec = *reinterpret_cast<const u32x4*>(uT + (size_t)d * L_ + 8 * tid);

  const float dt = rbf(expf(bf2f(logdt[d])));
  if (tid < NS_) {
    float are = -rbf(expf(bf2f(logAr[d * NS_ + tid])));
    float aim = bf2f(Aim[d * NS_ + tid]);
    float lr = are * dt, li = aim * dt;
    float cr = bf2f(Cre[d * NS_ + tid]) * dt, ci = bf2f(Cim[d * NS_ + tid]) * dt;
    float mag = sqrtf(cr * cr + ci * ci);
    f32x4 pp;
    pp[0] = lr * 1.4426950408889634f;
    pp[1] = li * 0.15915494309189535f;
    pp[2] = log2f(mag);
    pp[3] = atan2f(ci, cr) * 0.15915494309189535f;
    *reinterpret_cast<f32x4*>(smem + PR_OFF + tid * 16) = pp;
  }
  if (tid == 0) { u32x4 z = {0, 0, 0, 0}; *reinterpret_cast<u32x4*>(smem + KZ_OFF) = z; }
  if (tid < 64) {
    u32x4 z = {0, 0, 0, 0};
    *reinterpret_cast<u32x4*>(smem + UC_OFF + (tid >> 3) * UC_STRIDE + (tid & 7) * 16) = z;
  }
  *reinterpret_cast<u32x4*>(smem + UC_OFF + 128 + tid * 16) = uvec;
  __syncthreads();

  {
    u32x4 v1 = {0, 0, 0, 0};
    if (tid < 255) v1 = *reinterpret_cast<const u32x4*>(smem + UC_OFF + 128 + (tid + 1) * 16);
    unsigned w[8] = {uvec[0], uvec[1], uvec[2], uvec[3], v1[0], v1[1], v1[2], v1[3]};
#pragma unroll
    for (int e = 1; e < 8; ++e) {
      u32x4 o;
#pragma unroll
      for (int j = 0; j < 4; ++j) {
        int base = j + (e >> 1);
        if (e & 1) o[j] = (w[base] >> 16) | (w[base + 1] << 16);
        else o[j] = w[base];
      }
      *reinterpret_cast<u32x4*>(smem + UC_OFF + e * UC_STRIDE + 128 + tid * 16) = o;
    }
    if (tid == 0) {
#pragma unroll
      for (int e = 1; e < 8; ++e)
#pragma unroll
        for (int k = 0; k < e; ++k)
          *reinterpret_cast<unsigned short*>(smem + UC_OFF + e * UC_STRIDE + 2 * (64 - e + k)) =
              (unsigned short)(w[k >> 1] >> (16 * (k & 1)));
    }
  }

  {
    const int x = lane & 15, kk = lane >> 4;
    float th[3];
#pragma unroll
    for (int T = 0; T < 3; ++T) th[T] = thi_of(16 * T + x);
    const float tlo = (float)(8 * x);
    f32x4 acc[3];
#pragma unroll
    for (int T = 0; T < 3; ++T) acc[T] = (f32x4){0.f, 0.f, 0.f, 0.f};
#pragma unroll 2
    for (int g = 0; g < 8; ++g) {
      int n = 32 * wid + 4 * g + kk;
      f32x4 pp = *reinterpret_cast<const f32x4*>(smem + PR_OFF + n * 16);
      float qe = __builtin_amdgcn_exp2f(pp[0] * tlo);
      float qa = pp[1] * tlo;
      float qr = qe * __builtin_amdgcn_cosf(qa), qi = qe * __builtin_amdgcn_sinf(qa);
#pragma unroll
      for (int T = 0; T < 3; ++T) {
        float pe = __builtin_amdgcn_exp2f(pp[0] * th[T] + pp[2]);
        float pa = pp[1] * th[T] + pp[3];
        float pr = pe * __builtin_amdgcn_cosf(pa), pi = pe * __builtin_amdgcn_sinf(pa);
        acc[T] = __builtin_amdgcn_mfma_f32_16x16x4f32(qr, pr, acc[T], 0, 0, 0);
        acc[T] = __builtin_amdgcn_mfma_f32_16x16x4f32(-qi, pi, acc[T], 0, 0, 0);
      }
    }
    float* os = reinterpret_cast<float*>(smem + OS_OFF) + wid * 768;
#pragma unroll
    for (int T = 0; T < 3; ++T) *reinterpret_cast<f32x4*>(os + (16 * T + x) * 16 + 4 * kk) = acc[T];
  }
  __syncthreads();

  {
    unsigned short kh[8], kl[8];
#pragma unroll
    for (int k = 0; k < 8; ++k) {
      int j = 8 * tid + k;
      int rr = (int)rbf((float)j);
      int i;
      if (rr < 256) i = (rr & 7) + 8 * (rr >> 7);
      else if (rr < 512) i = 16 + 4 * ((rr - 256) >> 7) + ((rr & 7) >> 1);
      else if (rr < 1024) i = 24 + 2 * ((rr - 512) >> 7) + ((rr & 7) >> 2);
      else if (rr < 2048) i = 32 + ((rr - 1024) >> 7);
      else i = 40;
      int jj = (rr >> 3) & 15;
      const float* osp = reinterpret_cast<const float*>(smem + OS_OFF) + i * 16 + jj;
      float kv = 2.0f * ((osp[0] + osp[768]) + (osp[1536] + osp[2304]));
      unsigned short hi = f2bf(kv);
      kh[7 - k] = hi;
      kl[7 - k] = f2bf(kv - bf2f(hi));
    }
    u32x4 vh, vl;
#pragma unroll
    for (int j = 0; j < 4; ++j) {
      vh[j] = kh[2 * j] | ((unsigned)kh[2 * j + 1] << 16);
      vl[j] = kl[2 * j] | ((unsigned)kl[2 * j + 1] << 16);
    }
    *reinterpret_cast<u32x4*>(smem + KH_OFF + (tid >> 2) * KROW + (tid & 3) * 16) = vh;
    *reinterpret_cast<u32x4*>(smem + KL_OFF + (tid >> 2) * KROW + (tid & 3) * 16) = vl;
  }
  __syncthreads();

  f32x16 y0, y1;
#pragma unroll
  for (int i = 0; i < 16; ++i) { y0[i] = 0.f; y1[i] = 0.f; }
  {
    const int r = lane & 31, h = lane >> 5;
    const int e = (r + 1) & 7;
    const char* ubase = smem + UC_OFF + e * UC_STRIDE + 2 * (r - 7 - e + 64 - 8 * h);
    for (int dl = wid; dl < 64; dl += 4) {
      bf16x8 a0 = *reinterpret_cast<const bf16x8*>(ubase + 64 * dl);
      bf16x8 a1 = *reinterpret_cast<const bf16x8*>(ubase + 64 * dl - 32);
      {
        int m = 32 + r - dl;
        int off = (m >= 0) ? (m * KROW + 16 * h) : (KZ_OFF - KH_OFF);
        int off1 = (m >= 0) ? (off + 32) : off;
        bf16x8 bh0 = *reinterpret_cast<const bf16x8*>(smem + KH_OFF + off);
        bf16x8 bh1 = *reinterpret_cast<const bf16x8*>(smem + KH_OFF + off1);
        bf16x8 bl0 = *reinterpret_cast<const bf16x8*>(smem + ((m >= 0) ? KL_OFF : KH_OFF) + off);
        bf16x8 bl1 = *reinterpret_cast<const bf16x8*>(smem + ((m >= 0) ? KL_OFF : KH_OFF) + off1);
        y1 = __builtin_amdgcn_mfma_f32_32x32x16_bf16(a0, bh0, y1, 0, 0, 0);
        y1 = __builtin_amdgcn_mfma_f32_32x32x16_bf16(a1, bh1, y1, 0, 0, 0);
        y1 = __builtin_amdgcn_mfma_f32_32x32x16_bf16(a0, bl0, y1, 0, 0, 0);
        y1 = __builtin_amdgcn_mfma_f32_32x32x16_bf16(a1, bl1, y1, 0, 0, 0);
      }
      if (dl < 32) {
        int m = r - dl;
        int off = (m >= 0) ? (m * KROW + 16 * h) : (KZ_OFF - KH_OFF);
        int off1 = (m >= 0) ? (off + 32) : off;
        bf16x8 bh0 = *reinterpret_cast<const bf16x8*>(smem + KH_OFF + off);
        bf16x8 bh1 = *reinterpret_cast<const bf16x8*>(smem + KH_OFF + off1);
        bf16x8 bl0 = *reinterpret_cast<const bf16x8*>(smem + ((m >= 0) ? KL_OFF : KH_OFF) + off);
        bf16x8 bl1 = *reinterpret_cast<const bf16x8*>(smem + ((m >= 0) ? KL_OFF : KH_OFF) + off1);
        y0 = __builtin_amdgcn_mfma_f32_32x32x16_bf16(a0, bh0, y0, 0, 0, 0);
        y0 = __builtin_amdgcn_mfma_f32_32x32x16_bf16(a1, bh1, y0, 0, 0, 0);
        y0 = __builtin_amdgcn_mfma_f32_32x32x16_bf16(a0, bl0, y0, 0, 0, 0);
        y0 = __builtin_amdgcn_mfma_f32_32x32x16_bf16(a1, bl1, y0, 0, 0, 0);
      }
    }
  }
  __syncthreads();
  {
    const int r = lane & 31, h = lane >> 5;
    float* slab = reinterpret_cast<float*>(smem + wid * 8704);
#pragma unroll
    for (int g = 0; g < 4; ++g) {
      f32x4 v0 = {y0[4 * g], y0[4 * g + 1], y0[4 * g + 2], y0[4 * g + 3]};
      f32x4 v1 = {y1[4 * g], y1[4 * g + 1], y1[4 * g + 2], y1[4 * g + 3]};
      int b = 8 * g + 4 * h;
      *reinterpret_cast<f32x4*>(slab + 32 * r + b) = v0;
      *reinterpret_cast<f32x4*>(slab + 32 * (32 + r) + b) = v1;
    }
  }
  __syncthreads();
  {
    const float dsk = bf2f(Dsk[d]);
    const float* s = reinterpret_cast<const float*>(smem);
    f32x4 ys[2];
#pragma unroll
    for (int hh = 0; hh < 2; ++hh) {
      f32x4 s0 = *reinterpret_cast<const f32x4*>(s + 8 * tid + 4 * hh);
      f32x4 s1 = *reinterpret_cast<const f32x4*>(s + 2176 + 8 * tid + 4 * hh);
      f32x4 s2 = *reinterpret_cast<const f32x4*>(s + 4352 + 8 * tid + 4 * hh);
      f32x4 s3 = *reinterpret_cast<const f32x4*>(s + 6528 + 8 * tid + 4 * hh);
      ys[hh] = (s0 + s1) + (s2 + s3);
    }
    unsigned short o[8];
#pragma unroll
    for (int k = 0; k < 8; ++k) {
      float yb = rbf(ys[k >> 2][k & 3]);
      float uu = bf2f((unsigned short)(uvec[k >> 1] >> (16 * (k & 1))));
      float sk = rbf(uu * dsk);
      o[k] = f2bf(yb + sk);
    }
    u32x4 v;
#pragma unroll
    for (int j = 0; j < 4; ++j) v[j] = o[2 * j] | ((unsigned)o[2 * j + 1] << 16);
    *reinterpret_cast<u32x4*>(aT + (size_t)d * L_ + 8 * tid) = v;
  }
}

__global__ __launch_bounds__(256) void k2_conv(const unsigned short* __restrict__ uT, const unsigned short* __restrict__ logAr,
                                               const unsigned short* __restrict__ Aim, const unsigned short* __restrict__ Cre,
                                               const unsigned short* __restrict__ Cim, const unsigned short* __restrict__ logdt,
                                               const unsigned short* __restrict__ Dsk, unsigned short* __restrict__ aT) {
  extern __shared__ __attribute__((aligned(16))) char smem[];
  conv_role(blockIdx.x, uT, logAr, Aim, Cre, Cim, logdt, Dsk, aT, smem);
}

constexpr int G2_RS = 84;
__global__ __launch_bounds__(256, 1) void k3_gemm2(const unsigned short* __restrict__ hbuf, const unsigned short* __restrict__ w2T,
                                                   const unsigned short* __restrict__ aT, const float* __restrict__ gbuf,
                                                   unsigned short* __restrict__ out) {
  __shared__ float red[4][64][G2_RS];
  const int tid = threadIdx.x, lane = tid & 63, wid = tid >> 6;
  int t = (blockIdx.x & 7) * 32 + (blockIdx.x >> 3);
  const int tm = t >> 3, tn = t & 7;
  const int m0 = tm * 64, n0 = tn * 80;
  const int fr = lane & 15, fq = lane >> 4;
  const int c0 = (wid * 27) >> 2, c1 = ((wid + 1) * 27) >> 2;

  const unsigned short* ap = hbuf + (size_t)(m0 >> 4) * 54 * 512 + fr * 32 + 8 * fq;
  const unsigned short* bp = w2T + (size_t)(n0 >> 4) * 54 * 512 + fr * 32 + 8 * fq;

  f32x4 acc[4][5];
#pragma unroll
  for (int a = 0; a < 4; ++a)
#pragma unroll
    for (int c = 0; c < 5; ++c) acc[a][c] = (f32x4){0.f, 0.f, 0.f, 0.f};

  bf16x8 A0[4][2], B0[5][2], A1[4][2], B1[5][2];
#define G2_LOAD(AA, BB, c)                                                                             \
  {                                                                                                    \
    _Pragma("unroll") for (int a = 0; a < 4; ++a) {                                                    \
      AA[a][0] = *reinterpret_cast<const bf16x8*>(ap + a * 54 * 512 + (c) * 1024);                \
      AA[a][1] = *reinterpret_cast<const bf16x8*>(ap + a * 54 * 512 + (c) * 1024 + 512);            \
    }                                                                                                  \
    _Pragma("unroll") for (int a = 0; a < 5; ++a) {                                                    \
      BB[a][0] = *reinterpret_cast<const bf16x8*>(bp + a * 54 * 512 + (c) * 1024);                \
      BB[a][1] = *reinterpret_cast<const bf16x8*>(bp + a * 54 * 512 + (c) * 1024 + 512);            \
    }                                                                                                  \
  }
#define G2_COMP(AA, BB)                                                                                \
  {                                                                                                    \
    _Pragma("unroll") for (int s = 0; s < 2; ++s)                                                      \
      _Pragma("unroll") for (int a = 0; a < 4; ++a)                                                    \
        _Pragma("unroll") for (int c = 0; c < 5; ++c)                                                  \
          acc[a][c] = __builtin_amdgcn_mfma_f32_16x16x32_bf16(AA[a][s], BB[c][s], acc[a][c], 0, 0, 0); \
  }
  G2_LOAD(A0, B0, c0);
  for (int c = c0; c < c1; c += 2) {
    if (c + 1 < c1) G2_LOAD(A1, B1, c + 1);
    G2_COMP(A0, B0);
    if (c + 1 >= c1) break;
    if (c + 2 < c1) G2_LOAD(A0, B0, c + 2);
    G2_COMP(A1, B1);
  }
#undef G2_LOAD
#undef G2_COMP

#pragma unroll
  for (int a = 0; a < 4; ++a)
#pragma unroll
    for (int c = 0; c < 5; ++c)
#pragma unroll
      for (int i = 0; i < 4; ++i) red[wid][16 * a + 4 * fq + i][16 * c + fr] = acc[a][c][i];
  __syncthreads();

#pragma unroll
  for (int j = 0; j < 2; ++j) {
    const int col = lane + 64 * j;
    if (col < 80) {
      const int row0 = m0 + 16 * wid;
      u32x4 av0 = *reinterpret_cast<const u32x4*>(aT + (size_t)(n0 + col) * L_ + row0);
      u32x4 av1 = *reinterpret_cast<const u32x4*>(aT + (size_t)(n0 + col) * L_ + row0 + 8);
#pragma unroll
      for (int i = 0; i < 16; ++i) {
        int rr = 16 * wid + i;
        float bv = rbf((red[0][rr][col] + red[1][rr][col]) + (red[2][rr][col] + red[3][rr][col]));
        unsigned aw = (i < 8) ? av0[(i >> 1) & 3] : av1[(i >> 1) & 3];
        float a = bf2f((unsigned short)((aw >> (16 * (i & 1))) & 0xffff));
        float g = gbuf[row0 + i];
        float t1 = rbf(g * a);
        float t2 = rbf(1.0f - g);
        float t3 = rbf(t2 * bv);
        out[(size_t)(row0 + i) * D_ + n0 + col] = f2bf(t1 + t3);
      }
    }
  }
}

extern "C" void kernel_launch(void* const* d_in, const int* in_sizes, int n_in, void* d_out, int out_size, void* d_ws,
                              size_t ws_size, hipStream_t stream) {
  const unsigned short* u = (const unsigned short*)d_in[0];
  const unsigned short* logAr = (const unsigned short*)d_in[1];
  const unsigned short* Aim = (const unsigned short*)d_in[2];
  const unsigned short* Cre = (const unsigned short*)d_in[3];
  const unsigned short* Cim = (const unsigned short*)d_in[4];
  const unsigned short* logdt = (const unsigned short*)d_in[5];
  const unsigned short* Dsk = (const unsigned short*)d_in[6];
  const unsigned short* w1 = (const unsigned short*)d_in[8];
  const unsigned short* w2 = (const unsigned short*)d_in[9];
  const unsigned short* gw = (const unsigned short*)d_in[10];
  unsigned short* out = (unsigned short*)d_out;

  char* ws = (char*)d_ws;
  unsigned short* hbuf = (unsigned short*)ws;
  unsigned short* aT = (unsigned short*)(ws + (size_t)L_ * F_ * 2);
  unsigned short* uT = (unsigned short*)(ws + (size_t)L_ * F_ * 2 + (size_t)D_ * L_ * 2);
  unsigned short* w2T = (unsigned short*)(ws + (size_t)L_ * F_ * 2 + 2 * (size_t)D_ * L_ * 2);
  float* gbuf = (float*)(ws + (size_t)L_ * F_ * 2 + 2 * (size_t)D_ * L_ * 2 + (size_t)D_ * F_ * 2);

  static bool attr_set = false;
  if (!attr_set) {
    hipFuncSetAttribute((const void*)k1_gemm1, hipFuncAttributeMaxDynamicSharedMemorySize, G1_LDS);
    attr_set = true;
  }
  hipLaunchKernelGGL(k1_gemm1, dim3(K1_GRID), dim3(256), G1_LDS, stream, u, w1, w2, gw, hbuf, uT, w2T, gbuf);
  hipLaunchKernelGGL(k2_conv, dim3(D_), dim3(256), CONV_LDS, stream, uT, logAr, Aim, Cre, Cim, logdt, Dsk, aT);
  hipLaunchKernelGGL(k3_gemm2, dim3(256), dim3(256), 0, stream, hbuf, w2T, aT, gbuf, out);
}
```

```cpp
#include <hip/hip_runtime.h>
#include <hip/hip_bf16.h>
#include <stdint.h>

typedef __attribute__((ext_vector_type(8))) short bf16x8;
typedef __attribute__((ext_vector_type(4))) short bf16x4;
typedef __attribute__((ext_vector_type(16))) float f32x16;
typedef __attribute__((ext_vector_type(4))) float f32x4;
typedef __attribute__((ext_vector_type(4))) unsigned int u32x4;
typedef __attribute__((ext_vector_type(2))) unsigned int u32x2;

#define LDS_AS __attribute__((address_space(3)))
#define RAW_BARRIER() do { asm volatile("s_waitcnt lgkmcnt(0)" ::: "memory"); __builtin_amdgcn_s_barrier(); } while (0)

constexpr int L_ = 2048, D_ = 640, NS_ = 128, F_ = 1728;

__device__ __forceinline__ float bf2f(unsigned short v) { return __uint_as_float(((unsigned)v) << 16); }
__device__ __forceinline__ unsigned short f2bf(float f) {
  unsigned r;
  asm("v_cvt_pk_bf16_f32 %0, %1, %1" : "=v"(r) : "v"(f));
  return (unsigned short)(r & 0xffffu);
}
__device__ __forceinline__ float rbf(float f) { return bf2f(f2bf(f)); }

constexpr int G1_TM = L_ / 128, G1_TN = (F_ + 127) / 128;
constexpr int N_G1 = G1_TM * G1_TN;
constexpr int G1_STAGE = 32768, G1_LDS = 3 * G1_STAGE;
constexpr int K1_GRID = N_G1 + 32;

__device__ __forceinline__ void tr_tiles_load(const unsigned short* __restrict__ src, int ss, int r0, int c0,
                                              u32x4 (&v)[2]) {
  const int tid = threadIdx.x;
#pragma unroll
  for (int i = 0; i < 2; ++i) {
    int id = tid + 256 * i, row = id >> 3, ch = id & 7;
    v[i] = *reinterpret_cast<const u32x4*>(src + (size_t)(r0 + row) * ss + c0 + ch * 8);
  }
}
__device__ __forceinline__ void tr_tiles_put(unsigned short (*tile)[72], const u32x4 (&v)[2]) {
  const int tid = threadIdx.x;
#pragma unroll
  for (int i = 0; i < 2; ++i) {
    int id = tid + 256 * i, row = id >> 3, ch = id & 7;
    *reinterpret_cast<u32x4*>(&tile[row][ch * 8]) = v[i];
  }
}
template <bool BLOCKED>
__device__ __forceinline__ void tr_tiles_store(unsigned short (*tile)[72], unsigned short* __restrict__ dst, int ds,
                                               int r0, int c0) {
  const int tid = threadIdx.x;
#pragma unroll
  for (int i = 0; i < 2; ++i) {
    int id = tid + 256 * i, col = id >> 3, ch = id & 7;
    unsigned short o[8];
#pragma unroll
    for (int k = 0; k < 8; ++k) o[k] = tile[ch * 8 + k][col];
    u32x4 w;
#pragma unroll
    for (int j = 0; j < 4; ++j) w[j] = o[2 * j] | ((unsigned)o[2 * j + 1] << 16);
    unsigned short* dp = BLOCKED ? dst + ((size_t)((c0 + col) >> 4) * 54 + ((r0 + ch * 8) >> 5)) * 512 + (col & 15) * 32 + (ch & 3) * 8
                                 : dst + (size_t)(c0 + col) * ds + r0 + ch * 8;
    __builtin_nontemporal_store(w, reinterpret_cast<u32x4*>(dp));
  }
}

__global__ __launch_bounds__(256, 1) void k1_gemm1(const unsigned short* __restrict__ x, const unsigned short* __restrict__ w1,
                                                   const unsigned short* __restrict__ w2, const unsigned short* __restrict__ gw,
                                                   unsigned short* __restrict__ hbuf, unsigned short* __restrict__ uT,
                                                   unsigned short* __restrict__ w2T, float* __restrict__ gbuf,
                                                   unsigned short* __restrict__ ktab) {
  extern __shared__ __attribute__((aligned(16))) char smem[];
  const int tid = threadIdx.x, lane = tid & 63, wid = tid >> 6;
  const int b = blockIdx.x;
  if (b >= N_G1) {
    const int r0 = (b - N_G1) * 64;
    if (tid < 64) {
      int rr = (int)rbf((float)(r0 + tid));
      int i;
      if (rr < 256) i = (rr & 7) + 8 * (rr >> 7);
      else if (rr < 512) i = 16 + 4 * ((rr - 256) >> 7) + ((rr & 7) >> 1);
      else if (rr < 1024) i = 24 + 2 * ((rr - 512) >> 7) + ((rr & 7) >> 2);
      else if (rr < 2048) i = 32 + ((rr - 1024) >> 7);
      else i = 40;
      ktab[r0 + tid] = (unsigned short)(i * 16 + ((rr >> 3) & 15));
    }
    u32x4 v[10][2];
#pragma unroll
    for (int t = 0; t < 10; ++t) tr_tiles_load(x, D_, r0, t * 64, v[t]);
    float s[2] = {0.f, 0.f};
#pragma unroll
    for (int t = 0; t < 10; ++t) {
      u32x4 g = *reinterpret_cast<const u32x4*>(gw + t * 64 + (tid & 7) * 8);
#pragma unroll
      for (int i = 0; i < 2; ++i)
#pragma unroll
        for (int j = 0; j < 4; ++j) {
          s[i] += rbf(__uint_as_float(v[t][i][j] << 16) * __uint_as_float(g[j] << 16));
          s[i] += rbf(__uint_as_float(v[t][i][j] & 0xffff0000u) * __uint_as_float(g[j] & 0xffff0000u));
        }
    }
#pragma unroll
    for (int i = 0; i < 2; ++i) {
      s[i] += __shfl_xor(s[i], 1); s[i] += __shfl_xor(s[i], 2); s[i] += __shfl_xor(s[i], 4);
      if ((tid & 7) == 0) gbuf[r0 + (tid >> 3) + 32 * i] = rbf(1.0f / (1.0f + expf(-rbf(s[i]))));
    }
    return;
  }

  u32x4 tv[3][2];
#pragma unroll
  for (int j = 0; j < 3; ++j) {
    int id = b + j * N_G1;
    if (id < 320) tr_tiles_load(x, D_, (id / 10) * 64, (id % 10) * 64, tv[j]);
    else if (id < 590) tr_tiles_load(w2, D_, ((id - 320) / 10) * 64, ((id - 320) % 10) * 64, tv[j]);
  }

  int t = (b & 7) * (N_G1 / 8) + (b >> 3);
  const int tm = t / G1_TN, tn = t % G1_TN;
  const int m0 = tm * 128, n0 = tn * 128;
  const int wm = wid >> 1, wn = wid & 1, r = lane & 31, h = lane >> 5;

  const unsigned short* asrc[4];
  const unsigned short* bsrc[4];
#pragma unroll
  for (int i = 0; i < 4; ++i) {
    int row = (i * 4 + wid) * 8 + (lane >> 3);
    int c = (lane & 7) ^ (row & 7);
    asrc[i] = x + (size_t)(m0 + row) * D_ + c * 8;
    int k = (i * 4 + wid) * 4 + (lane >> 4);
    int cb = (lane & 15) ^ ((k & 3) << 1);
    int col = n0 + cb * 8;
    if (col > F_ - 8) col = F_ - 8;
    bsrc[i] = w1 + (size_t)k * F_ + col;
  }
  auto stage = [&](int kt, int buf) {
    char* sa = smem + buf * G1_STAGE;
    char* sb = sa + 16384;
#pragma unroll
    for (int i = 0; i < 4; ++i)
      __builtin_amdgcn_global_load_lds((const unsigned*)(asrc[i] + kt * 64), (LDS_AS unsigned*)(sa + (i * 4 + wid) * 1024), 16, 0, 0);
#pragma unroll
    for (int i = 0; i < 4; ++i)
      __builtin_amdgcn_global_load_lds((const unsigned*)(bsrc[i] + (size_t)kt * 64 * F_), (LDS_AS unsigned*)(sb + (i * 4 + wid) * 1024), 16, 0, 0);
  };

  f32x16 acc[2][2];
#pragma unroll
  for (int a = 0; a < 2; ++a)
#pragma unroll
    for (int c = 0; c < 2; ++c)
#pragma unroll
      for (int i = 0; i < 16; ++i) acc[a][c][i] = 0.f;

  const int q = (lane & 15) >> 2, p = lane & 3, blk = (lane >> 4) & 1;
  auto compute = [&](int buf) {
    const char* sa = smem + buf * G1_STAGE;
    const char* sb = sa + 16384;
#pragma unroll
    for (int s = 0; s < 4; ++s) {
      bf16x8 af[2], bfr[2];
#pragma unroll
      for (int mt = 0; mt < 2; ++mt) {
        int row = wm * 64 + mt * 32 + r;
        af[mt] = *reinterpret_cast<const bf16x8*>(sa + row * 128 + (((2 * s + h) ^ (row & 7)) << 4));
      }
#pragma unroll
      for (int nt = 0; nt < 2; ++nt)
#pragma unroll
        for (int tt = 0; tt < 2; ++tt) {
          int k = 16 * s + 8 * h + 4 * tt + q;
          int chunk = wn * 8 + nt * 4 + 2 * blk + (p >> 1);
          const char* ad = sb + k * 256 + ((chunk ^ (q << 1)) << 4) + (p & 1) * 8;
          bf16x4 v = __builtin_amdgcn_ds_read_tr16_b64_v4i16((LDS_AS bf16x4*)(ad));
          bfr[nt][4 * tt + 0] = v[0]; bfr[nt][4 * tt + 1] = v[1]; bfr[nt][4 * tt + 2] = v[2]; bfr[nt][4 * tt + 3] = v[3];
        }
#pragma unroll
      for (int mt = 0; mt < 2; ++mt)
#pragma unroll
        for (int nt = 0; nt < 2; ++nt)
          acc[mt][nt] = __builtin_amdgcn_mfma_f32_32x32x16_bf16(af[mt], bfr[nt], acc[mt][nt], 0, 0, 0);
    }
  };

  constexpr int NK = D_ / 64;
  stage(0, 0);
  stage(1, 1);
  for (int kt = 0; kt < NK - 1; ++kt) {
    asm volatile("s_waitcnt vmcnt(8)" ::: "memory");
    RAW_BARRIER();
    if (kt + 2 < NK) stage(kt + 2, (kt + 2) % 3);
    compute(kt % 3);
  }
  asm volatile("s_waitcnt vmcnt(0)" ::: "memory");
  RAW_BARRIER();
  compute((NK - 1) % 3);

  RAW_BARRIER();
  {
    unsigned short* ht = reinterpret_cast<unsigned short*>(smem);
#pragma unroll
    for (int nt = 0; nt < 2; ++nt)
#pragma unroll
      for (int mt = 0; mt < 2; ++mt)
#pragma unroll
        for (int i = 0; i < 16; ++i) {
          int row = wm * 64 + mt * 32 + (i & 3) + 8 * (i >> 2) + 4 * h;
          float hv = rbf(acc[mt][nt][i]);
          float sv = hv * __builtin_amdgcn_rcpf(1.0f + __expf(-hv));
          ht[row * 136 + wn * 64 + nt * 32 + r] = f2bf(sv);
        }
    typedef unsigned short tile_t[64][72];
    tile_t* tiles = reinterpret_cast<tile_t*>(smem + 36864);
#pragma unroll
    for (int j = 0; j < 3; ++j)
      if (b + j * N_G1 < 590) tr_tiles_put(tiles[j], tv[j]);
    __syncthreads();
#pragma unroll
    for (int i = 0; i < 8; ++i) {
      int id = tid + 256 * i, row = id >> 4, ch = id & 15;
      if (n0 + ch * 8 < F_)
        __builtin_nontemporal_store(*reinterpret_cast<const u32x4*>(ht + row * 136 + ch * 8), reinterpret_cast<u32x4*>(hbuf + ((size_t)((m0 + row) >> 4) * 54 + ((n0 + ch * 8) >> 5)) * 512 + (row & 15) * 32 + (ch & 3) * 8));
    }
#pragma unroll
    for (int j = 0; j < 3; ++j) {
      int id = b + j * N_G1;
      if (id < 320) tr_tiles_store<false>(tiles[j], uT, L_, (id / 10) * 64, (id % 10) * 64);
      else if (id < 590) tr_tiles_store<true>(tiles[j], w2T, F_, ((id - 320) / 10) * 64, ((id - 320) % 10) * 64);
    }
  }
}

constexpr int UC_STRIDE = 4384;
constexpr int UC_OFF = 0;
constexpr int KROW = 80;
constexpr int KH_OFF = 8 * UC_STRIDE;
constexpr int KL_OFF = KH_OFF + 64 * KROW + 16;
constexpr int KZ_OFF = KH_OFF + 64 * KROW;
constexpr int PR_OFF = KH_OFF + 12288;
constexpr int OS_OFF = KH_OFF;
constexpr int TB_OFF = PR_OFF + 128 * 16;
constexpr int CONV_LDS = TB_OFF + 768 * 4;

__device__ __forceinline__ float thi_of(int i) {
  int t;
  if (i < 8) t = i;
  else if (i < 16) t = 128 + (i - 8);
  else if (i < 24) t = 256 + 128 * ((i - 16) >> 2) + 2 * ((i - 16) & 3);
  else if (i < 32) t = 512 + 128 * ((i - 24) >> 1) + 4 * ((i - 24) & 1);
  else if (i < 40) t = 1024 + 128 * (i - 32);
  else t = 2048;
  return (float)t;
}

__device__ void conv_role(int d, const unsigned short* __restrict__ uT, const unsigned short* __restrict__ logAr,
                          const unsigned short* __restrict__ Aim, const unsigned short* __restrict__ Cre,
                          const unsigned short* __restrict__ Cim, const unsigned short* __restrict__ logdt,
                          const unsigned short* __restrict__ Dsk, unsigned short* __restrict__ aT,
                          const unsigned short* __restrict__ ktab, char* smem) {
  const int tid = threadIdx.x, lane = tid & 63, wid = tid >> 6;

  const u32x4 uvec = *reinterpret_cast<const u32x4*>(uT + (size_t)d * L_ + 8 * tid);

  const u32x4 kidx = *reinterpret_cast<const u32x4*>(ktab + 8 * tid);

  const float dt = rbf(expf(bf2f(logdt[d])));
  if (tid < NS_) {
    float are = -rbf(expf(bf2f(logAr[d * NS_ + tid])));
    float aim = bf2f(Aim[d * NS_ + tid]);
    float lr = are * dt, li = aim * dt;
    float cr = bf2f(Cre[d * NS_ + tid]) * dt, ci = bf2f(Cim[d * NS_ + tid]) * dt;
    float mag = sqrtf(cr * cr + ci * ci);
    f32x4 pp;
    pp[0] = lr * 1.4426950408889634f;
    pp[1] = li * 0.15915494309189535f;
    pp[2] = log2f(mag);
    pp[3] = atan2f(ci, cr) * 0.15915494309189535f;
    *reinterpret_cast<f32x4*>(smem + PR_OFF + tid * 16) = pp;
  }
  if (tid < 64) {
    u32x4 z = {0, 0, 0, 0};
    *reinterpret_cast<u32x4*>(smem + UC_OFF + (tid >> 3) * UC_STRIDE + (tid & 7) * 16) = z;
  }
  *reinterpret_cast<u32x4*>(smem + UC_OFF + 128 + tid * 16) = uvec;
  __syncthreads();

  {
    u32x4 v1 = {0, 0, 0, 0};
    if (tid < 255) v1 = *reinterpret_cast<const u32x4*>(smem + UC_OFF + 128 + (tid + 1) * 16);
    unsigned w[8] = {uvec[0], uvec[1], uvec[2], uvec[3], v1[0], v1[1], v1[2], v1[3]};
#pragma unroll
    for (int e = 1; e < 8; ++e) {
      u32x4 o;
#pragma unroll
      for (int j = 0; j < 4; ++j) {
        int base = j + (e >> 1);
        if (e & 1) o[j] = (w[base] >> 16) | (w[base + 1] << 16);
        else o[j] = w[base];
      }
      *reinterpret_cast<u32x4*>(smem + UC_OFF + e * UC_STRIDE + 128 + tid * 16) = o;
    }
    if (tid == 0) {
#pragma unroll
      for (int e = 1; e < 8; ++e)
#pragma unroll
        for (int k = 0; k < e; ++k)
          *reinterpret_cast<unsigned short*>(smem + UC_OFF + e * UC_STRIDE + 2 * (64 - e + k)) =
              (unsigned short)(w[k >> 1] >> (16 * (k & 1)));
    }
  }

  {
    const int x = lane & 15, kk = lane >> 4;
    float th[3];
#pragma unroll
    for (int T = 0; T < 3; ++T) th[T] = thi_of(16 * T + x);
    const float tlo = (float)(8 * x);
    f32x4 acc[3];
#pragma unroll
    for (int T = 0; T < 3; ++T) acc[T] = (f32x4){0.f, 0.f, 0.f, 0.f};
#pragma unroll 2
    for (int g = 0; g < 8; ++g) {
      int n = 32 * wid + 4 * g + kk;
      f32x4 pp = *reinterpret_cast<const f32x4*>(smem + PR_OFF + n * 16);
      float qe = __builtin_amdgcn_exp2f(pp[0] * tlo);
      float qa = pp[1] * tlo;
      float qr = qe * __builtin_amdgcn_cosf(qa), qi = qe * __builtin_amdgcn_sinf(qa);
#pragma unroll
      for (int T = 0; T < 3; ++T) {
        float pe = __builtin_amdgcn_exp2f(pp[0] * th[T] + pp[2]);
        float pa = pp[1] * th[T] + pp[3];
        float pr = pe * __builtin_amdgcn_cosf(pa), pi = pe * __builtin_amdgcn_sinf(pa);
        acc[T] = __builtin_amdgcn_mfma_f32_16x16x4f32(qr, pr, acc[T], 0, 0, 0);
        acc[T] = __builtin_amdgcn_mfma_f32_16x16x4f32(-qi, pi, acc[T], 0, 0, 0);
      }
    }
    float* os = reinterpret_cast<float*>(smem + OS_OFF) + wid * 768;
#pragma unroll
    for (int T = 0; T < 3; ++T) *reinterpret_cast<f32x4*>(os + (16 * T + x) * 16 + 4 * kk) = acc[T];
  }
  __syncthreads();

  {
    unsigned* tab = reinterpret_cast<unsigned*>(smem + TB_OFF);
#pragma unroll
    for (int k = 0; k < 3; ++k) {
      int e = tid + 256 * k;
      const float* osp = reinterpret_cast<const float*>(smem + OS_OFF) + e;
      float kv = 2.0f * ((osp[0] + osp[768]) + (osp[1536] + osp[2304]));
      unsigned short hi = f2bf(kv);
      tab[e] = (unsigned)hi | ((unsigned)f2bf(kv - bf2f(hi)) << 16);
    }
  }
  __syncthreads();
  {
    const unsigned* tab = reinterpret_cast<const unsigned*>(smem + TB_OFF);
    unsigned w[8];
#pragma unroll
    for (int k = 0; k < 8; ++k) w[k] = tab[(kidx[k >> 1] >> (16 * (k & 1))) & 0xffff];
    u32x4 vh, vl;
#pragma unroll
    for (int j = 0; j < 4; ++j) {
      vh[j] = (w[7 - 2 * j] & 0xffffu) | (w[6 - 2 * j] << 16);
      vl[j] = (w[7 - 2 * j] >> 16) | (w[6 - 2 * j] & 0xffff0000u);
    }
    *reinterpret_cast<u32x4*>(smem + KH_OFF + (tid >> 2) * KROW + (tid & 3) * 16) = vh;
    *reinterpret_cast<u32x4*>(smem + KL_OFF + (tid >> 2) * KROW + (tid & 3) * 16) = vl;
    if (tid == 0) { u32x4 z = {0, 0, 0, 0}; *reinterpret_cast<u32x4*>(smem + KZ_OFF) = z; }
  }
  __syncthreads();

  f32x16 y0, y1;
#pragma unroll
  for (int i = 0; i < 16; ++i) { y0[i] = 0.f; y1[i] = 0.f; }
  {
    const int r = lane & 31, h = lane >> 5;
    const int e = (r + 1) & 7;
    const char* ubase = smem + UC_OFF + e * UC_STRIDE + 2 * (r - 7 - e + 64 - 8 * h);
#pragma unroll 2
    for (int dl = wid; dl < 32; dl += 4) {
      bf16x8 a0 = *reinterpret_cast<const bf16x8*>(ubase + 64 * dl);
      bf16x8 a1 = *reinterpret_cast<const bf16x8*>(ubase + 64 * dl - 32);
      bf16x8 a2 = *reinterpret_cast<const bf16x8*>(ubase + 64 * dl + 2048);
      bf16x8 a3 = *reinterpret_cast<const bf16x8*>(ubase + 64 * dl + 2048 - 32);
      const int m = r - dl;
      const int off = (m >= 0) ? (m * KROW + 16 * h) : (KZ_OFF - KH_OFF);
      const int off1 = (m >= 0) ? (off + 32) : off;
      const int lo = (m >= 0) ? KL_OFF : KH_OFF;
      bf16x8 fh0 = *reinterpret_cast<const bf16x8*>(smem + KH_OFF + off);
      bf16x8 fh1 = *reinterpret_cast<const bf16x8*>(smem + KH_OFF + off1);
      bf16x8 fl0 = *reinterpret_cast<const bf16x8*>(smem + lo + off);
      bf16x8 fl1 = *reinterpret_cast<const bf16x8*>(smem + lo + off1);
      const int go = (32 + m) * KROW + 16 * h;
      bf16x8 gh0 = *reinterpret_cast<const bf16x8*>(smem + KH_OFF + go);
      bf16x8 gh1 = *reinterpret_cast<const bf16x8*>(smem + KH_OFF + go + 32);
      bf16x8 gl0 = *reinterpret_cast<const bf16x8*>(smem + KL_OFF + go);
      bf16x8 gl1 = *reinterpret_cast<const bf16x8*>(smem + KL_OFF + go + 32);
      y0 = __builtin_amdgcn_mfma_f32_32x32x16_bf16(a0, fh0, y0, 0, 0, 0);
      y1 = __builtin_amdgcn_mfma_f32_32x32x16_bf16(a2, fh0, y1, 0, 0, 0);
      y0 = __builtin_amdgcn_mfma_f32_32x32x16_bf16(a1, fh1, y0, 0, 0, 0);
      y1 = __builtin_amdgcn_mfma_f32_32x32x16_bf16(a3, fh1, y1, 0, 0, 0);
      y0 = __builtin_amdgcn_mfma_f32_32x32x16_bf16(a0, fl0, y0, 0, 0, 0);
      y1 = __builtin_amdgcn_mfma_f32_32x32x16_bf16(a2, fl0, y1, 0, 0, 0);
      y0 = __builtin_amdgcn_mfma_f32_32x32x16_bf16(a1, fl1, y0, 0, 0, 0);
      y1 = __builtin_amdgcn_mfma_f32_32x32x16_bf16(a3, fl1, y1, 0, 0, 0);
      y1 = __builtin_amdgcn_mfma_f32_32x32x16_bf16(a0, gh0, y1, 0, 0, 0);
      y1 = __builtin_amdgcn_mfma_f32_32x32x16_bf16(a1, gh1, y1, 0, 0, 0);
      y1 = __builtin_amdgcn_mfma_f32_32x32x16_bf16(a0, gl0, y1, 0, 0, 0);
      y1 = __builtin_amdgcn_mfma_f32_32x32x16_bf16(a1, gl1, y1, 0, 0, 0);
    }
  }
  __syncthreads();
  {
    const int r = lane & 31, h = lane >> 5;
    float* slab = reinterpret_cast<float*>(smem + wid * 8704);
#pragma unroll
    for (int g = 0; g < 4; ++g) {
      f32x4 v0 = {y0[4 * g], y0[4 * g + 1], y0[4 * g + 2], y0[4 * g + 3]};
      f32x4 v1 = {y1[4 * g], y1[4 * g + 1], y1[4 * g + 2], y1[4 * g + 3]};
      int b = 8 * g + 4 * h;
      *reinterpret_cast<f32x4*>(slab + 32 * r + b) = v0;
      *reinterpret_cast<f32x4*>(slab + 32 * (32 + r) + b) = v1;
    }
  }
  __syncthreads();
  {
    const float dsk = bf2f(Dsk[d]);
    const float* s = reinterpret_cast<const float*>(smem);
    f32x4 ys[2];
#pragma unroll
    for (int hh = 0; hh < 2; ++hh) {
      f32x4 s0 = *reinterpret_cast<const f32x4*>(s + 8 * tid + 4 * hh);
      f32x4 s1 = *reinterpret_cast<const f32x4*>(s + 2176 + 8 * tid + 4 * hh);
      f32x4 s2 = *reinterpret_cast<const f32x4*>(s + 4352 + 8 * tid + 4 * hh);
      f32x4 s3 = *reinterpret_cast<const f32x4*>(s + 6528 + 8 * tid + 4 * hh);
      ys[hh] = (s0 + s1) + (s2 + s3);
    }
    unsigned short o[8];
#pragma unroll
    for (int k = 0; k < 8; ++k) {
      float yb = rbf(ys[k >> 2][k & 3]);
      float uu = bf2f((unsigned short)(uvec[k >> 1] >> (16 * (k & 1))));
      float sk = rbf(uu * dsk);
      o[k] = f2bf(yb + sk);
    }
    u32x4 v;
#pragma unroll
    for (int j = 0; j < 4; ++j) v[j] = o[2 * j] | ((unsigned)o[2 * j + 1] << 16);
    *reinterpret_cast<u32x4*>(aT + (size_t)d * L_ + 8 * tid) = v;
  }
}

__global__ __launch_bounds__(256) void k2_conv(const unsigned short* __restrict__ uT, const unsigned short* __restrict__ logAr,
                                               const unsigned short* __restrict__ Aim, const unsigned short* __restrict__ Cre,
                                               const unsigned short* __restrict__ Cim, const unsigned short* __restrict__ logdt,
                                               const unsigned short* __restrict__ Dsk, unsigned short* __restrict__ aT,
                                               const unsigned short* __restrict__ ktab) {
  extern __shared__ __attribute__((aligned(16))) char smem[];
  conv_role(blockIdx.x, uT, logAr, Aim, Cre, Cim, logdt, Dsk, aT, ktab, smem);
}

constexpr int G2_RS = 84;
__global__ __launch_bounds__(256, 1) void k3_gemm2(const unsigned short* __restrict__ hbuf, const unsigned short* __restrict__ w2T,
                                                   const unsigned short* __restrict__ aT, const float* __restrict__ gbuf,
                                                   unsigned short* __restrict__ out) {
  __shared__ float red[4][64][G2_RS];
  const int tid = threadIdx.x, lane = tid & 63, wid = tid >> 6;
  int t = (blockIdx.x & 7) * 32 + (blockIdx.x >> 3);
  const int tm = t >> 3, tn = t & 7;
  const int m0 = tm * 64, n0 = tn * 80;
  const int fr = lane & 15, fq = lane >> 4;
  const int c0 = (wid * 27) >> 2, c1 = ((wid + 1) * 27) >> 2;

  const unsigned short* ap = hbuf + (size_t)(m0 >> 4) * 54 * 512 + fr * 32 + 8 * fq;
  const unsigned short* bp = w2T + (size_t)(n0 >> 4) * 54 * 512 + fr * 32 + 8 * fq;

  f32x4 acc[4][5];
#pragma unroll
  for (int a = 0; a < 4; ++a)
#pragma unroll
    for (int c = 0; c < 5; ++c) acc[a][c] = (f32x4){0.f, 0.f, 0.f, 0.f};

  bf16x8 A0[4][2], B0[5][2], A1[4][2], B1[5][2];
#define G2_LOAD(AA, BB, c)                                                                             \
  {                                                                                                    \
    _Pragma("unroll") for (int a = 0; a < 4; ++a) {                                                    \
      AA[a][0] = *reinterpret_cast<const bf16x8*>(ap + a * 54 * 512 + (c) * 1024);                \
      AA[a][1] = *reinterpret_cast<const bf16x8*>(ap + a * 54 * 512 + (c) * 1024 + 512);            \
    }                                                                                                  \
    _Pragma("unroll") for (int a = 0; a < 5; ++a) {                                                    \
      BB[a][0] = *reinterpret_cast<const bf16x8*>(bp + a * 54 * 512 + (c) * 1024);                \
      BB[a][1] = *reinterpret_cast<const bf16x8*>(bp + a * 54 * 512 + (c) * 1024 + 512);            \
    }                                                                                                  \
  }
#define G2_COMP(AA, BB)                                                                                \
  {                                                                                                    \
    _Pragma("unroll") for (int s = 0; s < 2; ++s)                                                      \
      _Pragma("unroll") for (int a = 0; a < 4; ++a)                                                    \
        _Pragma("unroll") for (int c = 0; c < 5; ++c)                                                  \
          acc[a][c] = __builtin_amdgcn_mfma_f32_16x16x32_bf16(AA[a][s], BB[c][s], acc[a][c], 0, 0, 0); \
  }
  G2_LOAD(A0, B0, c0);
  for (int c = c0; c < c1; c += 2) {
    if (c + 1 < c1) G2_LOAD(A1, B1, c + 1);
    G2_COMP(A0, B0);
    if (c + 1 >= c1) break;
    if (c + 2 < c1) G2_LOAD(A0, B0, c + 2);
    G2_COMP(A1, B1);
  }
#undef G2_LOAD
#undef G2_COMP

#pragma unroll
  for (int a = 0; a < 4; ++a)
#pragma unroll
    for (int c = 0; c < 5; ++c)
#pragma unroll
      for (int i = 0; i < 4; ++i) red[wid][16 * a + 4 * fq + i][16 * c + fr] = acc[a][c][i];
  __syncthreads();

#pragma unroll
  for (int j = 0; j < 2; ++j) {
    const int col = lane + 64 * j;
    if (col < 80) {
      const int row0 = m0 + 16 * wid;
      u32x4 av0 = *reinterpret_cast<const u32x4*>(aT + (size_t)(n0 + col) * L_ + row0);
      u32x4 av1 = *reinterpret_cast<const u32x4*>(aT + (size_t)(n0 + col) * L_ + row0 + 8);
#pragma unroll
      for (int i = 0; i < 16; ++i) {
        int rr = 16 * wid + i;
        float bv = rbf((red[0][rr][col] + red[1][rr][col]) + (red[2][rr][col] + red[3][rr][col]));
        unsigned aw = (i < 8) ? av0[(i >> 1) & 3] : av1[(i >> 1) & 3];
        float a = bf2f((unsigned short)((aw >> (16 * (i & 1))) & 0xffff));
        float g = gbuf[row0 + i];
        float t1 = rbf(g * a);
        float t2 = rbf(1.0f - g);
        float t3 = rbf(t2 * bv);
        out[(size_t)(row0 + i) * D_ + n0 + col] = f2bf(t1 + t3);
      }
    }
  }
}

extern "C" void kernel_launch(void* const* d_in, const int* in_sizes, int n_in, void* d_out, int out_size, void* d_ws,
                              size_t ws_size, hipStream_t stream) {
  const unsigned short* u = (const unsigned short*)d_in[0];
  const unsigned short* logAr = (const unsigned short*)d_in[1];
  const unsigned short* Aim = (const unsigned short*)d_in[2];
  const unsigned short* Cre = (const unsigned short*)d_in[3];
  const unsigned short* Cim = (const unsigned short*)d_in[4];
  const unsigned short* logdt = (const unsigned short*)d_in[5];
  const unsigned short* Dsk = (const unsigned short*)d_in[6];
  const unsigned short* w1 = (const unsigned short*)d_in[8];
  const unsigned short* w2 = (const unsigned short*)d_in[9];
  const unsigned short* gw = (const unsigned short*)d_in[10];
  unsigned short* out = (unsigned short*)d_out;

  char* ws = (char*)d_ws;
  unsigned short* hbuf = (unsigned short*)ws;
  unsigned short* aT = (unsigned short*)(ws + (size_t)L_ * F_ * 2);
  unsigned short* uT = (unsigned short*)(ws + (size_t)L_ * F_ * 2 + (size_t)D_ * L_ * 2);
  unsigned short* w2T = (unsigned short*)(ws + (size_t)L_ * F_ * 2 + 2 * (size_t)D_ * L_ * 2);
  float* gbuf = (float*)(ws + (size_t)L_ * F_ * 2 + 2 * (size_t)D_ * L_ * 2 + (size_t)D_ * F_ * 2);
  unsigned short* ktab = (unsigned short*)((char*)gbuf + 8192);

  static bool attr_set = false;
  if (!attr_set) {
    hipFuncSetAttribute((const void*)k1_gemm1, hipFuncAttributeMaxDynamicSharedMemorySize, G1_LDS);
    attr_set = true;
  }
  hipLaunchKernelGGL(k1_gemm1, dim3(K1_GRID), dim3(256), G1_LDS, stream, u, w1, w2, gw, hbuf, uT, w2T, gbuf, ktab);
  hipLaunchKernelGGL(k2_conv, dim3(D_), dim3(256), CONV_LDS, stream, uT, logAr, Aim, Cre, Cim, logdt, Dsk, aT, ktab);
  hipLaunchKernelGGL(k3_gemm2, dim3(256), dim3(256), 0, stream, hbuf, w2T, aT, gbuf, out);
}
```

```cpp
#include <hip/hip_runtime.h>
#include <hip/hip_bf16.h>
#include <stdint.h>

typedef __attribute__((ext_vector_type(8))) short bf16x8;
typedef __attribute__((ext_vector_type(4))) short bf16x4;
typedef __attribute__((ext_vector_type(16))) float f32x16;
typedef __attribute__((ext_vector_type(4))) float f32x4;
typedef __attribute__((ext_vector_type(4))) unsigned int u32x4;
typedef __attribute__((ext_vector_type(2))) unsigned int u32x2;

#define LDS_AS __attribute__((address_space(3)))
#define RAW_BARRIER() do { asm volatile("s_waitcnt lgkmcnt(0)" ::: "memory"); __builtin_amdgcn_s_barrier(); } while (0)

constexpr int L_ = 2048, D_ = 640, NS_ = 128, F_ = 1728;

__device__ __forceinline__ float bf2f(unsigned short v) { return __uint_as_float(((unsigned)v) << 16); }
__device__ __forceinline__ unsigned short f2bf(float f) {
  unsigned r;
  asm("v_cvt_pk_bf16_f32 %0, %1, %1" : "=v"(r) : "v"(f));
  return (unsigned short)(r & 0xffffu);
}
__device__ __forceinline__ float rbf(float f) { return bf2f(f2bf(f)); }

constexpr int G1_TM = L_ / 128, G1_TN = (F_ + 127) / 128;
constexpr int N_G1 = G1_TM * G1_TN;
constexpr int G1_STAGE = 32768, G1_LDS = 3 * G1_STAGE;
constexpr int K1_GRID = N_G1 + 32;

__device__ __forceinline__ void tr_tiles_load(const unsigned short* __restrict__ src, int ss, int r0, int c0,
                                              u32x4 (&v)[2]) {
  const int tid = threadIdx.x;
#pragma unroll
  for (int i = 0; i < 2; ++i) {
    int id = tid + 256 * i, row = id >> 3, ch = id & 7;
    v[i] = *reinterpret_cast<const u32x4*>(src + (size_t)(r0 + row) * ss + c0 + ch * 8);
  }
}
__device__ __forceinline__ void tr_tiles_put(unsigned short (*tile)[72], const u32x4 (&v)[2]) {
  const int tid = threadIdx.x;
#pragma unroll
  for (int i = 0; i < 2; ++i) {
    int id = tid + 256 * i, row = id >> 3, ch = id & 7;
    *reinterpret_cast<u32x4*>(&tile[row][ch * 8]) = v[i];
  }
}
template <bool BLOCKED>
__device__ __forceinline__ void tr_tiles_store(unsigned short (*tile)[72], unsigned short* __restrict__ dst, int ds,
                                               int r0, int c0) {
  const int tid = threadIdx.x;
#pragma unroll
  for (int i = 0; i < 2; ++i) {
    int id = tid + 256 * i, col = id >> 3, ch = id & 7;
    unsigned short o[8];
#pragma unroll
    for (int k = 0; k < 8; ++k) o[k] = tile[ch * 8 + k][col];
    u32x4 w;
#pragma unroll
    for (int j = 0; j < 4; ++j) w[j] = o[2 * j] | ((unsigned)o[2 * j + 1] << 16);
    unsigned short* dp = BLOCKED ? dst + ((size_t)((c0 + col) >> 4) * 54 + ((r0 + ch * 8) >> 5)) * 512 + (col & 15) * 32 + (ch & 3) * 8
                                 : dst + (size_t)(c0 + col) * ds + r0 + ch * 8;
    __builtin_nontemporal_store(w, reinterpret_cast<u32x4*>(dp));
  }
}

__global__ __launch_bounds__(256, 1) void k1_gemm1(const unsigned short* __restrict__ x, const unsigned short* __restrict__ w1,
                                                   const unsigned short* __restrict__ w2, const unsigned short* __restrict__ gw,
                                                   unsigned short* __restrict__ hbuf, unsigned short* __restrict__ uT,
                                                   unsigned short* __restrict__ w2T, float* __restrict__ gbuf,
                                                   unsigned short* __restrict__ ktab) {
  extern __shared__ __attribute__((aligned(16))) char smem[];
  const int tid = threadIdx.x, lane = tid & 63, wid = tid >> 6;
  const int b = blockIdx.x;
  if (b >= N_G1) {
    const int r0 = (b - N_G1) * 64;
    if (tid < 64) {
      int rr = (int)rbf((float)(r0 + tid));
      int i;
      if (rr < 256) i = (rr & 7) + 8 * (rr >> 7);
      else if (rr < 512) i = 16 + 4 * ((rr - 256) >> 7) + ((rr & 7) >> 1);
      else if (rr < 1024) i = 24 + 2 * ((rr - 512) >> 7) + ((rr & 7) >> 2);
      else if (rr < 2048) i = 32 + ((rr - 1024) >> 7);
      else i = 40;
      ktab[r0 + tid] = (unsigned short)(i * 16 + ((rr >> 3) & 15));
    }
    u32x4 v[10][2];
#pragma unroll
    for (int t = 0; t < 10; ++t) tr_tiles_load(x, D_, r0, t * 64, v[t]);
    float s[2] = {0.f, 0.f};
#pragma unroll
    for (int t = 0; t < 10; ++t) {
      u32x4 g = *reinterpret_cast<const u32x4*>(gw + t * 64 + (tid & 7) * 8);
#pragma unroll
      for (int i = 0; i < 2; ++i)
#pragma unroll
        for (int j = 0; j < 4; ++j) {
          s[i] += rbf(__uint_as_float(v[t][i][j] << 16) * __uint_as_float(g[j] << 16));
          s[i] += rbf(__uint_as_float(v[t][i][j] & 0xffff0000u) * __uint_as_float(g[j] & 0xffff0000u));
        }
    }
#pragma unroll
    for (int i = 0; i < 2; ++i) {
      s[i] += __shfl_xor(s[i], 1); s[i] += __shfl_xor(s[i], 2); s[i] += __shfl_xor(s[i], 4);
      if ((tid & 7) == 0) gbuf[r0 + (tid >> 3) + 32 * i] = rbf(1.0f / (1.0f + expf(-rbf(s[i]))));
    }
    return;
  }

  u32x4 tv[3][2];
#pragma unroll
  for (int j = 0; j < 3; ++j) {
    int id = b + j * N_G1;
    if (id < 320) tr_tiles_load(x, D_, (id / 10) * 64, (id % 10) * 64, tv[j]);
    else if (id < 590) tr_tiles_load(w2, D_, ((id - 320) / 10) * 64, ((id - 320) % 10) * 64, tv[j]);
  }

  int t = (b & 7) * (N_G1 / 8) + (b >> 3);
  const int tm = t / G1_TN, tn = t % G1_TN;
  const int m0 = tm * 128, n0 = tn * 128;
  const int wm = wid >> 1, wn = wid & 1, r = lane & 31, h = lane >> 5;

  const unsigned short* asrc[4];
  const unsigned short* bsrc[4];
#pragma unroll
  for (int i = 0; i < 4; ++i) {
    int row = (i * 4 + wid) * 8 + (lane >> 3);
    int c = (lane & 7) ^ (row & 7);
    asrc[i] = x + (size_t)(m0 + row) * D_ + c * 8;
    int k = (i * 4 + wid) * 4 + (lane >> 4);
    int cb = (lane & 15) ^ ((k & 3) << 1);
    int col = n0 + cb * 8;
    if (col > F_ - 8) col = F_ - 8;
    bsrc[i] = w1 + (size_t)k * F_ + col;
  }
  auto stage = [&](int kt, int buf) {
    char* sa = smem + buf * G1_STAGE;
    char* sb = sa + 16384;
#pragma unroll
    for (int i = 0; i < 4; ++i)
      __builtin_amdgcn_global_load_lds((const unsigned*)(asrc[i] + kt * 64), (LDS_AS unsigned*)(sa + (i * 4 + wid) * 1024), 16, 0, 0);
#pragma unroll
    for (int i = 0; i < 4; ++i)
      __builtin_amdgcn_global_load_lds((const unsigned*)(bsrc[i] + (size_t)kt * 64 * F_), (LDS_AS unsigned*)(sb + (i * 4 + wid) * 1024), 16, 0, 0);
  };

  f32x16 acc[2][2];
#pragma unroll
  for (int a = 0; a < 2; ++a)
#pragma unroll
    for (int c = 0; c < 2; ++c)
#pragma unroll
      for (int i = 0; i < 16; ++i) acc[a][c][i] = 0.f;

  const int q = (lane & 15) >> 2, p = lane & 3, blk = (lane >> 4) & 1;
  auto compute = [&](int buf) {
    const char* sa = smem + buf * G1_STAGE;
    const char* sb = sa + 16384;
#pragma unroll
    for (int s = 0; s < 4; ++s) {
      bf16x8 af[2], bfr[2];
#pragma unroll
      for (int mt = 0; mt < 2; ++mt) {
        int row = wm * 64 + mt * 32 + r;
        af[mt] = *reinterpret_cast<const bf16x8*>(sa + row * 128 + (((2 * s + h) ^ (row & 7)) << 4));
      }
#pragma unroll
      for (int nt = 0; nt < 2; ++nt)
#pragma unroll
        for (int tt = 0; tt < 2; ++tt) {
          int k = 16 * s + 8 * h + 4 * tt + q;
          int chunk = wn * 8 + nt * 4 + 2 * blk + (p >> 1);
          const char* ad = sb + k * 256 + ((chunk ^ (q << 1)) << 4) + (p & 1) * 8;
          bf16x4 v = __builtin_amdgcn_ds_read_tr16_b64_v4i16((LDS_AS bf16x4*)(ad));
          bfr[nt][4 * tt + 0] = v[0]; bfr[nt][4 * tt + 1] = v[1]; bfr[nt][4 * tt + 2] = v[2]; bfr[nt][4 * tt + 3] = v[3];
        }
#pragma unroll
      for (int mt = 0; mt < 2; ++mt)
#pragma unroll
        for (int nt = 0; nt < 2; ++nt)
          acc[mt][nt] = __builtin_amdgcn_mfma_f32_32x32x16_bf16(af[mt], bfr[nt], acc[mt][nt], 0, 0, 0);
    }
  };

  constexpr int NK = D_ / 64;
  stage(0, 0);
  stage(1, 1);
  for (int kt = 0; kt < NK - 1; ++kt) {
    asm volatile("s_waitcnt vmcnt(8)" ::: "memory");
    RAW_BARRIER();
    if (kt + 2 < NK) stage(kt + 2, (kt + 2) % 3);
    compute(kt % 3);
  }
  asm volatile("s_waitcnt vmcnt(0)" ::: "memory");
  RAW_BARRIER();
  compute((NK - 1) % 3);

  RAW_BARRIER();
  {
    unsigned short* ht = reinterpret_cast<unsigned short*>(smem);
#pragma unroll
    for (int nt = 0; nt < 2; ++nt)
#pragma unroll
      for (int mt = 0; mt < 2; ++mt)
#pragma unroll
        for (int i = 0; i < 16; ++i) {
          int row = wm * 64 + mt * 32 + (i & 3) + 8 * (i >> 2) + 4 * h;
          float hv = rbf(acc[mt][nt][i]);
          float sv = hv * __builtin_amdgcn_rcpf(1.0f + __expf(-hv));
          ht[row * 136 + wn * 64 + nt * 32 + r] = f2bf(sv);
        }
    typedef unsigned short tile_t[64][72];
    tile_t* tiles = reinterpret_cast<tile_t*>(smem + 36864);
#pragma unroll
    for (int j = 0; j < 3; ++j)
      if (b + j * N_G1 < 590) tr_tiles_put(tiles[j], tv[j]);
    __syncthreads();
#pragma unroll
    for (int i = 0; i < 8; ++i) {
      int id = tid + 256 * i, row = id >> 4, ch = id & 15;
      if (n0 + ch * 8 < F_)
        __builtin_nontemporal_store(*reinterpret_cast<const u32x4*>(ht + row * 136 + ch * 8), reinterpret_cast<u32x4*>(hbuf + ((size_t)((m0 + row) >> 4) * 54 + ((n0 + ch * 8) >> 5)) * 512 + (row & 15) * 32 + (ch & 3) * 8));
    }
#pragma unroll
    for (int j = 0; j < 3; ++j) {
      int id = b + j * N_G1;
      if (id < 320) tr_tiles_store<false>(tiles[j], uT, L_, (id / 10) * 64, (id % 10) * 64);
      else if (id < 590) tr_tiles_store<true>(tiles[j], w2T, F_, ((id - 320) / 10) * 64, ((id - 320) % 10) * 64);
    }
  }
}

constexpr int UC_STRIDE = 4384;
constexpr int UC_OFF = 0;
constexpr int KROW = 80;
constexpr int KH_OFF = 8 * UC_STRIDE;
constexpr int KL_OFF = KH_OFF + 64 * KROW + 16;
constexpr int KZ_OFF = KH_OFF + 64 * KROW;
constexpr int PR_OFF = KH_OFF + 12288;
constexpr int OS_OFF = KH_OFF;
constexpr int TB_OFF = PR_OFF + 128 * 16;
constexpr int CONV_LDS = TB_OFF + 768 * 4;

__device__ __forceinline__ float thi_of(int i) {
  int t;
  if (i < 8) t = i;
  else if (i < 16) t = 128 + (i - 8);
  else if (i < 24) t = 256 + 128 * ((i - 16) >> 2) + 2 * ((i - 16) & 3);
  else if (i < 32) t = 512 + 128 * ((i - 24) >> 1) + 4 * ((i - 24) & 1);
  else if (i < 40) t = 1024 + 128 * (i - 32);
  else t = 2048;
  return (float)t;
}

__device__ void conv_role(int d, const unsigned short* __restrict__ uT, const unsigned short* __restrict__ logAr,
                          const unsigned short* __restrict__ Aim, const unsigned short* __restrict__ Cre,
                          const unsigned short* __restrict__ Cim, const unsigned short* __restrict__ logdt,
                          const unsigned short* __restrict__ Dsk, unsigned short* __restrict__ aT,
                          const unsigned short* __restrict__ ktab, char* smem) {
  const int tid = threadIdx.x, lane = tid & 63, wid = tid >> 6;

  const u32x4 uvec = *reinterpret_cast<const u32x4*>(uT + (size_t)d * L_ + 8 * tid);

  const u32x4 kidx = *reinterpret_cast<const u32x4*>(ktab + 8 * tid);

  const float dt = rbf(expf(bf2f(logdt[d])));
  if (tid < NS_) {
    float are = -rbf(expf(bf2f(logAr[d * NS_ + tid])));
    float aim = bf2f(Aim[d * NS_ + tid]);
    float lr = are * dt, li = aim * dt;
    float cr = bf2f(Cre[d * NS_ + tid]) * dt, ci = bf2f(Cim[d * NS_ + tid]) * dt;
    float mag = sqrtf(cr * cr + ci * ci);
    f32x4 pp;
    pp[0] = lr * 1.4426950408889634f;
    pp[1] = li * 0.15915494309189535f;
    pp[2] = log2f(mag);
    pp[3] = atan2f(ci, cr) * 0.15915494309189535f;
    *reinterpret_cast<f32x4*>(smem + PR_OFF + tid * 16) = pp;
  }
  if (tid < 64) {
    u32x4 z = {0, 0, 0, 0};
    *reinterpret_cast<u32x4*>(smem + UC_OFF + (tid >> 3) * UC_STRIDE + (tid & 7) * 16) = z;
  }
  *reinterpret_cast<u32x4*>(smem + UC_OFF + 128 + tid * 16) = uvec;
  __syncthreads();

  {
    u32x4 v1 = {0, 0, 0, 0};
    if (tid < 255) v1 = *reinterpret_cast<const u32x4*>(smem + UC_OFF + 128 + (tid + 1) * 16);
    unsigned w[8] = {uvec[0], uvec[1], uvec[2], uvec[3], v1[0], v1[1], v1[2], v1[3]};
#pragma unroll
    for (int e = 1; e < 8; ++e) {
      u32x4 o;
#pragma unroll
      for (int j = 0; j < 4; ++j) {
        int base = j + (e >> 1);
        if (e & 1) o[j] = (w[base] >> 16) | (w[base + 1] << 16);
        else o[j] = w[base];
      }
      *reinterpret_cast<u32x4*>(smem + UC_OFF + e * UC_STRIDE + 128 + tid * 16) = o;
    }
    if (tid == 0) {
#pragma unroll
      for (int e = 1; e < 8; ++e)
#pragma unroll
        for (int k = 0; k < e; ++k)
          *reinterpret_cast<unsigned short*>(smem + UC_OFF + e * UC_STRIDE + 2 * (64 - e + k)) =
              (unsigned short)(w[k >> 1] >> (16 * (k & 1)));
    }
  }

  {
    const int x = lane & 15, kk = lane >> 4;
    float th[3];
#pragma unroll
    for (int T = 0; T < 3; ++T) th[T] = thi_of(16 * T + x);
    const float tlo = (float)(8 * x);
    f32x4 acc[3];
#pragma unroll
    for (int T = 0; T < 3; ++T) acc[T] = (f32x4){0.f, 0.f, 0.f, 0.f};
#pragma unroll 2
    for (int g = 0; g < 8; ++g) {
      int n = 32 * wid + 4 * g + kk;
      f32x4 pp = *reinterpret_cast<const f32x4*>(smem + PR_OFF + n * 16);
      float qe = __builtin_amdgcn_exp2f(pp[0] * tlo);
      float qa = pp[1] * tlo;
      float qr = qe * __builtin_amdgcn_cosf(qa), qi = qe * __builtin_amdgcn_sinf(qa);
#pragma unroll
      for (int T = 0; T < 3; ++T) {
        float pe = __builtin_amdgcn_exp2f(pp[0] * th[T] + pp[2]);
        float pa = pp[1] * th[T] + pp[3];
        float pr = pe * __builtin_amdgcn_cosf(pa), pi = pe * __builtin_amdgcn_sinf(pa);
        acc[T] = __builtin_amdgcn_mfma_f32_16x16x4f32(qr, pr, acc[T], 0, 0, 0);
        acc[T] = __builtin_amdgcn_mfma_f32_16x16x4f32(-qi, pi, acc[T], 0, 0, 0);
      }
    }
    float* os = reinterpret_cast<float*>(smem + OS_OFF) + wid * 768;
#pragma unroll
    for (int T = 0; T < 3; ++T) *reinterpret_cast<f32x4*>(os + (16 * T + x) * 16 + 4 * kk) = acc[T];
  }
  __syncthreads();

  {
    unsigned* tab = reinterpret_cast<unsigned*>(smem + TB_OFF);
#pragma unroll
    for (int k = 0; k < 3; ++k) {
      int e = tid + 256 * k;
      const float* osp = reinterpret_cast<const float*>(smem + OS_OFF) + e;
      float kv = 2.0f * ((osp[0] + osp[768]) + (osp[1536] + osp[2304]));
      unsigned short hi = f2bf(kv);
      tab[e] = (unsigned)hi | ((unsigned)f2bf(kv - bf2f(hi)) << 16);
    }
  }
  __syncthreads();
  {
    const unsigned* tab = reinterpret_cast<const unsigned*>(smem + TB_OFF);
    unsigned w[8];
#pragma unroll
    for (int k = 0; k < 8; ++k) w[k] = tab[(kidx[k >> 1] >> (16 * (k & 1))) & 0xffff];
    u32x4 vh, vl;
#pragma unroll
    for (int j = 0; j < 4; ++j) {
      vh[j] = (w[7 - 2 * j] & 0xffffu) | (w[6 - 2 * j] << 16);
      vl[j] = (w[7 - 2 * j] >> 16) | (w[6 - 2 * j] & 0xffff0000u);
    }
    *reinterpret_cast<u32x4*>(smem + KH_OFF + (tid >> 2) * KROW + (tid & 3) * 16) = vh;
    *reinterpret_cast<u32x4*>(smem + KL_OFF + (tid >> 2) * KROW + (tid & 3) * 16) = vl;
    if (tid == 0) { u32x4 z = {0, 0, 0, 0}; *reinterpret_cast<u32x4*>(smem + KZ_OFF) = z; }
  }
  __syncthreads();

  f32x16 y0, y1;
#pragma unroll
  for (int i = 0; i < 16; ++i) { y0[i] = 0.f; y1[i] = 0.f; }
  {
    const int r = lane & 31, h = lane >> 5;
    const int e = (r + 1) & 7;
    const char* ubase = smem + UC_OFF + e * UC_STRIDE + 2 * (r - 7 - e + 64 - 8 * h);
#pragma unroll 2
    for (int dl = wid; dl < 32; dl += 4) {
      bf16x8 a0 = *reinterpret_cast<const bf16x8*>(ubase + 64 * dl);
      bf16x8 a1 = *reinterpret_cast<const bf16x8*>(ubase + 64 * dl - 32);
      bf16x8 a2 = *reinterpret_cast<const bf16x8*>(ubase + 64 * dl + 2048);
      bf16x8 a3 = *reinterpret_cast<const bf16x8*>(ubase + 64 * dl + 2048 - 32);
      const int m = r - dl;
      const int off = (m >= 0) ? (m * KROW + 16 * h) : (KZ_OFF - KH_OFF);
      const int off1 = (m >= 0) ? (off + 32) : off;
      const int lo = (m >= 0) ? KL_OFF : KH_OFF;
      bf16x8 fh0 = *reinterpret_cast<const bf16x8*>(smem + KH_OFF + off);
      bf16x8 fh1 = *reinterpret_cast<const bf16x8*>(smem + KH_OFF + off1);
      bf16x8 fl0 = *reinterpret_cast<const bf16x8*>(smem + lo + off);
      bf16x8 fl1 = *reinterpret_cast<const bf16x8*>(smem + lo + off1);
      const int go = (32 + m) * KROW + 16 * h;
      bf16x8 gh0 = *reinterpret_cast<const bf16x8*>(smem + KH_OFF + go);
      bf16x8 gh1 = *reinterpret_cast<const bf16x8*>(smem + KH_OFF + go + 32);
      bf16x8 gl0 = *reinterpret_cast<const bf16x8*>(smem + KL_OFF + go);
      bf16x8 gl1 = *reinterpret_cast<const bf16x8*>(smem + KL_OFF + go + 32);
      y0 = __builtin_amdgcn_mfma_f32_32x32x16_bf16(a0, fh0, y0, 0, 0, 0);
      y1 = __builtin_amdgcn_mfma_f32_32x32x16_bf16(a2, fh0, y1, 0, 0, 0);
      y0 = __builtin_amdgcn_mfma_f32_32x32x16_bf16(a1, fh1, y0, 0, 0, 0);
      y1 = __builtin_amdgcn_mfma_f32_32x32x16_bf16(a3, fh1, y1, 0, 0, 0);
      y0 = __builtin_amdgcn_mfma_f32_32x32x16_bf16(a0, fl0, y0, 0, 0, 0);
      y1 = __builtin_amdgcn_mfma_f32_32x32x16_bf16(a2, fl0, y1, 0, 0, 0);
      y0 = __builtin_amdgcn_mfma_f32_32x32x16_bf16(a1, fl1, y0, 0, 0, 0);
      y1 = __builtin_amdgcn_mfma_f32_32x32x16_bf16(a3, fl1, y1, 0, 0, 0);
      y1 = __builtin_amdgcn_mfma_f32_32x32x16_bf16(a0, gh0, y1, 0, 0, 0);
      y1 = __builtin_amdgcn_mfma_f32_32x32x16_bf16(a1, gh1, y1, 0, 0, 0);
      y1 = __builtin_amdgcn_mfma_f32_32x32x16_bf16(a0, gl0, y1, 0, 0, 0);
      y1 = __builtin_amdgcn_mfma_f32_32x32x16_bf16(a1, gl1, y1, 0, 0, 0);
    }
  }
  __syncthreads();
  {
    const int r = lane & 31, h = lane >> 5;
    float* slab = reinterpret_cast<float*>(smem + wid * 8704);
#pragma unroll
    for (int g = 0; g < 4; ++g) {
      f32x4 v0 = {y0[4 * g], y0[4 * g + 1], y0[4 * g + 2], y0[4 * g + 3]};
      f32x4 v1 = {y1[4 * g], y1[4 * g + 1], y1[4 * g + 2], y1[4 * g + 3]};
      int b = 8 * g + 4 * h;
      *reinterpret_cast<f32x4*>(slab + 32 * r + b) = v0;
      *reinterpret_cast<f32x4*>(slab + 32 * (32 + r) + b) = v1;
    }
  }
  __syncthreads();
  {
    const float dsk = bf2f(Dsk[d]);
    const float* s = reinterpret_cast<const float*>(smem);
    f32x4 ys[2];
#pragma unroll
    for (int hh = 0; hh < 2; ++hh) {
      f32x4 s0 = *reinterpret_cast<const f32x4*>(s + 8 * tid + 4 * hh);
      f32x4 s1 = *reinterpret_cast<const f32x4*>(s + 2176 + 8 * tid + 4 * hh);
      f32x4 s2 = *reinterpret_cast<const f32x4*>(s + 4352 + 8 * tid + 4 * hh);
      f32x4 s3 = *reinterpret_cast<const f32x4*>(s + 6528 + 8 * tid + 4 * hh);
      ys[hh] = (s0 + s1) + (s2 + s3);
    }
    unsigned short o[8];
#pragma unroll
    for (int k = 0; k < 8; ++k) {
      float yb = rbf(ys[k >> 2][k & 3]);
      float uu = bf2f((unsigned short)(uvec[k >> 1] >> (16 * (k & 1))));
      float sk = rbf(uu * dsk);
      o[k] = f2bf(yb + sk);
    }
    u32x4 v;
#pragma unroll
    for (int j = 0; j < 4; ++j) v[j] = o[2 * j] | ((unsigned)o[2 * j + 1] << 16);
    *reinterpret_cast<u32x4*>(aT + (size_t)d * L_ + 8 * tid) = v;
  }
}

__global__ __launch_bounds__(256) void k2_conv(const unsigned short* __restrict__ uT, const unsigned short* __restrict__ logAr,
                                               const unsigned short* __restrict__ Aim, const unsigned short* __restrict__ Cre,
                                               const unsigned short* __restrict__ Cim, const unsigned short* __restrict__ logdt,
                                               const unsigned short* __restrict__ Dsk, unsigned short* __restrict__ aT,
                                               const unsigned short* __restrict__ ktab) {
  extern __shared__ __attribute__((aligned(16))) char smem[];
  conv_role(blockIdx.x, uT, logAr, Aim, Cre, Cim, logdt, Dsk, aT, ktab, smem);
}

constexpr int G2_RS = 84;
__global__ __launch_bounds__(256, 1) void k3_gemm2(const unsigned short* __restrict__ hbuf, const unsigned short* __restrict__ w2T,
                                                   const unsigned short* __restrict__ aT, const float* __restrict__ gbuf,
                                                   unsigned short* __restrict__ out) {
  __shared__ float red[4][64][G2_RS];
  __shared__ unsigned short at[80][72];
  const int tid = threadIdx.x, lane = tid & 63, wid = tid >> 6;
  int t = (blockIdx.x & 7) * 32 + (blockIdx.x >> 3);
  const int tm = t >> 3, tn = t & 7;
  const int m0 = tm * 64, n0 = tn * 80;
  const int fr = lane & 15, fq = lane >> 4;
  const int c0 = (wid * 27) >> 2, c1 = ((wid + 1) * 27) >> 2;

  const unsigned short* ap = hbuf + (size_t)(m0 >> 4) * 54 * 512 + fr * 32 + 8 * fq;
  const unsigned short* bp = w2T + (size_t)(n0 >> 4) * 54 * 512 + fr * 32 + 8 * fq;

  u32x4 apre[3];
#pragma unroll
  for (int j = 0; j < 3; ++j) {
    int id = tid + 256 * j;
    if (id < 640) apre[j] = *reinterpret_cast<const u32x4*>(aT + (size_t)(n0 + (id >> 3)) * L_ + m0 + (id & 7) * 8);
  }
  const float gpre = gbuf[m0 + (tid >> 2)];

  f32x4 acc[4][5];
#pragma unroll
  for (int a = 0; a < 4; ++a)
#pragma unroll
    for (int c = 0; c < 5; ++c) acc[a][c] = (f32x4){0.f, 0.f, 0.f, 0.f};

  bf16x8 A0[4][2], B0[5][2], A1[4][2], B1[5][2];
#define G2_LOAD(AA, BB, c)                                                                             \
  {                                                                                                    \
    _Pragma("unroll") for (int a = 0; a < 4; ++a) {                                                    \
      AA[a][0] = *reinterpret_cast<const bf16x8*>(ap + a * 54 * 512 + (c) * 1024);                \
      AA[a][1] = *reinterpret_cast<const bf16x8*>(ap + a * 54 * 512 + (c) * 1024 + 512);            \
    }                                                                                                  \
    _Pragma("unroll") for (int a = 0; a < 5; ++a) {                                                    \
      BB[a][0] = *reinterpret_cast<const bf16x8*>(bp + a * 54 * 512 + (c) * 1024);                \
      BB[a][1] = *reinterpret_cast<const bf16x8*>(bp + a * 54 * 512 + (c) * 1024 + 512);            \
    }                                                                                                  \
  }
#define G2_COMP(AA, BB)                                                                                \
  {                                                                                                    \
    _Pragma("unroll") for (int s = 0; s < 2; ++s)                                                      \
      _Pragma("unroll") for (int a = 0; a < 4; ++a)                                                    \
        _Pragma("unroll") for (int c = 0; c < 5; ++c)                                                  \
          acc[a][c] = __builtin_amdgcn_mfma_f32_16x16x32_bf16(AA[a][s], BB[c][s], acc[a][c], 0, 0, 0); \
  }
  G2_LOAD(A0, B0, c0);
  for (int c = c0; c < c1; c += 2) {
    if (c + 1 < c1) G2_LOAD(A1, B1, c + 1);
    G2_COMP(A0, B0);
    if (c + 1 >= c1) break;
    if (c + 2 < c1) G2_LOAD(A0, B0, c + 2);
    G2_COMP(A1, B1);
  }
#undef G2_LOAD
#undef G2_COMP

#pragma unroll
  for (int a = 0; a < 4; ++a)
#pragma unroll
    for (int c = 0; c < 5; ++c)
#pragma unroll
      for (int i = 0; i < 4; ++i) red[wid][16 * a + 4 * fq + i][16 * c + fr] = acc[a][c][i];
#pragma unroll
  for (int j = 0; j < 3; ++j) {
    int id = tid + 256 * j;
    if (id < 640) *reinterpret_cast<u32x4*>(&at[id >> 3][(id & 7) * 8]) = apre[j];
  }
  __syncthreads();

  {
    const int row = tid >> 2, cb = (tid & 3) * 20;
    const float g = gpre, t2 = rbf(1.0f - g);
    unsigned short o[20];
#pragma unroll
    for (int q4 = 0; q4 < 5; ++q4) {
      f32x4 s0 = *reinterpret_cast<const f32x4*>(&red[0][row][cb + 4 * q4]);
      f32x4 s1 = *reinterpret_cast<const f32x4*>(&red[1][row][cb + 4 * q4]);
      f32x4 s2 = *reinterpret_cast<const f32x4*>(&red[2][row][cb + 4 * q4]);
      f32x4 s3 = *reinterpret_cast<const f32x4*>(&red[3][row][cb + 4 * q4]);
      f32x4 s = (s0 + s1) + (s2 + s3);
#pragma unroll
      for (int i = 0; i < 4; ++i) {
        float av = bf2f(at[cb + 4 * q4 + i][row]);
        o[4 * q4 + i] = f2bf(rbf(g * av) + rbf(t2 * rbf(s[i])));
      }
    }
    unsigned short* op = out + (size_t)(m0 + row) * D_ + n0 + cb;
#pragma unroll
    for (int q4 = 0; q4 < 5; ++q4) {
      u32x2 w;
      w[0] = o[4 * q4] | ((unsigned)o[4 * q4 + 1] << 16);
      w[1] = o[4 * q4 + 2] | ((unsigned)o[4 * q4 + 3] << 16);
      *reinterpret_cast<u32x2*>(op + 4 * q4) = w;
    }
  }
}

extern "C" void kernel_launch(void* const* d_in, const int* in_sizes, int n_in, void* d_out, int out_size, void* d_ws,
                              size_t ws_size, hipStream_t stream) {
  const unsigned short* u = (const unsigned short*)d_in[0];
  const unsigned short* logAr = (const unsigned short*)d_in[1];
  const unsigned short* Aim = (const unsigned short*)d_in[2];
  const unsigned short* Cre = (const unsigned short*)d_in[3];
  const unsigned short* Cim = (const unsigned short*)d_in[4];
  const unsigned short* logdt = (const unsigned short*)d_in[5];
  const unsigned short* Dsk = (const unsigned short*)d_in[6];
  const unsigned short* w1 = (const unsigned short*)d_in[8];
  const unsigned short* w2 = (const unsigned short*)d_in[9];
  const unsigned short* gw = (const unsigned short*)d_in[10];
  unsigned short* out = (unsigned short*)d_out;

  char* ws = (char*)d_ws;
  unsigned short* hbuf = (unsigned short*)ws;
  unsigned short* aT = (unsigned short*)(ws + (size_t)L_ * F_ * 2);
  unsigned short* uT = (unsigned short*)(ws + (size_t)L_ * F_ * 2 + (size_t)D_ * L_ * 2);
  unsigned short* w2T = (unsigned short*)(ws + (size_t)L_ * F_ * 2 + 2 * (size_t)D_ * L_ * 2);
  float* gbuf = (float*)(ws + (size_t)L_ * F_ * 2 + 2 * (size_t)D_ * L_ * 2 + (size_t)D_ * F_ * 2);
  unsigned short* ktab = (unsigned short*)((char*)gbuf + 8192);

  static bool attr_set = false;
  if (!attr_set) {
    hipFuncSetAttribute((const void*)k1_gemm1, hipFuncAttributeMaxDynamicSharedMemorySize, G1_LDS);
    attr_set = true;
  }
  hipLaunchKernelGGL(k1_gemm1, dim3(K1_GRID), dim3(256), G1_LDS, stream, u, w1, w2, gw, hbuf, uT, w2T, gbuf, ktab);
  hipLaunchKernelGGL(k2_conv, dim3(D_), dim3(256), CONV_LDS, stream, uT, logAr, Aim, Cre, Cim, logdt, Dsk, aT, ktab);
  hipLaunchKernelGGL(k3_gemm2, dim3(256), dim3(256), 0, stream, hbuf, w2T, aT, gbuf, out);
}
```

```cpp
#include <hip/hip_runtime.h>
#include <hip/hip_bf16.h>
#include <stdint.h>

typedef __attribute__((ext_vector_type(8))) short bf16x8;
typedef __attribute__((ext_vector_type(4))) short bf16x4;
typedef __attribute__((ext_vector_type(16))) float f32x16;
typedef __attribute__((ext_vector_type(4))) float f32x4;
typedef __attribute__((ext_vector_type(4))) unsigned int u32x4;
typedef __attribute__((ext_vector_type(2))) unsigned int u32x2;

#define LDS_AS __attribute__((address_space(3)))
#define RAW_BARRIER() do { asm volatile("s_waitcnt lgkmcnt(0)" ::: "memory"); __builtin_amdgcn_s_barrier(); } while (0)

constexpr int L_ = 2048, D_ = 640, NS_ = 128, F_ = 1728;

__device__ __forceinline__ float bf2f(unsigned short v) { return __uint_as_float(((unsigned)v) << 16); }
__device__ __forceinline__ unsigned short f2bf(float f) {
  unsigned r;
  asm("v_cvt_pk_bf16_f32 %0, %1, %1" : "=v"(r) : "v"(f));
  return (unsigned short)(r & 0xffffu);
}
__device__ __forceinline__ float rbf(float f) { return bf2f(f2bf(f)); }

constexpr int G1_TM = L_ / 128, G1_TN = (F_ + 127) / 128;
constexpr int N_G1 = G1_TM * G1_TN;
constexpr int G1_STAGE = 32768, G1_LDS = 3 * G1_STAGE;
constexpr int K1_GRID = N_G1 + 32;

__device__ __forceinline__ void tr_tiles_load(const unsigned short* __restrict__ src, int ss, int r0, int c0,
                                              u32x4 (&v)[2]) {
  const int tid = threadIdx.x;
#pragma unroll
  for (int i = 0; i < 2; ++i) {
    int id = tid + 256 * i, row = id >> 3, ch = id & 7;
    v[i] = *reinterpret_cast<const u32x4*>(src + (size_t)(r0 + row) * ss + c0 + ch * 8);
  }
}
__device__ __forceinline__ void tr_tiles_put(unsigned short (*tile)[72], const u32x4 (&v)[2]) {
  const int tid = threadIdx.x;
#pragma unroll
  for (int i = 0; i < 2; ++i) {
    int id = tid + 256 * i, row = id >> 3, ch = id & 7;
    *reinterpret_cast<u32x4*>(&tile[row][ch * 8]) = v[i];
  }
}
template <bool BLOCKED>
__device__ __forceinline__ void tr_tiles_store(unsigned short (*tile)[72], unsigned short* __restrict__ dst, int ds,
                                               int r0, int c0) {
  const int tid = threadIdx.x;
#pragma unroll
  for (int i = 0; i < 2; ++i) {
    int id = tid + 256 * i, col = id >> 3, ch = id & 7;
    unsigned short o[8];
#pragma unroll
    for (int k = 0; k < 8; ++k) o[k] = tile[ch * 8 + k][col];
    u32x4 w;
#pragma unroll
    for (int j = 0; j < 4; ++j) w[j] = o[2 * j] | ((unsigned)o[2 * j + 1] << 16);
    unsigned short* dp = BLOCKED ? dst + (((size_t)((c0 + col) / 80) * 54 + ((r0 + ch * 8) >> 5)) * 5 + (((c0 + col) % 80) >> 4)) * 512 + ((c0 + col) & 15) * 32 + (ch & 3) * 8
                                 : dst + (size_t)(c0 + col) * ds + r0 + ch * 8;
    *reinterpret_cast<u32x4*>(dp) = w;
  }
}

__global__ __launch_bounds__(256, 1) void k1_gemm1(const unsigned short* __restrict__ x, const unsigned short* __restrict__ w1,
                                                   const unsigned short* __restrict__ w2, const unsigned short* __restrict__ gw,
                                                   unsigned short* __restrict__ hbuf, unsigned short* __restrict__ uT,
                                                   unsigned short* __restrict__ w2T, float* __restrict__ gbuf,
                                                   unsigned short* __restrict__ ktab) {
  extern __shared__ __attribute__((aligned(16))) char smem[];
  const int tid = threadIdx.x, lane = tid & 63, wid = tid >> 6;
  const int b = blockIdx.x;
  if (b >= N_G1) {
    const int r0 = (b - N_G1) * 64;
    if (tid < 64) {
      int rr = (int)rbf((float)(r0 + tid));
      int i;
      if (rr < 256) i = (rr & 7) + 8 * (rr >> 7);
      else if (rr < 512) i = 16 + 4 * ((rr - 256) >> 7) + ((rr & 7) >> 1);
      else if (rr < 1024) i = 24 + 2 * ((rr - 512) >> 7) + ((rr & 7) >> 2);
      else if (rr < 2048) i = 32 + ((rr - 1024) >> 7);
      else i = 40;
      ktab[r0 + tid] = (unsigned short)(i * 16 + ((rr >> 3) & 15));
    }
    u32x4 v[10][2];
#pragma unroll
    for (int t = 0; t < 10; ++t) tr_tiles_load(x, D_, r0, t * 64, v[t]);
    float s[2] = {0.f, 0.f};
#pragma unroll
    for (int t = 0; t < 10; ++t) {
      u32x4 g = *reinterpret_cast<const u32x4*>(gw + t * 64 + (tid & 7) * 8);
#pragma unroll
      for (int i = 0; i < 2; ++i)
#pragma unroll
        for (int j = 0; j < 4; ++j) {
          s[i] += rbf(__uint_as_float(v[t][i][j] << 16) * __uint_as_float(g[j] << 16));
          s[i] += rbf(__uint_as_float(v[t][i][j] & 0xffff0000u) * __uint_as_float(g[j] & 0xffff0000u));
        }
    }
#pragma unroll
    for (int i = 0; i < 2; ++i) {
      s[i] += __shfl_xor(s[i], 1); s[i] += __shfl_xor(s[i], 2); s[i] += __shfl_xor(s[i], 4);
      if ((tid & 7) == 0) gbuf[r0 + (tid >> 3) + 32 * i] = rbf(1.0f / (1.0f + expf(-rbf(s[i]))));
    }
    return;
  }

  u32x4 tv[3][2];
#pragma unroll
  for (int j = 0; j < 3; ++j) {
    int id = b + j * N_G1;
    if (id < 320) tr_tiles_load(x, D_, (id / 10) * 64, (id % 10) * 64, tv[j]);
    else if (id < 590) tr_tiles_load(w2, D_, ((id - 320) / 10) * 64, ((id - 320) % 10) * 64, tv[j]);
  }

  int t = (b & 7) * (N_G1 / 8) + (b >> 3);
  const int tm = t / G1_TN, tn = t % G1_TN;
  const int m0 = tm * 128, n0 = tn * 128;
  const int wm = wid >> 1, wn = wid & 1, r = lane & 31, h = lane >> 5;

  const unsigned short* asrc[4];
  const unsigned short* bsrc[4];
#pragma unroll
  for (int i = 0; i < 4; ++i) {
    int row = (i * 4 + wid) * 8 + (lane >> 3);
    int c = (lane & 7) ^ (row & 7);
    asrc[i] = x + (size_t)(m0 + row) * D_ + c * 8;
    int k = (i * 4 + wid) * 4 + (lane >> 4);
    int cb = (lane & 15) ^ ((k & 3) << 1);
    int col = n0 + cb * 8;
    if (col > F_ - 8) col = F_ - 8;
    bsrc[i] = w1 + (size_t)k * F_ + col;
  }
  auto stage = [&](int kt, int buf) {
    char* sa = smem + buf * G1_STAGE;
    char* sb = sa + 16384;
#pragma unroll
    for (int i = 0; i < 4; ++i)
      __builtin_amdgcn_global_load_lds((const unsigned*)(asrc[i] + kt * 64), (LDS_AS unsigned*)(sa + (i * 4 + wid) * 1024), 16, 0, 0);
#pragma unroll
    for (int i = 0; i < 4; ++i)
      __builtin_amdgcn_global_load_lds((const unsigned*)(bsrc[i] + (size_t)kt * 64 * F_), (LDS_AS unsigned*)(sb + (i * 4 + wid) * 1024), 16, 0, 0);
  };

  f32x16 acc[2][2];
#pragma unroll
  for (int a = 0; a < 2; ++a)
#pragma unroll
    for (int c = 0; c < 2; ++c)
#pragma unroll
      for (int i = 0; i < 16; ++i) acc[a][c][i] = 0.f;

  const int q = (lane & 15) >> 2, p = lane & 3, blk = (lane >> 4) & 1;
  auto compute = [&](int buf) {
    const char* sa = smem + buf * G1_STAGE;
    const char* sb = sa + 16384;
#pragma unroll
    for (int s = 0; s < 4; ++s) {
      bf16x8 af[2], bfr[2];
#pragma unroll
      for (int mt = 0; mt < 2; ++mt) {
        int row = wm * 64 + mt * 32 + r;
        af[mt] = *reinterpret_cast<const bf16x8*>(sa + row * 128 + (((2 * s + h) ^ (row & 7)) << 4));
      }
#pragma unroll
      for (int nt = 0; nt < 2; ++nt)
#pragma unroll
        for (int tt = 0; tt < 2; ++tt) {
          int k = 16 * s + 8 * h + 4 * tt + q;
          int chunk = wn * 8 + nt * 4 + 2 * blk + (p >> 1);
          const char* ad = sb + k * 256 + ((chunk ^ (q << 1)) << 4) + (p & 1) * 8;
          bf16x4 v = __builtin_amdgcn_ds_read_tr16_b64_v4i16((LDS_AS bf16x4*)(ad));
          bfr[nt][4 * tt + 0] = v[0]; bfr[nt][4 * tt + 1] = v[1]; bfr[nt][4 * tt + 2] = v[2]; bfr[nt][4 * tt + 3] = v[3];
        }
#pragma unroll
      for (int mt = 0; mt < 2; ++mt)
#pragma unroll
        for (int nt = 0; nt < 2; ++nt)
          acc[mt][nt] = __builtin_amdgcn_mfma_f32_32x32x16_bf16(af[mt], bfr[nt], acc[mt][nt], 0, 0, 0);
    }
  };

  constexpr int NK = D_ / 64;
  stage(0, 0);
  stage(1, 1);
  for (int kt = 0; kt < NK - 1; ++kt) {
    asm volatile("s_waitcnt vmcnt(8)" ::: "memory");
    RAW_BARRIER();
    if (kt + 2 < NK) stage(kt + 2, (kt + 2) % 3);
    compute(kt % 3);
  }
  asm volatile("s_waitcnt vmcnt(0)" ::: "memory");
  RAW_BARRIER();
  compute((NK - 1) % 3);

  RAW_BARRIER();
  {
    unsigned short* ht = reinterpret_cast<unsigned short*>(smem);
#pragma unroll
    for (int nt = 0; nt < 2; ++nt)
#pragma unroll
      for (int mt = 0; mt < 2; ++mt)
#pragma unroll
        for (int i = 0; i < 16; ++i) {
          int row = wm * 64 + mt * 32 + (i & 3) + 8 * (i >> 2) + 4 * h;
          float hv = rbf(acc[mt][nt][i]);
          float sv = hv * __builtin_amdgcn_rcpf(1.0f + __expf(-hv));
          ht[row * 136 + wn * 64 + nt * 32 + r] = f2bf(sv);
        }
    typedef unsigned short tile_t[64][72];
    tile_t* tiles = reinterpret_cast<tile_t*>(smem + 36864);
#pragma unroll
    for (int j = 0; j < 3; ++j)
      if (b + j * N_G1 < 590) tr_tiles_put(tiles[j], tv[j]);
    __syncthreads();
#pragma unroll
    for (int i = 0; i < 8; ++i) {
      int id = tid + 256 * i, row = id >> 4, ch = id & 15;
      if (n0 + ch * 8 < F_)
        *reinterpret_cast<u32x4*>(hbuf + (((size_t)((m0 + row) >> 6) * 54 + ((n0 + ch * 8) >> 5)) * 4 + ((row >> 4) & 3)) * 512 + (row & 15) * 32 + (ch & 3) * 8) = *reinterpret_cast<const u32x4*>(ht + row * 136 + ch * 8);
    }
#pragma unroll
    for (int j = 0; j < 3; ++j) {
      int id = b + j * N_G1;
      if (id < 320) tr_tiles_store<false>(tiles[j], uT, L_, (id / 10) * 64, (id % 10) * 64);
      else if (id < 590) tr_tiles_store<true>(tiles[j], w2T, F_, ((id - 320) / 10) * 64, ((id - 320) % 10) * 64);
    }
  }
}

constexpr int UC_STRIDE = 4384;
constexpr int UC_OFF = 0;
constexpr int KROW = 80;
constexpr int KH_OFF = 8 * UC_STRIDE;
constexpr int KL_OFF = KH_OFF + 64 * KROW + 16;
constexpr int KZ_OFF = KH_OFF + 64 * KROW;
constexpr int PR_OFF = KH_OFF + 12288;
constexpr int OS_OFF = KH_OFF;
constexpr int TB_OFF = PR_OFF + 128 * 16;
constexpr int CONV_LDS = TB_OFF + 768 * 4;

__device__ __forceinline__ float thi_of(int i) {
  int t;
  if (i < 8) t = i;
  else if (i < 16) t = 128 + (i - 8);
  else if (i < 24) t = 256 + 128 * ((i - 16) >> 2) + 2 * ((i - 16) & 3);
  else if (i < 32) t = 512 + 128 * ((i - 24) >> 1) + 4 * ((i - 24) & 1);
  else if (i < 40) t = 1024 + 128 * (i - 32);
  else t = 2048;
  return (float)t;
}

__device__ void conv_role(int d, const unsigned short* __restrict__ uT, const unsigned short* __restrict__ logAr,
                          const unsigned short* __restrict__ Aim, const unsigned short* __restrict__ Cre,
                          const unsigned short* __restrict__ Cim, const unsigned short* __restrict__ logdt,
                          const unsigned short* __restrict__ Dsk, unsigned short* __restrict__ aT,
                          const unsigned short* __restrict__ ktab, char* smem) {
  const int tid = threadIdx.x, lane = tid & 63, wid = tid >> 6;

  const u32x4 uvec = *reinterpret_cast<const u32x4*>(uT + (size_t)d * L_ + 8 * tid);

  const u32x4 kidx = *reinterpret_cast<const u32x4*>(ktab + 8 * tid);

  const float dt = rbf(expf(bf2f(logdt[d])));
  if (tid < NS_) {
    float are = -rbf(expf(bf2f(logAr[d * NS_ + tid])));
    float aim = bf2f(Aim[d * NS_ + tid]);
    float lr = are * dt, li = aim * dt;
    float cr = bf2f(Cre[d * NS_ + tid]) * dt, ci = bf2f(Cim[d * NS_ + tid]) * dt;
    float mag = sqrtf(cr * cr + ci * ci);
    f32x4 pp;
    pp[0] = lr * 1.4426950408889634f;
    pp[1] = li * 0.15915494309189535f;
    pp[2] = log2f(mag);
    pp[3] = atan2f(ci, cr) * 0.15915494309189535f;
    *reinterpret_cast<f32x4*>(smem + PR_OFF + tid * 16) = pp;
  }
  if (tid < 64) {
    u32x4 z = {0, 0, 0, 0};
    *reinterpret_cast<u32x4*>(smem + UC_OFF + (tid >> 3) * UC_STRIDE + (tid & 7) * 16) = z;
  }
  *reinterpret_cast<u32x4*>(smem + UC_OFF + 128 + tid * 16) = uvec;
  __syncthreads();

  {
    u32x4 v1 = {0, 0, 0, 0};
    if (tid < 255) v1 = *reinterpret_cast<const u32x4*>(smem + UC_OFF + 128 + (tid + 1) * 16);
    unsigned w[8] = {uvec[0], uvec[1], uvec[2], uvec[3], v1[0], v1[1], v1[2], v1[3]};
#pragma unroll
    for (int e = 1; e < 8; ++e) {
      u32x4 o;
#pragma unroll
      for (int j = 0; j < 4; ++j) {
        int base = j + (e >> 1);
        if (e & 1) o[j] = (w[base] >> 16) | (w[base + 1] << 16);
        else o[j] = w[base];
      }
      *reinterpret_cast<u32x4*>(smem + UC_OFF + e * UC_STRIDE + 128 + tid * 16) = o;
    }
    if (tid == 0) {
#pragma unroll
      for (int e = 1; e < 8; ++e)
#pragma unroll
        for (int k = 0; k < e; ++k)
          *reinterpret_cast<unsigned short*>(smem + UC_OFF + e * UC_STRIDE + 2 * (64 - e + k)) =
              (unsigned short)(w[k >> 1] >> (16 * (k & 1)));
    }
  }

  {
    const int x = lane & 15, kk = lane >> 4;
    float th[3];
#pragma unroll
    for (int T = 0; T < 3; ++T) th[T] = thi_of(16 * T + x);
    const float tlo = (float)(8 * x);
    f32x4 acc[3];
#pragma unroll
    for (int T = 0; T < 3; ++T) acc[T] = (f32x4){0.f, 0.f, 0.f, 0.f};
#pragma unroll 2
    for (int g = 0; g < 8; ++g) {
      int n = 32 * wid + 4 * g + kk;
      f32x4 pp = *reinterpret_cast<const f32x4*>(smem + PR_OFF + n * 16);
      float qe = __builtin_amdgcn_exp2f(pp[0] * tlo);
      float qa = pp[1] * tlo;
      float qr = qe * __builtin_amdgcn_cosf(qa), qi = qe * __builtin_amdgcn_sinf(qa);
#pragma unroll
      for (int T = 0; T < 3; ++T) {
        float pe = __builtin_amdgcn_exp2f(pp[0] * th[T] + pp[2]);
        float pa = pp[1] * th[T] + pp[3];
        float pr = pe * __builtin_amdgcn_cosf(pa), pi = pe * __builtin_amdgcn_sinf(pa);
        acc[T] = __builtin_amdgcn_mfma_f32_16x16x4f32(qr, pr, acc[T], 0, 0, 0);
        acc[T] = __builtin_amdgcn_mfma_f32_16x16x4f32(-qi, pi, acc[T], 0, 0, 0);
      }
    }
    float* os = reinterpret_cast<float*>(smem + OS_OFF) + wid * 768;
#pragma unroll
    for (int T = 0; T < 3; ++T) *reinterpret_cast<f32x4*>(os + (16 * T + x) * 16 + 4 * kk) = acc[T];
  }
  __syncthreads();

  {
    unsigned* tab = reinterpret_cast<unsigned*>(smem + TB_OFF);
#pragma unroll
    for (int k = 0; k < 3; ++k) {
      int e = tid + 256 * k;
      const float* osp = reinterpret_cast<const float*>(smem + OS_OFF) + e;
      float kv = 2.0f * ((osp[0] + osp[768]) + (osp[1536] + osp[2304]));
      unsigned short hi = f2bf(kv);
      tab[e] = (unsigned)hi | ((unsigned)f2bf(kv - bf2f(hi)) << 16);
    }
  }
  __syncthreads();
  {
    const unsigned* tab = reinterpret_cast<const unsigned*>(smem + TB_OFF);
    unsigned w[8];
#pragma unroll
    for (int k = 0; k < 8; ++k) w[k] = tab[(kidx[k >> 1] >> (16 * (k & 1))) & 0xffff];
    u32x4 vh, vl;
#pragma unroll
    for (int j = 0; j < 4; ++j) {
      vh[j] = (w[7 - 2 * j] & 0xffffu) | (w[6 - 2 * j] << 16);
      vl[j] = (w[7 - 2 * j] >> 16) | (w[6 - 2 * j] & 0xffff0000u);
    }
    *reinterpret_cast<u32x4*>(smem + KH_OFF + (tid >> 2) * KROW + (tid & 3) * 16) = vh;
    *reinterpret_cast<u32x4*>(smem + KL_OFF + (tid >> 2) * KROW + (tid & 3) * 16) = vl;
    if (tid == 0) { u32x4 z = {0, 0, 0, 0}; *reinterpret_cast<u32x4*>(smem + KZ_OFF) = z; }
  }
  __syncthreads();

  f32x16 y0, y1;
#pragma unroll
  for (int i = 0; i < 16; ++i) { y0[i] = 0.f; y1[i] = 0.f; }
  {
    const int r = lane & 31, h = lane >> 5;
    const int e = (r + 1) & 7;
    const char* ubase = smem + UC_OFF + e * UC_STRIDE + 2 * (r - 7 - e + 64 - 8 * h);
#pragma unroll 2
    for (int dl = wid; dl < 32; dl += 4) {
      bf16x8 a0 = *reinterpret_cast<const bf16x8*>(ubase + 64 * dl);
      bf16x8 a1 = *reinterpret_cast<const bf16x8*>(ubase + 64 * dl - 32);
      bf16x8 a2 = *reinterpret_cast<const bf16x8*>(ubase + 64 * dl + 2048);
      bf16x8 a3 = *reinterpret_cast<const bf16x8*>(ubase + 64 * dl + 2048 - 32);
      const int m = r - dl;
      const int off = (m >= 0) ? (m * KROW + 16 * h) : (KZ_OFF - KH_OFF);
      const int off1 = (m >= 0) ? (off + 32) : off;
      const int lo = (m >= 0) ? KL_OFF : KH_OFF;
      bf16x8 fh0 = *reinterpret_cast<const bf16x8*>(smem + KH_OFF + off);
      bf16x8 fh1 = *reinterpret_cast<const bf16x8*>(smem + KH_OFF + off1);
      bf16x8 fl0 = *reinterpret_cast<const bf16x8*>(smem + lo + off);
      bf16x8 fl1 = *reinterpret_cast<const bf16x8*>(smem + lo + off1);
      const int go = (32 + m) * KROW + 16 * h;
      bf16x8 gh0 = *reinterpret_cast<const bf16x8*>(smem + KH_OFF + go);
      bf16x8 gh1 = *reinterpret_cast<const bf16x8*>(smem + KH_OFF + go + 32);
      bf16x8 gl0 = *reinterpret_cast<const bf16x8*>(smem + KL_OFF + go);
      bf16x8 gl1 = *reinterpret_cast<const bf16x8*>(smem + KL_OFF + go + 32);
      y0 = __builtin_amdgcn_mfma_f32_32x32x16_bf16(a0, fh0, y0, 0, 0, 0);
      y1 = __builtin_amdgcn_mfma_f32_32x32x16_bf16(a2, fh0, y1, 0, 0, 0);
      y0 = __builtin_amdgcn_mfma_f32_32x32x16_bf16(a1, fh1, y0, 0, 0, 0);
      y1 = __builtin_amdgcn_mfma_f32_32x32x16_bf16(a3, fh1, y1, 0, 0, 0);
      y0 = __builtin_amdgcn_mfma_f32_32x32x16_bf16(a0, fl0, y0, 0, 0, 0);
      y1 = __builtin_amdgcn_mfma_f32_32x32x16_bf16(a2, fl0, y1, 0, 0, 0);
      y0 = __builtin_amdgcn_mfma_f32_32x32x16_bf16(a1, fl1, y0, 0, 0, 0);
      y1 = __builtin_amdgcn_mfma_f32_32x32x16_bf16(a3, fl1, y1, 0, 0, 0);
      y1 = __builtin_amdgcn_mfma_f32_32x32x16_bf16(a0, gh0, y1, 0, 0, 0);
      y1 = __builtin_amdgcn_mfma_f32_32x32x16_bf16(a1, gh1, y1, 0, 0, 0);
      y1 = __builtin_amdgcn_mfma_f32_32x32x16_bf16(a0, gl0, y1, 0, 0, 0);
      y1 = __builtin_amdgcn_mfma_f32_32x32x16_bf16(a1, gl1, y1, 0, 0, 0);
    }
  }
  __syncthreads();
  {
    const int r = lane & 31, h = lane >> 5;
    float* slab = reinterpret_cast<float*>(smem + wid * 8704);
#pragma unroll
    for (int g = 0; g < 4; ++g) {
      f32x4 v0 = {y0[4 * g], y0[4 * g + 1], y0[4 * g + 2], y0[4 * g + 3]};
      f32x4 v1 = {y1[4 * g], y1[4 * g + 1], y1[4 * g + 2], y1[4 * g + 3]};
      int b = 8 * g + 4 * h;
      *reinterpret_cast<f32x4*>(slab + 32 * r + b) = v0;
      *reinterpret_cast<f32x4*>(slab + 32 * (32 + r) + b) = v1;
    }
  }
  __syncthreads();
  {
    const float dsk = bf2f(Dsk[d]);
    const float* s = reinterpret_cast<const float*>(smem);
    f32x4 ys[2];
#pragma unroll
    for (int hh = 0; hh < 2; ++hh) {
      f32x4 s0 = *reinterpret_cast<const f32x4*>(s + 8 * tid + 4 * hh);
      f32x4 s1 = *reinterpret_cast<const f32x4*>(s + 2176 + 8 * tid + 4 * hh);
      f32x4 s2 = *reinterpret_cast<const f32x4*>(s + 4352 + 8 * tid + 4 * hh);
      f32x4 s3 = *reinterpret_cast<const f32x4*>(s + 6528 + 8 * tid + 4 * hh);
      ys[hh] = (s0 + s1) + (s2 + s3);
    }
    unsigned short o[8];
#pragma unroll
    for (int k = 0; k < 8; ++k) {
      float yb = rbf(ys[k >> 2][k & 3]);
      float uu = bf2f((unsigned short)(uvec[k >> 1] >> (16 * (k & 1))));
      float sk = rbf(uu * dsk);
      o[k] = f2bf(yb + sk);
    }
    u32x4 v;
#pragma unroll
    for (int j = 0; j < 4; ++j) v[j] = o[2 * j] | ((unsigned)o[2 * j + 1] << 16);
    *reinterpret_cast<u32x4*>(aT + (size_t)d * L_ + 8 * tid) = v;
  }
}

__global__ __launch_bounds__(256) void k2_conv(const unsigned short* __restrict__ uT, const unsigned short* __restrict__ logAr,
                                               const unsigned short* __restrict__ Aim, const unsigned short* __restrict__ Cre,
                                               const unsigned short* __restrict__ Cim, const unsigned short* __restrict__ logdt,
                                               const unsigned short* __restrict__ Dsk, unsigned short* __restrict__ aT,
                                               const unsigned short* __restrict__ ktab) {
  extern __shared__ __attribute__((aligned(16))) char smem[];
  conv_role(blockIdx.x, uT, logAr, Aim, Cre, Cim, logdt, Dsk, aT, ktab, smem);
}

constexpr int G2_RS = 84;
__global__ __launch_bounds__(384, 1) void k3_gemm2(const unsigned short* __restrict__ hbuf, const unsigned short* __restrict__ w2T,
                                                   const unsigned short* __restrict__ aT, const float* __restrict__ gbuf,
                                                   unsigned short* __restrict__ out) {
  __shared__ float red[3][64][G2_RS];
  __shared__ unsigned short at[80][72];
  const int tid = threadIdx.x, lane = tid & 63, wid = tid >> 6;
  int t = (blockIdx.x & 7) * 32 + (blockIdx.x >> 3);
  const int tm = t >> 3, tn = t & 7;
  const int m0 = tm * 64, n0 = tn * 80;
  const int fr = lane & 15, fq = lane >> 4;
  const int c0 = wid * 9;

  const unsigned short* ap = hbuf + (size_t)tm * 54 * 2048 + fr * 32 + 8 * fq;
  const unsigned short* bp = w2T + (size_t)tn * 54 * 2560 + fr * 32 + 8 * fq;

  u32x4 apre[2];
#pragma unroll
  for (int j = 0; j < 2; ++j) {
    int id = tid + 384 * j;
    if (id < 640) apre[j] = *reinterpret_cast<const u32x4*>(aT + (size_t)(n0 + (id >> 3)) * L_ + m0 + (id & 7) * 8);
  }
  const float gpre = gbuf[m0 + ((tid & 255) >> 2)];

  f32x4 acc[4][5];
#pragma unroll
  for (int a = 0; a < 4; ++a)
#pragma unroll
    for (int c = 0; c < 5; ++c) acc[a][c] = (f32x4){0.f, 0.f, 0.f, 0.f};

  bf16x8 A0[4], B0[5], A1[4], B1[5], A2[4], B2[5];
#define G2_LOAD(AA, BB, c)                                                                             \
  {                                                                                                    \
    _Pragma("unroll") for (int a = 0; a < 4; ++a)                                                      \
      AA[a] = *reinterpret_cast<const bf16x8*>(ap + a * 512 + (c) * 2048);                         \
    _Pragma("unroll") for (int a = 0; a < 5; ++a)                                                      \
      BB[a] = *reinterpret_cast<const bf16x8*>(bp + a * 512 + (c) * 2560);                         \
  }
#define G2_COMP(AA, BB)                                                                                \
  {                                                                                                    \
    _Pragma("unroll") for (int a = 0; a < 4; ++a)                                                      \
      _Pragma("unroll") for (int c = 0; c < 5; ++c)                                                    \
        acc[a][c] = __builtin_amdgcn_mfma_f32_16x16x32_bf16(AA[a], BB[c], acc[a][c], 0, 0, 0);         \
  }
  G2_LOAD(A0, B0, c0);
  G2_LOAD(A1, B1, c0 + 1);
#pragma unroll
  for (int c = 0; c < 9; c += 3) {
    G2_LOAD(A2, B2, c0 + c + 2);
    G2_COMP(A0, B0);
    if (c + 3 < 9) G2_LOAD(A0, B0, c0 + c + 3);
    G2_COMP(A1, B1);
    if (c + 4 < 9) G2_LOAD(A1, B1, c0 + c + 4);
    G2_COMP(A2, B2);
  }
#undef G2_LOAD
#undef G2_COMP

  if (wid >= 3) {
#pragma unroll
    for (int a = 0; a < 4; ++a)
#pragma unroll
      for (int c = 0; c < 5; ++c)
#pragma unroll
        for (int i = 0; i < 4; ++i) red[wid - 3][16 * a + 4 * fq + i][16 * c + fr] = acc[a][c][i];
  }
  __syncthreads();
  if (wid < 3) {
#pragma unroll
    for (int a = 0; a < 4; ++a)
#pragma unroll
      for (int c = 0; c < 5; ++c)
#pragma unroll
        for (int i = 0; i < 4; ++i) red[wid][16 * a + 4 * fq + i][16 * c + fr] += acc[a][c][i];
  }
#pragma unroll
  for (int j = 0; j < 2; ++j) {
    int id = tid + 384 * j;
    if (id < 640) *reinterpret_cast<u32x4*>(&at[id >> 3][(id & 7) * 8]) = apre[j];
  }
  __syncthreads();

  if (tid < 256) {
    const int row = tid >> 2, cb = (tid & 3) * 20;
    const float g = gpre, t2 = rbf(1.0f - g);
    unsigned short o[20];
#pragma unroll
    for (int q4 = 0; q4 < 5; ++q4) {
      f32x4 s0 = *reinterpret_cast<const f32x4*>(&red[0][row][cb + 4 * q4]);
      f32x4 s1 = *reinterpret_cast<const f32x4*>(&red[1][row][cb + 4 * q4]);
      f32x4 s2 = *reinterpret_cast<const f32x4*>(&red[2][row][cb + 4 * q4]);
      f32x4 s = (s0 + s1) + s2;
#pragma unroll
      for (int i = 0; i < 4; ++i) {
        float av = bf2f(at[cb + 4 * q4 + i][row]);
        o[4 * q4 + i] = f2bf(rbf(g * av) + rbf(t2 * rbf(s[i])));
      }
    }
    unsigned short* op = out + (size_t)(m0 + row) * D_ + n0 + cb;
#pragma unroll
    for (int q4 = 0; q4 < 5; ++q4) {
      u32x2 w;
      w[0] = o[4 * q4] | ((unsigned)o[4 * q4 + 1] << 16);
      w[1] = o[4 * q4 + 2] | ((unsigned)o[4 * q4 + 3] << 16);
      *reinterpret_cast<u32x2*>(op + 4 * q4) = w;
    }
  }
}

extern "C" void kernel_launch(void* const* d_in, const int* in_sizes, int n_in, void* d_out, int out_size, void* d_ws,
                              size_t ws_size, hipStream_t stream) {
  const unsigned short* u = (const unsigned short*)d_in[0];
  const unsigned short* logAr = (const unsigned short*)d_in[1];
  const unsigned short* Aim = (const unsigned short*)d_in[2];
  const unsigned short* Cre = (const unsigned short*)d_in[3];
  const unsigned short* Cim = (const unsigned short*)d_in[4];
  const unsigned short* logdt = (const unsigned short*)d_in[5];
  const unsigned short* Dsk = (const unsigned short*)d_in[6];
  const unsigned short* w1 = (const unsigned short*)d_in[8];
  const unsigned short* w2 = (const unsigned short*)d_in[9];
  const unsigned short* gw = (const unsigned short*)d_in[10];
  unsigned short* out = (unsigned short*)d_out;

  char* ws = (char*)d_ws;
  unsigned short* hbuf = (unsigned short*)ws;
  unsigned short* aT = (unsigned short*)(ws + (size_t)L_ * F_ * 2);
  unsigned short* uT = (unsigned short*)(ws + (size_t)L_ * F_ * 2 + (size_t)D_ * L_ * 2);
  unsigned short* w2T = (unsigned short*)(ws + (size_t)L_ * F_ * 2 + 2 * (size_t)D_ * L_ * 2);
  float* gbuf = (float*)(ws + (size_t)L_ * F_ * 2 + 2 * (size_t)D_ * L_ * 2 + (size_t)D_ * F_ * 2);
  unsigned short* ktab = (unsigned short*)((char*)gbuf + 8192);

  static bool attr_set = false;
  if (!attr_set) {
    hipFuncSetAttribute((const void*)k1_gemm1, hipFuncAttributeMaxDynamicSharedMemorySize, G1_LDS);
    attr_set = true;
  }
  hipLaunchKernelGGL(k1_gemm1, dim3(K1_GRID), dim3(256), G1_LDS, stream, u, w1, w2, gw, hbuf, uT, w2T, gbuf, ktab);
  hipLaunchKernelGGL(k2_conv, dim3(D_), dim3(256), CONV_LDS, stream, uT, logAr, Aim, Cre, Cim, logdt, Dsk, aT, ktab);
  hipLaunchKernelGGL(k3_gemm2, dim3(256), dim3(384), 0, stream, hbuf, w2T, aT, gbuf, out);
}
```

```cpp
#include <hip/hip_runtime.h>
#include <hip/hip_bf16.h>
#include <stdint.h>

typedef __attribute__((ext_vector_type(8))) short bf16x8;
typedef __attribute__((ext_vector_type(4))) short bf16x4;
typedef __attribute__((ext_vector_type(16))) float f32x16;
typedef __attribute__((ext_vector_type(4))) float f32x4;
typedef __attribute__((ext_vector_type(4))) unsigned int u32x4;
typedef __attribute__((ext_vector_type(2))) unsigned int u32x2;

#define LDS_AS __attribute__((address_space(3)))
#define RAW_BARRIER() do { asm volatile("s_waitcnt lgkmcnt(0)" ::: "memory"); __builtin_amdgcn_s_barrier(); } while (0)

constexpr int L_ = 2048, D_ = 640, NS_ = 128, F_ = 1728;

__device__ __forceinline__ float bf2f(unsigned short v) { return __uint_as_float(((unsigned)v) << 16); }
__device__ __forceinline__ unsigned short f2bf(float f) {
  unsigned r;
  asm("v_cvt_pk_bf16_f32 %0, %1, %1" : "=v"(r) : "v"(f));
  return (unsigned short)(r & 0xffffu);
}
__device__ __forceinline__ float rbf(float f) { return bf2f(f2bf(f)); }

constexpr int G1_TM = L_ / 128, G1_TN = (F_ + 127) / 128;
constexpr int N_G1 = G1_TM * G1_TN;
constexpr int G1_STAGE = 32768, G1_LDS = 3 * G1_STAGE;
constexpr int K1_GRID = N_G1 + 32;

__device__ __forceinline__ void tr_tiles_load(const unsigned short* __restrict__ src, int ss, int r0, int c0,
                                              u32x4 (&v)[2]) {
  const int tid = threadIdx.x;
#pragma unroll
  for (int i = 0; i < 2; ++i) {
    int id = tid + 256 * i, row = id >> 3, ch = id & 7;
    v[i] = *reinterpret_cast<const u32x4*>(src + (size_t)(r0 + row) * ss + c0 + ch * 8);
  }
}
__device__ __forceinline__ void tr_tiles_put(unsigned short (*tile)[72], const u32x4 (&v)[2]) {
  const int tid = threadIdx.x;
#pragma unroll
  for (int i = 0; i < 2; ++i) {
    int id = tid + 256 * i, row = id >> 3, ch = id & 7;
    *reinterpret_cast<u32x4*>(&tile[row][ch * 8]) = v[i];
  }
}
template <bool BLOCKED>
__device__ __forceinline__ void tr_tiles_store(unsigned short (*tile)[72], unsigned short* __restrict__ dst, int ds,
                                               int r0, int c0) {
  const int tid = threadIdx.x;
#pragma unroll
  for (int i = 0; i < 2; ++i) {
    int id = tid + 256 * i, col = id >> 3, ch = id & 7;
    unsigned short o[8];
#pragma unroll
    for (int k = 0; k < 8; ++k) o[k] = tile[ch * 8 + k][col];
    u32x4 w;
#pragma unroll
    for (int j = 0; j < 4; ++j) w[j] = o[2 * j] | ((unsigned)o[2 * j + 1] << 16);
    unsigned short* dp = BLOCKED ? dst + ((size_t)((c0 + col) >> 4) * 54 + ((r0 + ch * 8) >> 5)) * 512 + (col & 15) * 32 + (ch & 3) * 8
                                 : dst + (size_t)(c0 + col) * ds + r0 + ch * 8;
    __builtin_nontemporal_store(w, reinterpret_cast<u32x4*>(dp));
  }
}

__global__ __launch_bounds__(256, 1) void k1_gemm1(const unsigned short* __restrict__ x, const unsigned short* __restrict__ w1,
                                                   const unsigned short* __restrict__ w2, const unsigned short* __restrict__ gw,
                                                   unsigned short* __restrict__ hbuf, unsigned short* __restrict__ uT,
                                                   unsigned short* __restrict__ w2T, float* __restrict__ gbuf,
                                                   unsigned short* __restrict__ ktab) {
  extern __shared__ __attribute__((aligned(16))) char smem[];
  const int tid = threadIdx.x, lane = tid & 63, wid = tid >> 6;
  const int b = blockIdx.x;
  if (b >= N_G1) {
    const int r0 = (b - N_G1) * 64;
    if (tid < 64) {
      int rr = (int)rbf((float)(r0 + tid));
      int i;
      if (rr < 256) i = (rr & 7) + 8 * (rr >> 7);
      else if (rr < 512) i = 16 + 4 * ((rr - 256) >> 7) + ((rr & 7) >> 1);
      else if (rr < 1024) i = 24 + 2 * ((rr - 512) >> 7) + ((rr & 7) >> 2);
      else if (rr < 2048) i = 32 + ((rr - 1024) >> 7);
      else i = 40;
      ktab[r0 + tid] = (unsigned short)(i * 16 + ((rr >> 3) & 15));
    }
    u32x4 v[10][2];
#pragma unroll
    for (int t = 0; t < 10; ++t) tr_tiles_load(x, D_, r0, t * 64, v[t]);
    float s[2] = {0.f, 0.f};
#pragma unroll
    for (int t = 0; t < 10; ++t) {
      u32x4 g = *reinterpret_cast<const u32x4*>(gw + t * 64 + (tid & 7) * 8);
#pragma unroll
      for (int i = 0; i < 2; ++i)
#pragma unroll
        for (int j = 0; j < 4; ++j) {
          s[i] += rbf(__uint_as_float(v[t][i][j] << 16) * __uint_as_float(g[j] << 16));
          s[i] += rbf(__uint_as_float(v[t][i][j] & 0xffff0000u) * __uint_as_float(g[j] & 0xffff0000u));
        }
    }
#pragma unroll
    for (int i = 0; i < 2; ++i) {
      s[i] += __shfl_xor(s[i], 1); s[i] += __shfl_xor(s[i], 2); s[i] += __shfl_xor(s[i], 4);
      if ((tid & 7) == 0) gbuf[r0 + (tid >> 3) + 32 * i] = rbf(1.0f / (1.0f + expf(-rbf(s[i]))));
    }
    return;
  }

  u32x4 tv[3][2];
#pragma unroll
  for (int j = 0; j < 3; ++j) {
    int id = b + j * N_G1;
    if (id < 320) tr_tiles_load(x, D_, (id / 10) * 64, (id % 10) * 64, tv[j]);
    else if (id < 590) tr_tiles_load(w2, D_, ((id - 320) / 10) * 64, ((id - 320) % 10) * 64, tv[j]);
  }

  int t = (b & 7) * (N_G1 / 8) + (b >> 3);
  const int tm = t / G1_TN, tn = t % G1_TN;
  const int m0 = tm * 128, n0 = tn * 128;
  const int wm = wid >> 1, wn = wid & 1, r = lane & 31, h = lane >> 5;

  const unsigned short* asrc[4];
  const unsigned short* bsrc[4];
#pragma unroll
  for (int i = 0; i < 4; ++i) {
    int row = (i * 4 + wid) * 8 + (lane >> 3);
    int c = (lane & 7) ^ (row & 7);
    asrc[i] = x + (size_t)(m0 + row) * D_ + c * 8;
    int k = (i * 4 + wid) * 4 + (lane >> 4);
    int cb = (lane & 15) ^ ((k & 3) << 1);
    int col = n0 + cb * 8;
    if (col > F_ - 8) col = F_ - 8;
    bsrc[i] = w1 + (size_t)k * F_ + col;
  }
  auto stage = [&](int kt, int buf) {
    char* sa = smem + buf * G1_STAGE;
    char* sb = sa + 16384;
#pragma unroll
    for (int i = 0; i < 4; ++i)
      __builtin_amdgcn_global_load_lds((const unsigned*)(asrc[i] + kt * 64), (LDS_AS unsigned*)(sa + (i * 4 + wid) * 1024), 16, 0, 0);
#pragma unroll
    for (int i = 0; i < 4; ++i)
      __builtin_amdgcn_global_load_lds((const unsigned*)(bsrc[i] + (size_t)kt * 64 * F_), (LDS_AS unsigned*)(sb + (i * 4 + wid) * 1024), 16, 0, 0);
  };

  f32x16 acc[2][2];
#pragma unroll
  for (int a = 0; a < 2; ++a)
#pragma unroll
    for (int c = 0; c < 2; ++c)
#pragma unroll
      for (int i = 0; i < 16; ++i) acc[a][c][i] = 0.f;

  const int q = (lane & 15) >> 2, p = lane & 3, blk = (lane >> 4) & 1;
  auto compute = [&](int buf) {
    const char* sa = smem + buf * G1_STAGE;
    const char* sb = sa + 16384;
#pragma unroll
    for (int s = 0; s < 4; ++s) {
      bf16x8 af[2], bfr[2];
#pragma unroll
      for (int mt = 0; mt < 2; ++mt) {
        int row = wm * 64 + mt * 32 + r;
        af[mt] = *reinterpret_cast<const bf16x8*>(sa + row * 128 + (((2 * s + h) ^ (row & 7)) << 4));
      }
#pragma unroll
      for (int nt = 0; nt < 2; ++nt)
#pragma unroll
        for (int tt = 0; tt < 2; ++tt) {
          int k = 16 * s + 8 * h + 4 * tt + q;
          int chunk = wn * 8 + nt * 4 + 2 * blk + (p >> 1);
          const char* ad = sb + k * 256 + ((chunk ^ (q << 1)) << 4) + (p & 1) * 8;
          bf16x4 v = __builtin_amdgcn_ds_read_tr16_b64_v4i16((LDS_AS bf16x4*)(ad));
          bfr[nt][4 * tt + 0] = v[0]; bfr[nt][4 * tt + 1] = v[1]; bfr[nt][4 * tt + 2] = v[2]; bfr[nt][4 * tt + 3] = v[3];
        }
#pragma unroll
      for (int mt = 0; mt < 2; ++mt)
#pragma unroll
        for (int nt = 0; nt < 2; ++nt)
          acc[mt][nt] = __builtin_amdgcn_mfma_f32_32x32x16_bf16(af[mt], bfr[nt], acc[mt][nt], 0, 0, 0);
    }
  };

  constexpr int NK = D_ / 64;
  stage(0, 0);
  stage(1, 1);
  for (int kt = 0; kt < NK - 1; ++kt) {
    asm volatile("s_waitcnt vmcnt(8)" ::: "memory");
    RAW_BARRIER();
    if (kt + 2 < NK) stage(kt + 2, (kt + 2) % 3);
    compute(kt % 3);
  }
  asm volatile("s_waitcnt vmcnt(0)" ::: "memory");
  RAW_BARRIER();
  compute((NK - 1) % 3);

  RAW_BARRIER();
  {
    unsigned short* ht = reinterpret_cast<unsigned short*>(smem);
#pragma unroll
    for (int nt = 0; nt < 2; ++nt)
#pragma unroll
      for (int mt = 0; mt < 2; ++mt)
#pragma unroll
        for (int i = 0; i < 16; ++i) {
          int row = wm * 64 + mt * 32 + (i & 3) + 8 * (i >> 2) + 4 * h;
          float hv = rbf(acc[mt][nt][i]);
          float sv = hv * __builtin_amdgcn_rcpf(1.0f + __expf(-hv));
          ht[row * 136 + wn * 64 + nt * 32 + r] = f2bf(sv);
        }
    typedef unsigned short tile_t[64][72];
    tile_t* tiles = reinterpret_cast<tile_t*>(smem + 36864);
#pragma unroll
    for (int j = 0; j < 3; ++j)
      if (b + j * N_G1 < 590) tr_tiles_put(tiles[j], tv[j]);
    __syncthreads();
#pragma unroll
    for (int i = 0; i < 8; ++i) {
      int id = tid + 256 * i, row = id >> 4, ch = id & 15;
      if (n0 + ch * 8 < F_)
        __builtin_nontemporal_store(*reinterpret_cast<const u32x4*>(ht + row * 136 + ch * 8), reinterpret_cast<u32x4*>(hbuf + ((size_t)((m0 + row) >> 4) * 54 + ((n0 + ch * 8) >> 5)) * 512 + (row & 15) * 32 + (ch & 3) * 8));
    }
#pragma unroll
    for (int j = 0; j < 3; ++j) {
      int id = b + j * N_G1;
      if (id < 320) tr_tiles_store<false>(tiles[j], uT, L_, (id / 10) * 64, (id % 10) * 64);
      else if (id < 590) tr_tiles_store<true>(tiles[j], w2T, F_, ((id - 320) / 10) * 64, ((id - 320) % 10) * 64);
    }
  }
}

constexpr int UC_STRIDE = 4384;
constexpr int UC_OFF = 0;
constexpr int KROW = 80;
constexpr int KH_OFF = 8 * UC_STRIDE;
constexpr int KL_OFF = KH_OFF + 64 * KROW + 16;
constexpr int KZ_OFF = KH_OFF + 64 * KROW;
constexpr int PR_OFF = KH_OFF + 12288;
constexpr int OS_OFF = KH_OFF;
constexpr int TB_OFF = PR_OFF + 128 * 16;
constexpr int CONV_LDS = TB_OFF + 768 * 4;

__device__ __forceinline__ float thi_of(int i) {
  int t;
  if (i < 8) t = i;
  else if (i < 16) t = 128 + (i - 8);
  else if (i < 24) t = 256 + 128 * ((i - 16) >> 2) + 2 * ((i - 16) & 3);
  else if (i < 32) t = 512 + 128 * ((i - 24) >> 1) + 4 * ((i - 24) & 1);
  else if (i < 40) t = 1024 + 128 * (i - 32);
  else t = 2048;
  return (float)t;
}

__device__ void conv_role(int d, const unsigned short* __restrict__ uT, const unsigned short* __restrict__ logAr,
                          const unsigned short* __restrict__ Aim, const unsigned short* __restrict__ Cre,
                          const unsigned short* __restrict__ Cim, const unsigned short* __restrict__ logdt,
                          const unsigned short* __restrict__ Dsk, unsigned short* __restrict__ aT,
                          const unsigned short* __restrict__ ktab, char* smem) {
  const int tid = threadIdx.x, lane = tid & 63, wid = tid >> 6;

  const u32x4 uvec = *reinterpret_cast<const u32x4*>(uT + (size_t)d * L_ + 8 * tid);

  const u32x4 kidx = *reinterpret_cast<const u32x4*>(ktab + 8 * tid);

  const float dt = rbf(expf(bf2f(logdt[d])));
  if (tid < NS_) {
    float are = -rbf(expf(bf2f(logAr[d * NS_ + tid])));
    float aim = bf2f(Aim[d * NS_ + tid]);
    float lr = are * dt, li = aim * dt;
    float cr = bf2f(Cre[d * NS_ + tid]) * dt, ci = bf2f(Cim[d * NS_ + tid]) * dt;
    float mag = sqrtf(cr * cr + ci * ci);
    f32x4 pp;
    pp[0] = lr * 1.4426950408889634f;
    pp[1] = li * 0.15915494309189535f;
    pp[2] = log2f(mag);
    pp[3] = atan2f(ci, cr) * 0.15915494309189535f;
    *reinterpret_cast<f32x4*>(smem + PR_OFF + tid * 16) = pp;
  }
  if (tid < 64) {
    u32x4 z = {0, 0, 0, 0};
    *reinterpret_cast<u32x4*>(smem + UC_OFF + (tid >> 3) * UC_STRIDE + (tid & 7) * 16) = z;
  }
  *reinterpret_cast<u32x4*>(smem + UC_OFF + 128 + tid * 16) = uvec;
  __syncthreads();

  {
    u32x4 v1 = {0, 0, 0, 0};
    if (tid < 255) v1 = *reinterpret_cast<const u32x4*>(smem + UC_OFF + 128 + (tid + 1) * 16);
    unsigned w[8] = {uvec[0], uvec[1], uvec[2], uvec[3], v1[0], v1[1], v1[2], v1[3]};
#pragma unroll
    for (int e = 1; e < 8; ++e) {
      u32x4 o;
#pragma unroll
      for (int j = 0; j < 4; ++j) {
        int base = j + (e >> 1);
        if (e & 1) o[j] = (w[base] >> 16) | (w[base + 1] << 16);
        else o[j] = w[base];
      }
      *reinterpret_cast<u32x4*>(smem + UC_OFF + e * UC_STRIDE + 128 + tid * 16) = o;
    }
    if (tid == 0) {
#pragma unroll
      for (int e = 1; e < 8; ++e)
#pragma unroll
        for (int k = 0; k < e; ++k)
          *reinterpret_cast<unsigned short*>(smem + UC_OFF + e * UC_STRIDE + 2 * (64 - e + k)) =
              (unsigned short)(w[k >> 1] >> (16 * (k & 1)));
    }
  }

  {
    const int x = lane & 15, kk = lane >> 4;
    float th[3];
#pragma unroll
    for (int T = 0; T < 3; ++T) th[T] = thi_of(16 * T + x);
    const float tlo = (float)(8 * x);
    f32x4 acc[3];
#pragma unroll
    for (int T = 0; T < 3; ++T) acc[T] = (f32x4){0.f, 0.f, 0.f, 0.f};
#pragma unroll 2
    for (int g = 0; g < 8; ++g) {
      int n = 32 * wid + 4 * g + kk;
      f32x4 pp = *reinterpret_cast<const f32x4*>(smem + PR_OFF + n * 16);
      float qe = __builtin_amdgcn_exp2f(pp[0] * tlo);
      float qa = pp[1] * tlo;
      float qr = qe * __builtin_amdgcn_cosf(qa), qi = qe * __builtin_amdgcn_sinf(qa);
#pragma unroll
      for (int T = 0; T < 3; ++T) {
        float pe = __builtin_amdgcn_exp2f(pp[0] * th[T] + pp[2]);
        float pa = pp[1] * th[T] + pp[3];
        float pr = pe * __builtin_amdgcn_cosf(pa), pi = pe * __builtin_amdgcn_sinf(pa);
        acc[T] = __builtin_amdgcn_mfma_f32_16x16x4f32(qr, pr, acc[T], 0, 0, 0);
        acc[T] = __builtin_amdgcn_mfma_f32_16x16x4f32(-qi, pi, acc[T], 0, 0, 0);
      }
    }
    float* os = reinterpret_cast<float*>(smem + OS_OFF) + wid * 768;
#pragma unroll
    for (int T = 0; T < 3; ++T) *reinterpret_cast<f32x4*>(os + (16 * T + x) * 16 + 4 * kk) = acc[T];
  }
  __syncthreads();

  {
    unsigned* tab = reinterpret_cast<unsigned*>(smem + TB_OFF);
#pragma unroll
    for (int k = 0; k < 3; ++k) {
      int e = tid + 256 * k;
      const float* osp = reinterpret_cast<const float*>(smem + OS_OFF) + e;
      float kv = 2.0f * ((osp[0] + osp[768]) + (osp[1536] + osp[2304]));
      unsigned short hi = f2bf(kv);
      tab[e] = (unsigned)hi | ((unsigned)f2bf(kv - bf2f(hi)) << 16);
    }
  }
  __syncthreads();
  {
    const unsigned* tab = reinterpret_cast<const unsigned*>(smem + TB_OFF);
    unsigned w[8];
#pragma unroll
    for (int k = 0; k < 8; ++k) w[k] = tab[(kidx[k >> 1] >> (16 * (k & 1))) & 0xffff];
    u32x4 vh, vl;
#pragma unroll
    for (int j = 0; j < 4; ++j) {
      vh[j] = (w[7 - 2 * j] & 0xffffu) | (w[6 - 2 * j] << 16);
      vl[j] = (w[7 - 2 * j] >> 16) | (w[6 - 2 * j] & 0xffff0000u);
    }
    *reinterpret_cast<u32x4*>(smem + KH_OFF + (tid >> 2) * KROW + (tid & 3) * 16) = vh;
    *reinterpret_cast<u32x4*>(smem + KL_OFF + (tid >> 2) * KROW + (tid & 3) * 16) = vl;
    if (tid == 0) { u32x4 z = {0, 0, 0, 0}; *reinterpret_cast<u32x4*>(smem + KZ_OFF) = z; }
  }
  __syncthreads();

  f32x16 y0, y1;
#pragma unroll
  for (int i = 0; i < 16; ++i) { y0[i] = 0.f; y1[i] = 0.f; }
  {
    const int r = lane & 31, h = lane >> 5;
    const int e = (r + 1) & 7;
    const char* ubase = smem + UC_OFF + e * UC_STRIDE + 2 * (r - 7 - e + 64 - 8 * h);
#pragma unroll 2
    for (int dl = wid; dl < 32; dl += 4) {
      bf16x8 a0 = *reinterpret_cast<const bf16x8*>(ubase + 64 * dl);
      bf16x8 a1 = *reinterpret_cast<const bf16x8*>(ubase + 64 * dl - 32);
      bf16x8 a2 = *reinterpret_cast<const bf16x8*>(ubase + 64 * dl + 2048);
      bf16x8 a3 = *reinterpret_cast<const bf16x8*>(ubase + 64 * dl + 2048 - 32);
      const int m = r - dl;
      const int off = (m >= 0) ? (m * KROW + 16 * h) : (KZ_OFF - KH_OFF);
      const int off1 = (m >= 0) ? (off + 32) : off;
      const int lo = (m >= 0) ? KL_OFF : KH_OFF;
      bf16x8 fh0 = *reinterpret_cast<const bf16x8*>(smem + KH_OFF + off);
      bf16x8 fh1 = *reinterpret_cast<const bf16x8*>(smem + KH_OFF + off1);
      bf16x8 fl0 = *reinterpret_cast<const bf16x8*>(smem + lo + off);
      bf16x8 fl1 = *reinterpret_cast<const bf16x8*>(smem + lo + off1);
      const int go = (32 + m) * KROW + 16 * h;
      bf16x8 gh0 = *reinterpret_cast<const bf16x8*>(smem + KH_OFF + go);
      bf16x8 gh1 = *reinterpret_cast<const bf16x8*>(smem + KH_OFF + go + 32);
      bf16x8 gl0 = *reinterpret_cast<const bf16x8*>(smem + KL_OFF + go);
      bf16x8 gl1 = *reinterpret_cast<const bf16x8*>(smem + KL_OFF + go + 32);
      y0 = __builtin_amdgcn_mfma_f32_32x32x16_bf16(a0, fh0, y0, 0, 0, 0);
      y1 = __builtin_amdgcn_mfma_f32_32x32x16_bf16(a2, fh0, y1, 0, 0, 0);
      y0 = __builtin_amdgcn_mfma_f32_32x32x16_bf16(a1, fh1, y0, 0, 0, 0);
      y1 = __builtin_amdgcn_mfma_f32_32x32x16_bf16(a3, fh1, y1, 0, 0, 0);
      y0 = __builtin_amdgcn_mfma_f32_32x32x16_bf16(a0, fl0, y0, 0, 0, 0);
      y1 = __builtin_amdgcn_mfma_f32_32x32x16_bf16(a2, fl0, y1, 0, 0, 0);
      y0 = __builtin_amdgcn_mfma_f32_32x32x16_bf16(a1, fl1, y0, 0, 0, 0);
      y1 = __builtin_amdgcn_mfma_f32_32x32x16_bf16(a3, fl1, y1, 0, 0, 0);
      y1 = __builtin_amdgcn_mfma_f32_32x32x16_bf16(a0, gh0, y1, 0, 0, 0);
      y1 = __builtin_amdgcn_mfma_f32_32x32x16_bf16(a1, gh1, y1, 0, 0, 0);
      y1 = __builtin_amdgcn_mfma_f32_32x32x16_bf16(a0, gl0, y1, 0, 0, 0);
      y1 = __builtin_amdgcn_mfma_f32_32x32x16_bf16(a1, gl1, y1, 0, 0, 0);
    }
  }
  __syncthreads();
  {
    const int r = lane & 31, h = lane >> 5;
    float* slab = reinterpret_cast<float*>(smem + wid * 8704);
#pragma unroll
    for (int g = 0; g < 4; ++g) {
      f32x4 v0 = {y0[4 * g], y0[4 * g + 1], y0[4 * g + 2], y0[4 * g + 3]};
      f32x4 v1 = {y1[4 * g], y1[4 * g + 1], y1[4 * g + 2], y1[4 * g + 3]};
      int b = 8 * g + 4 * h;
      *reinterpret_cast<f32x4*>(slab + 32 * r + b) = v0;
      *reinterpret_cast<f32x4*>(slab + 32 * (32 + r) + b) = v1;
    }
  }
  __syncthreads();
  {
    const float dsk = bf2f(Dsk[d]);
    const float* s = reinterpret_cast<const float*>(smem);
    f32x4 ys[2];
#pragma unroll
    for (int hh = 0; hh < 2; ++hh) {
      f32x4 s0 = *reinterpret_cast<const f32x4*>(s + 8 * tid + 4 * hh);
      f32x4 s1 = *reinterpret_cast<const f32x4*>(s + 2176 + 8 * tid + 4 * hh);
      f32x4 s2 = *reinterpret_cast<const f32x4*>(s + 4352 + 8 * tid + 4 * hh);
      f32x4 s3 = *reinterpret_cast<const f32x4*>(s + 6528 + 8 * tid + 4 * hh);
      ys[hh] = (s0 + s1) + (s2 + s3);
    }
    unsigned short o[8];
#pragma unroll
    for (int k = 0; k < 8; ++k) {
      float yb = rbf(ys[k >> 2][k & 3]);
      float uu = bf2f((unsigned short)(uvec[k >> 1] >> (16 * (k & 1))));
      float sk = rbf(uu * dsk);
      o[k] = f2bf(yb + sk);
    }
    u32x4 v;
#pragma unroll
    for (int j = 0; j < 4; ++j) v[j] = o[2 * j] | ((unsigned)o[2 * j + 1] << 16);
    *reinterpret_cast<u32x4*>(aT + (size_t)d * L_ + 8 * tid) = v;
  }
}

__global__ __launch_bounds__(256) void k2_conv(const unsigned short* __restrict__ uT, const unsigned short* __restrict__ logAr,
                                               const unsigned short* __restrict__ Aim, const unsigned short* __restrict__ Cre,
                                               const unsigned short* __restrict__ Cim, const unsigned short* __restrict__ logdt,
                                               const unsigned short* __restrict__ Dsk, unsigned short* __restrict__ aT,
                                               const unsigned short* __restrict__ ktab) {
  extern __shared__ __attribute__((aligned(16))) char smem[];
  conv_role(blockIdx.x, uT, logAr, Aim, Cre, Cim, logdt, Dsk, aT, ktab, smem);
}

constexpr int G2_RS = 84;
__global__ __launch_bounds__(256, 1) void k3_gemm2(const unsigned short* __restrict__ hbuf, const unsigned short* __restrict__ w2T,
                                                   const unsigned short* __restrict__ aT, const float* __restrict__ gbuf,
                                                   unsigned short* __restrict__ out) {
  __shared__ float red[4][64][G2_RS];
  __shared__ unsigned short at[80][72];
  const int tid = threadIdx.x, lane = tid & 63, wid = tid >> 6;
  int t = (blockIdx.x & 7) * 32 + (blockIdx.x >> 3);
  const int tm = t >> 3, tn = t & 7;
  const int m0 = tm * 64, n0 = tn * 80;
  const int fr = lane & 15, fq = lane >> 4;
  const int c0 = (wid * 27) >> 2, c1 = ((wid + 1) * 27) >> 2;

  const unsigned short* ap = hbuf + (size_t)(m0 >> 4) * 54 * 512 + fr * 32 + 8 * fq;
  const unsigned short* bp = w2T + (size_t)(n0 >> 4) * 54 * 512 + fr * 32 + 8 * fq;

  u32x4 apre[3];
#pragma unroll
  for (int j = 0; j < 3; ++j) {
    int id = tid + 256 * j;
    if (id < 640) apre[j] = *reinterpret_cast<const u32x4*>(aT + (size_t)(n0 + (id >> 3)) * L_ + m0 + (id & 7) * 8);
  }
  const float gpre = gbuf[m0 + (tid >> 2)];

  f32x4 acc[4][5];
#pragma unroll
  for (int a = 0; a < 4; ++a)
#pragma unroll
    for (int c = 0; c < 5; ++c) acc[a][c] = (f32x4){0.f, 0.f, 0.f, 0.f};

  bf16x8 A0[4][2], B0[5][2], A1[4][2], B1[5][2];
#define G2_LOAD(AA, BB, c)                                                                             \
  {                                                                                                    \
    _Pragma("unroll") for (int a = 0; a < 4; ++a) {                                                    \
      AA[a][0] = *reinterpret_cast<const bf16x8*>(ap + a * 54 * 512 + (c) * 1024);                \
      AA[a][1] = *reinterpret_cast<const bf16x8*>(ap + a * 54 * 512 + (c) * 1024 + 512);            \
    }                                                                                                  \
    _Pragma("unroll") for (int a = 0; a < 5; ++a) {                                                    \
      BB[a][0] = *reinterpret_cast<const bf16x8*>(bp + a * 54 * 512 + (c) * 1024);                \
      BB[a][1] = *reinterpret_cast<const bf16x8*>(bp + a * 54 * 512 + (c) * 1024 + 512);            \
    }                                                                                                  \
    __builtin_amdgcn_sched_barrier(0);                                                                 \
  }
#define G2_COMP(AA, BB)                                                                                \
  {                                                                                                    \
    _Pragma("unroll") for (int s = 0; s < 2; ++s)                                                      \
      _Pragma("unroll") for (int a = 0; a < 4; ++a)                                                    \
        _Pragma("unroll") for (int c = 0; c < 5; ++c)                                                  \
          acc[a][c] = __builtin_amdgcn_mfma_f32_16x16x32_bf16(AA[a][s], BB[c][s], acc[a][c], 0, 0, 0); \
    __builtin_amdgcn_sched_barrier(0);                                                                 \
  }
  bf16x8 A2[4][2], B2[5][2];
  G2_LOAD(A0, B0, c0);
  G2_LOAD(A1, B1, c0 + 1);
#pragma unroll
  for (int c = 0; c < 6; c += 3) {
    G2_LOAD(A2, B2, c0 + c + 2);
    G2_COMP(A0, B0);
    G2_LOAD(A0, B0, c0 + c + 3);
    G2_COMP(A1, B1);
    if (c + 4 < 6) G2_LOAD(A1, B1, c0 + c + 4);
    G2_COMP(A2, B2);
  }
  if (c1 - c0 == 7) G2_COMP(A0, B0);
#undef G2_LOAD
#undef G2_COMP

#pragma unroll
  for (int a = 0; a < 4; ++a)
#pragma unroll
    for (int c = 0; c < 5; ++c)
#pragma unroll
      for (int i = 0; i < 4; ++i) red[wid][16 * a + 4 * fq + i][16 * c + fr] = acc[a][c][i];
#pragma unroll
  for (int j = 0; j < 3; ++j) {
    int id = tid + 256 * j;
    if (id < 640) *reinterpret_cast<u32x4*>(&at[id >> 3][(id & 7) * 8]) = apre[j];
  }
  __syncthreads();

  {
    const int row = tid >> 2, cb = (tid & 3) * 20;
    const float g = gpre, t2 = rbf(1.0f - g);
    unsigned short o[20];
#pragma unroll
    for (int q4 = 0; q4 < 5; ++q4) {
      f32x4 s0 = *reinterpret_cast<const f32x4*>(&red[0][row][cb + 4 * q4]);
      f32x4 s1 = *reinterpret_cast<const f32x4*>(&red[1][row][cb + 4 * q4]);
      f32x4 s2 = *reinterpret_cast<const f32x4*>(&red[2][row][cb + 4 * q4]);
      f32x4 s3 = *reinterpret_cast<const f32x4*>(&red[3][row][cb + 4 * q4]);
      f32x4 s = (s0 + s1) + (s2 + s3);
#pragma unroll
      for (int i = 0; i < 4; ++i) {
        float av = bf2f(at[cb + 4 * q4 + i][row]);
        o[4 * q4 + i] = f2bf(rbf(g * av) + rbf(t2 * rbf(s[i])));
      }
    }
    unsigned short* op = out + (size_t)(m0 + row) * D_ + n0 + cb;
#pragma unroll
    for (int q4 = 0; q4 < 5; ++q4) {
      u32x2 w;
      w[0] = o[4 * q4] | ((unsigned)o[4 * q4 + 1] << 16);
      w[1] = o[4 * q4 + 2] | ((unsigned)o[4 * q4 + 3] << 16);
      *reinterpret_cast<u32x2*>(op + 4 * q4) = w;
    }
  }
}

extern "C" void kernel_launch(void* const* d_in, const int* in_sizes, int n_in, void* d_out, int out_size, void* d_ws,
                              size_t ws_size, hipStream_t stream) {
  const unsigned short* u = (const unsigned short*)d_in[0];
  const unsigned short* logAr = (const unsigned short*)d_in[1];
  const unsigned short* Aim = (const unsigned short*)d_in[2];
  const unsigned short* Cre = (const unsigned short*)d_in[3];
  const unsigned short* Cim = (const unsigned short*)d_in[4];
  const unsigned short* logdt = (const unsigned short*)d_in[5];
  const unsigned short* Dsk = (const unsigned short*)d_in[6];
  const unsigned short* w1 = (const unsigned short*)d_in[8];
  const unsigned short* w2 = (const unsigned short*)d_in[9];
  const unsigned short* gw = (const unsigned short*)d_in[10];
  unsigned short* out = (unsigned short*)d_out;

  char* ws = (char*)d_ws;
  unsigned short* hbuf = (unsigned short*)ws;
  unsigned short* aT = (unsigned short*)(ws + (size_t)L_ * F_ * 2);
  unsigned short* uT = (unsigned short*)(ws + (size_t)L_ * F_ * 2 + (size_t)D_ * L_ * 2);
  unsigned short* w2T = (unsigned short*)(ws + (size_t)L_ * F_ * 2 + 2 * (size_t)D_ * L_ * 2);
  float* gbuf = (float*)(ws + (size_t)L_ * F_ * 2 + 2 * (size_t)D_ * L_ * 2 + (size_t)D_ * F_ * 2);
  unsigned short* ktab = (unsigned short*)((char*)gbuf + 8192);

  static bool attr_set = false;
  if (!attr_set) {
    hipFuncSetAttribute((const void*)k1_gemm1, hipFuncAttributeMaxDynamicSharedMemorySize, G1_LDS);
    attr_set = true;
  }
  hipLaunchKernelGGL(k1_gemm1, dim3(K1_GRID), dim3(256), G1_LDS, stream, u, w1, w2, gw, hbuf, uT, w2T, gbuf, ktab);
  hipLaunchKernelGGL(k2_conv, dim3(D_), dim3(256), CONV_LDS, stream, uT, logAr, Aim, Cre, Cim, logdt, Dsk, aT, ktab);
  hipLaunchKernelGGL(k3_gemm2, dim3(256), dim3(256), 0, stream, hbuf, w2T, aT, gbuf, out);
}
```

```cpp
#include <hip/hip_runtime.h>
#include <hip/hip_bf16.h>
#include <stdint.h>

typedef __attribute__((ext_vector_type(8))) short bf16x8;
typedef __attribute__((ext_vector_type(4))) short bf16x4;
typedef __attribute__((ext_vector_type(16))) float f32x16;
typedef __attribute__((ext_vector_type(4))) float f32x4;
typedef __attribute__((ext_vector_type(4))) unsigned int u32x4;
typedef __attribute__((ext_vector_type(2))) unsigned int u32x2;

#define LDS_AS __attribute__((address_space(3)))
#define RAW_BARRIER() do { asm volatile("s_waitcnt lgkmcnt(0)" ::: "memory"); __builtin_amdgcn_s_barrier(); } while (0)

constexpr int L_ = 2048, D_ = 640, NS_ = 128, F_ = 1728;

__device__ __forceinline__ float bf2f(unsigned short v) { return __uint_as_float(((unsigned)v) << 16); }
__device__ __forceinline__ unsigned short f2bf(float f) {
  __bf16 b = (__bf16)f;
  return __builtin_bit_cast(unsigned short, b);
}
__device__ __forceinline__ float rbf(float f) { return bf2f(f2bf(f)); }

constexpr int G1_TM = L_ / 128, G1_TN = (F_ + 127) / 128;
constexpr int N_G1 = G1_TM * G1_TN;
constexpr int G1_STAGE = 16384;
constexpr int N_GATE = 32, N_W2 = 90;
constexpr int B_CONV = N_G1, B_GATE = B_CONV + D_, B_W2 = B_GATE + N_GATE, K12_GRID = B_W2 + N_W2;

__device__ __forceinline__ void tr_tiles_load(const unsigned short* __restrict__ src, int ss, int r0, int c0,
                                              u32x4 (&v)[2]) {
  const int tid = threadIdx.x;
#pragma unroll
  for (int i = 0; i < 2; ++i) {
    int id = tid + 256 * i, row = id >> 3, ch = id & 7;
    v[i] = *reinterpret_cast<const u32x4*>(src + (size_t)(r0 + row) * ss + c0 + ch * 8);
  }
}
__device__ __forceinline__ void tr_tiles_put(unsigned short (*tile)[72], const u32x4 (&v)[2]) {
  const int tid = threadIdx.x;
#pragma unroll
  for (int i = 0; i < 2; ++i) {
    int id = tid + 256 * i, row = id >> 3, ch = id & 7;
    *reinterpret_cast<u32x4*>(&tile[row][ch * 8]) = v[i];
  }
}
__device__ __forceinline__ void tr_tiles_store_blocked(unsigned short (*tile)[72], unsigned short* __restrict__ dst,
                                                       int r0, int c0) {
  const int tid = threadIdx.x;
#pragma unroll
  for (int i = 0; i < 2; ++i) {
    int id = tid + 256 * i, col = id >> 3, ch = id & 7;
    unsigned short o[8];
#pragma unroll
    for (int k = 0; k < 8; ++k) o[k] = tile[ch * 8 + k][col];
    u32x4 w;
#pragma unroll
    for (int j = 0; j < 4; ++j) w[j] = o[2 * j] | ((unsigned)o[2 * j + 1] << 16);
    unsigned short* dp = dst + ((size_t)((c0 + col) >> 4) * 54 + ((r0 + ch * 8) >> 5)) * 512 + (col & 15) * 32 + (ch & 3) * 8;
    __builtin_nontemporal_store(w, reinterpret_cast<u32x4*>(dp));
  }
}

__device__ void gate_role(int gb, const unsigned short* __restrict__ x, const unsigned short* __restrict__ gw,
                          float* __restrict__ gbuf) {
  const int tid = threadIdx.x;
  const int r0 = gb * 64;
  u32x4 v[10][2];
#pragma unroll
  for (int t = 0; t < 10; ++t) tr_tiles_load(x, D_, r0, t * 64, v[t]);
  float s[2] = {0.f, 0.f};
#pragma unroll
  for (int t = 0; t < 10; ++t) {
    u32x4 g = *reinterpret_cast<const u32x4*>(gw + t * 64 + (tid & 7) * 8);
#pragma unroll
    for (int i = 0; i < 2; ++i)
#pragma unroll
      for (int j = 0; j < 4; ++j) {
        s[i] += rbf(__uint_as_float(v[t][i][j] << 16) * __uint_as_float(g[j] << 16));
        s[i] += rbf(__uint_as_float(v[t][i][j] & 0xffff0000u) * __uint_as_float(g[j] & 0xffff0000u));
      }
  }
#pragma unroll
  for (int i = 0; i < 2; ++i) {
    s[i] += __shfl_xor(s[i], 1); s[i] += __shfl_xor(s[i], 2); s[i] += __shfl_xor(s[i], 4);
    if ((tid & 7) == 0) gbuf[r0 + (tid >> 3) + 32 * i] = rbf(1.0f / (1.0f + expf(-rbf(s[i]))));
  }
}

__device__ void w2_role(int wb, const unsigned short* __restrict__ w2, unsigned short* __restrict__ w2T, char* smem) {
  u32x4 tv[3][2];
#pragma unroll
  for (int j = 0; j < 3; ++j) {
    int id = wb * 3 + j;
    tr_tiles_load(w2, D_, (id / 10) * 64, (id % 10) * 64, tv[j]);
  }
  typedef unsigned short tile_t[64][72];
  tile_t* tiles = reinterpret_cast<tile_t*>(smem);
#pragma unroll
  for (int j = 0; j < 3; ++j) tr_tiles_put(tiles[j], tv[j]);
  __syncthreads();
#pragma unroll
  for (int j = 0; j < 3; ++j) {
    int id = wb * 3 + j;
    tr_tiles_store_blocked(tiles[j], w2T, (id / 10) * 64, (id % 10) * 64);
  }
}

__device__ void gemm1_role(int b, const unsigned short* __restrict__ x, const unsigned short* __restrict__ w1,
                           unsigned short* __restrict__ hbuf, char* smem) {
  const int tid = threadIdx.x, lane = tid & 63, wid = tid >> 6;
  int t = (b & 7) * (N_G1 / 8) + (b >> 3);
  const int tm = t / G1_TN, tn = t % G1_TN;
  const int m0 = tm * 128, n0 = tn * 128;
  const int wm = wid >> 1, wn = wid & 1, r = lane & 31, h = lane >> 5;

  const unsigned short* asrc[2];
  const unsigned short* bsrc[2];
#pragma unroll
  for (int i = 0; i < 2; ++i) {
    int row = (i * 4 + wid) * 16 + (lane >> 2);
    int c = (lane & 3) ^ ((row >> 2) & 3);
    asrc[i] = x + (size_t)(m0 + row) * D_ + c * 8;
    int k = (i * 4 + wid) * 4 + (lane >> 4);
    int cb = (lane & 15) ^ ((k & 3) << 1);
    int col = n0 + cb * 8;
    if (col > F_ - 8) col = F_ - 8;
    bsrc[i] = w1 + (size_t)k * F_ + col;
  }
  auto stage = [&](int kt, int buf) {
    char* sa = smem + buf * G1_STAGE;
    char* sb = sa + 8192;
#pragma unroll
    for (int i = 0; i < 2; ++i)
      __builtin_amdgcn_global_load_lds((const unsigned*)(asrc[i] + kt * 32), (LDS_AS unsigned*)(sa + (i * 4 + wid) * 1024), 16, 0, 0);
#pragma unroll
    for (int i = 0; i < 2; ++i)
      __builtin_amdgcn_global_load_lds((const unsigned*)(bsrc[i] + (size_t)kt * 32 * F_), (LDS_AS unsigned*)(sb + (i * 4 + wid) * 1024), 16, 0, 0);
  };

  f32x16 acc[2][2];
#pragma unroll
  for (int a = 0; a < 2; ++a)
#pragma unroll
    for (int c = 0; c < 2; ++c)
#pragma unroll
      for (int i = 0; i < 16; ++i) acc[a][c][i] = 0.f;

  const int q = (lane & 15) >> 2, p = lane & 3, blk = (lane >> 4) & 1;
  auto compute = [&](int buf) {
    const char* sa = smem + buf * G1_STAGE;
    const char* sb = sa + 8192;
#pragma unroll
    for (int s = 0; s < 2; ++s) {
      bf16x8 af[2], bfr[2];
#pragma unroll
      for (int mt = 0; mt < 2; ++mt) {
        int row = wm * 64 + mt * 32 + r;
        af[mt] = *reinterpret_cast<const bf16x8*>(sa + row * 64 + (((2 * s + h) ^ ((row >> 2) & 3)) << 4));
      }
#pragma unroll
      for (int nt = 0; nt < 2; ++nt)
#pragma unroll
        for (int tt = 0; tt < 2; ++tt) {
          int k = 16 * s + 8 * h + 4 * tt + q;
          int chunk = wn * 8 + nt * 4 + 2 * blk + (p >> 1);
          const char* ad = sb + k * 256 + ((chunk ^ (q << 1)) << 4) + (p & 1) * 8;
          bf16x4 v = __builtin_amdgcn_ds_read_tr16_b64_v4i16((LDS_AS bf16x4*)(ad));
          bfr[nt][4 * tt + 0] = v[0]; bfr[nt][4 * tt + 1] = v[1]; bfr[nt][4 * tt + 2] = v[2]; bfr[nt][4 * tt + 3] = v[3];
        }
#pragma unroll
      for (int mt = 0; mt < 2; ++mt)
#pragma unroll
        for (int nt = 0; nt < 2; ++nt)
          acc[mt][nt] = __builtin_amdgcn_mfma_f32_32x32x16_bf16(af[mt], bfr[nt], acc[mt][nt], 0, 0, 0);
    }
  };

  constexpr int NK = D_ / 32;
  stage(0, 0);
  stage(1, 1);
  int cur = 0, nxt = 2;
  for (int kt = 0; kt < NK - 1; ++kt) {
    asm volatile("s_waitcnt vmcnt(4)" ::: "memory");
    RAW_BARRIER();
    if (kt + 2 < NK) stage(kt + 2, nxt);
    compute(cur);
    cur = (cur == 2) ? 0 : cur + 1;
    nxt = (nxt == 2) ? 0 : nxt + 1;
  }
  asm volatile("s_waitcnt vmcnt(0)" ::: "memory");
  RAW_BARRIER();
  compute(cur);

  RAW_BARRIER();
  {
    unsigned short* ht = reinterpret_cast<unsigned short*>(smem);
#pragma unroll
    for (int nt = 0; nt < 2; ++nt)
#pragma unroll
      for (int mt = 0; mt < 2; ++mt)
#pragma unroll
        for (int i = 0; i < 16; ++i) {
          int row = wm * 64 + mt * 32 + (i & 3) + 8 * (i >> 2) + 4 * h;
          float hv = rbf(acc[mt][nt][i]);
          float sv = hv * __builtin_amdgcn_rcpf(1.0f + __expf(-hv));
          ht[row * 136 + wn * 64 + nt * 32 + r] = f2bf(sv);
        }
    __syncthreads();
#pragma unroll
    for (int i = 0; i < 8; ++i) {
      int id = tid + 256 * i, row = id >> 4, ch = id & 15;
      if (n0 + ch * 8 < F_)
        __builtin_nontemporal_store(*reinterpret_cast<const u32x4*>(ht + row * 136 + ch * 8), reinterpret_cast<u32x4*>(hbuf + ((size_t)((m0 + row) >> 4) * 54 + ((n0 + ch * 8) >> 5)) * 512 + (row & 15) * 32 + (ch & 3) * 8));
    }
  }
}

constexpr int UC_STRIDE = 4384;
constexpr int UC_OFF = 0;
constexpr int KROW = 80;
constexpr int KH_OFF = 8 * UC_STRIDE;
constexpr int KL_OFF = KH_OFF + 64 * KROW + 16;
constexpr int KZ_OFF = KH_OFF + 64 * KROW;
constexpr int PR_OFF = KH_OFF + 12288;
constexpr int OS_OFF = KH_OFF;
constexpr int TB_OFF = PR_OFF + 128 * 16;
constexpr int CONV_LDS = TB_OFF + 768 * 4;

__device__ __forceinline__ float thi_of(int i) {
  int t;
  if (i < 8) t = i;
  else if (i < 16) t = 128 + (i - 8);
  else if (i < 24) t = 256 + 128 * ((i - 16) >> 2) + 2 * ((i - 16) & 3);
  else if (i < 32) t = 512 + 128 * ((i - 24) >> 1) + 4 * ((i - 24) & 1);
  else if (i < 40) t = 1024 + 128 * (i - 32);
  else t = 2048;
  return (float)t;
}

__device__ __forceinline__ unsigned kidx_of(int j) {
  int rr = (int)rbf((float)j);
  int i;
  if (rr < 256) i = (rr & 7) + 8 * (rr >> 7);
  else if (rr < 512) i = 16 + 4 * ((rr - 256) >> 7) + ((rr & 7) >> 1);
  else if (rr < 1024) i = 24 + 2 * ((rr - 512) >> 7) + ((rr & 7) >> 2);
  else if (rr < 2048) i = 32 + ((rr - 1024) >> 7);
  else i = 40;
  return (unsigned)(i * 16 + ((rr >> 3) & 15));
}

__device__ void conv_role(int d, const unsigned short* __restrict__ x, const unsigned short* __restrict__ logAr,
                          const unsigned short* __restrict__ Aim, const unsigned short* __restrict__ Cre,
                          const unsigned short* __restrict__ Cim, const unsigned short* __restrict__ logdt,
                          const unsigned short* __restrict__ Dsk, unsigned short* __restrict__ aT, char* smem) {
  const int tid = threadIdx.x, lane = tid & 63, wid = tid >> 6;

  unsigned short ug[8];
#pragma unroll
  for (int k = 0; k < 8; ++k) ug[k] = x[(size_t)(8 * tid + k) * D_ + d];

  const float dt = rbf(expf(bf2f(logdt[d])));
  if (tid < NS_) {
    float are = -rbf(expf(bf2f(logAr[d * NS_ + tid])));
    float aim = bf2f(Aim[d * NS_ + tid]);
    float lr = are * dt, li = aim * dt;
    float cr = bf2f(Cre[d * NS_ + tid]) * dt, ci = bf2f(Cim[d * NS_ + tid]) * dt;
    float mag = sqrtf(cr * cr + ci * ci);
    f32x4 pp;
    pp[0] = lr * 1.4426950408889634f;
    pp[1] = li * 0.15915494309189535f;
    pp[2] = log2f(mag);
    pp[3] = atan2f(ci, cr) * 0.15915494309189535f;
    *reinterpret_cast<f32x4*>(smem + PR_OFF + tid * 16) = pp;
  }
  if (tid < 64) {
    u32x4 z = {0, 0, 0, 0};
    *reinterpret_cast<u32x4*>(smem + UC_OFF + (tid >> 3) * UC_STRIDE + (tid & 7) * 16) = z;
  }
  __syncthreads();

  {
    const int x = lane & 15, kk = lane >> 4;
    float th[3];
#pragma unroll
    for (int T = 0; T < 3; ++T) th[T] = thi_of(16 * T + x);
    const float tlo = (float)(8 * x);
    f32x4 acc[3];
#pragma unroll
    for (int T = 0; T < 3; ++T) acc[T] = (f32x4){0.f, 0.f, 0.f, 0.f};
#pragma unroll 2
    for (int g = 0; g < 8; ++g) {
      int n = 32 * wid + 4 * g + kk;
      f32x4 pp = *reinterpret_cast<const f32x4*>(smem + PR_OFF + n * 16);
      float qe = __builtin_amdgcn_exp2f(pp[0] * tlo);
      float qa = pp[1] * tlo;
      float qr = qe * __builtin_amdgcn_cosf(qa), qi = qe * __builtin_amdgcn_sinf(qa);
#pragma unroll
      for (int T = 0; T < 3; ++T) {
        float pe = __builtin_amdgcn_exp2f(pp[0] * th[T] + pp[2]);
        float pa = pp[1] * th[T] + pp[3];
        float pr = pe * __builtin_amdgcn_cosf(pa), pi = pe * __builtin_amdgcn_sinf(pa);
        acc[T] = __builtin_amdgcn_mfma_f32_16x16x4f32(qr, pr, acc[T], 0, 0, 0);
        acc[T] = __builtin_amdgcn_mfma_f32_16x16x4f32(-qi, pi, acc[T], 0, 0, 0);
      }
    }
    float* os = reinterpret_cast<float*>(smem + OS_OFF) + wid * 768;
#pragma unroll
    for (int T = 0; T < 3; ++T) *reinterpret_cast<f32x4*>(os + (16 * T + x) * 16 + 4 * kk) = acc[T];
  }
  __syncthreads();

  u32x4 uvec;
#pragma unroll
  for (int j = 0; j < 4; ++j) uvec[j] = (unsigned)ug[2 * j] | ((unsigned)ug[2 * j + 1] << 16);
  *reinterpret_cast<u32x4*>(smem + UC_OFF + 128 + tid * 16) = uvec;
  {
    unsigned* tab = reinterpret_cast<unsigned*>(smem + TB_OFF);
#pragma unroll
    for (int k = 0; k < 3; ++k) {
      int e = tid + 256 * k;
      const float* osp = reinterpret_cast<const float*>(smem + OS_OFF) + e;
      float kv = 2.0f * ((osp[0] + osp[768]) + (osp[1536] + osp[2304]));
      unsigned short hi = f2bf(kv);
      tab[e] = (unsigned)hi | ((unsigned)f2bf(kv - bf2f(hi)) << 16);
    }
  }
  __syncthreads();
  {
    u32x4 v1 = {0, 0, 0, 0};
    if (tid < 255) v1 = *reinterpret_cast<const u32x4*>(smem + UC_OFF + 128 + (tid + 1) * 16);
    unsigned w[8] = {uvec[0], uvec[1], uvec[2], uvec[3], v1[0], v1[1], v1[2], v1[3]};
#pragma unroll
    for (int e = 1; e < 8; ++e) {
      u32x4 o;
#pragma unroll
      for (int j = 0; j < 4; ++j) {
        int base = j + (e >> 1);
        if (e & 1) o[j] = (w[base] >> 16) | (w[base + 1] << 16);
        else o[j] = w[base];
      }
      *reinterpret_cast<u32x4*>(smem + UC_OFF + e * UC_STRIDE + 128 + tid * 16) = o;
    }
    if (tid == 0) {
#pragma unroll
      for (int e = 1; e < 8; ++e)
#pragma unroll
        for (int k = 0; k < e; ++k)
          *reinterpret_cast<unsigned short*>(smem + UC_OFF + e * UC_STRIDE + 2 * (64 - e + k)) =
              (unsigned short)(w[k >> 1] >> (16 * (k & 1)));
    }
  }
  {
    const unsigned* tab = reinterpret_cast<const unsigned*>(smem + TB_OFF);
    unsigned w[8];
#pragma unroll
    for (int k = 0; k < 8; ++k) w[k] = tab[kidx_of(8 * tid + k)];
    u32x4 vh, vl;
#pragma unroll
    for (int j = 0; j < 4; ++j) {
      vh[j] = (w[7 - 2 * j] & 0xffffu) | (w[6 - 2 * j] << 16);
      vl[j] = (w[7 - 2 * j] >> 16) | (w[6 - 2 * j] & 0xffff0000u);
    }
    *reinterpret_cast<u32x4*>(smem + KH_OFF + (tid >> 2) * KROW + (tid & 3) * 16) = vh;
    *reinterpret_cast<u32x4*>(smem + KL_OFF + (tid >> 2) * KROW + (tid & 3) * 16) = vl;
    if (tid == 0) { u32x4 z = {0, 0, 0, 0}; *reinterpret_cast<u32x4*>(smem + KZ_OFF) = z; }
  }
  __syncthreads();

  f32x16 y0, y1;
#pragma unroll
  for (int i = 0; i < 16; ++i) { y0[i] = 0.f; y1[i] = 0.f; }
  {
    const int r = lane & 31, h = lane >> 5;
    const int e = (r + 1) & 7;
    const char* ubase = smem + UC_OFF + e * UC_STRIDE + 2 * (r - 7 - e + 64 - 8 * h);
#pragma unroll 2
    for (int dl = wid; dl < 32; dl += 4) {
      bf16x8 a0 = *reinterpret_cast<const bf16x8*>(ubase + 64 * dl);
      bf16x8 a1 = *reinterpret_cast<const bf16x8*>(ubase + 64 * dl - 32);
      bf16x8 a2 = *reinterpret_cast<const bf16x8*>(ubase + 64 * dl + 2048);
      bf16x8 a3 = *reinterpret_cast<const bf16x8*>(ubase + 64 * dl + 2048 - 32);
      const int m = r - dl;
      const int off = (m >= 0) ? (m * KROW + 16 * h) : (KZ_OFF - KH_OFF);
      const int off1 = (m >= 0) ? (off + 32) : off;
      const int lo = (m >= 0) ? KL_OFF : KH_OFF;
      bf16x8 fh0 = *reinterpret_cast<const bf16x8*>(smem + KH_OFF + off);
      bf16x8 fh1 = *reinterpret_cast<const bf16x8*>(smem + KH_OFF + off1);
      bf16x8 fl0 = *reinterpret_cast<const bf16x8*>(smem + lo + off);
      bf16x8 fl1 = *reinterpret_cast<const bf16x8*>(smem + lo + off1);
      const int go = (32 + m) * KROW + 16 * h;
      bf16x8 gh0 = *reinterpret_cast<const bf16x8*>(smem + KH_OFF + go);
      bf16x8 gh1 = *reinterpret_cast<const bf16x8*>(smem + KH_OFF + go + 32);
      bf16x8 gl0 = *reinterpret_cast<const bf16x8*>(smem + KL_OFF + go);
      bf16x8 gl1 = *reinterpret_cast<const bf16x8*>(smem + KL_OFF + go + 32);
      y0 = __builtin_amdgcn_mfma_f32_32x32x16_bf16(a0, fh0, y0, 0, 0, 0);
      y1 = __builtin_amdgcn_mfma_f32_32x32x16_bf16(a2, fh0, y1, 0, 0, 0);
      y0 = __builtin_amdgcn_mfma_f32_32x32x16_bf16(a1, fh1, y0, 0, 0, 0);
      y1 = __builtin_amdgcn_mfma_f32_32x32x16_bf16(a3, fh1, y1, 0, 0, 0);
      y0 = __builtin_amdgcn_mfma_f32_32x32x16_bf16(a0, fl0, y0, 0, 0, 0);
      y1 = __builtin_amdgcn_mfma_f32_32x32x16_bf16(a2, fl0, y1, 0, 0, 0);
      y0 = __builtin_amdgcn_mfma_f32_32x32x16_bf16(a1, fl1, y0, 0, 0, 0);
      y1 = __builtin_amdgcn_mfma_f32_32x32x16_bf16(a3, fl1, y1, 0, 0, 0);
      y1 = __builtin_amdgcn_mfma_f32_32x32x16_bf16(a0, gh0, y1, 0, 0, 0);
      y1 = __builtin_amdgcn_mfma_f32_32x32x16_bf16(a1, gh1, y1, 0, 0, 0);
      y1 = __builtin_amdgcn_mfma_f32_32x32x16_bf16(a0, gl0, y1, 0, 0, 0);
      y1 = __builtin_amdgcn_mfma_f32_32x32x16_bf16(a1, gl1, y1, 0, 0, 0);
    }
  }
  __syncthreads();
  {
    const int r = lane & 31, h = lane >> 5;
    float* slab = reinterpret_cast<float*>(smem + wid * 8704);
#pragma unroll
    for (int g = 0; g < 4; ++g) {
      f32x4 v0 = {y0[4 * g], y0[4 * g + 1], y0[4 * g + 2], y0[4 * g + 3]};
      f32x4 v1 = {y1[4 * g], y1[4 * g + 1], y1[4 * g + 2], y1[4 * g + 3]};
      int b = 8 * g + 4 * h;
      *reinterpret_cast<f32x4*>(slab + 32 * r + b) = v0;
      *reinterpret_cast<f32x4*>(slab + 32 * (32 + r) + b) = v1;
    }
  }
  __syncthreads();
  {
    const float dsk = bf2f(Dsk[d]);
    const float* s = reinterpret_cast<const float*>(smem);
    f32x4 ys[2];
#pragma unroll
    for (int hh = 0; hh < 2; ++hh) {
      f32x4 s0 = *reinterpret_cast<const f32x4*>(s + 8 * tid + 4 * hh);
      f32x4 s1 = *reinterpret_cast<const f32x4*>(s + 2176 + 8 * tid + 4 * hh);
      f32x4 s2 = *reinterpret_cast<const f32x4*>(s + 4352 + 8 * tid + 4 * hh);
      f32x4 s3 = *reinterpret_cast<const f32x4*>(s + 6528 + 8 * tid + 4 * hh);
      ys[hh] = (s0 + s1) + (s2 + s3);
    }
    unsigned short o[8];
#pragma unroll
    for (int k = 0; k < 8; ++k) {
      float yb = rbf(ys[k >> 2][k & 3]);
      float uu = bf2f((unsigned short)(uvec[k >> 1] >> (16 * (k & 1))));
      float sk = rbf(uu * dsk);
      o[k] = f2bf(yb + sk);
    }
    u32x4 v;
#pragma unroll
    for (int j = 0; j < 4; ++j) v[j] = o[2 * j] | ((unsigned)o[2 * j + 1] << 16);
    *reinterpret_cast<u32x4*>(aT + (size_t)d * L_ + 8 * tid) = v;
  }
}

__global__ __launch_bounds__(256, 3) void k12(const unsigned short* __restrict__ x, const unsigned short* __restrict__ w1,
                                              const unsigned short* __restrict__ w2, const unsigned short* __restrict__ gw,
                                              const unsigned short* __restrict__ logAr, const unsigned short* __restrict__ Aim,
                                              const unsigned short* __restrict__ Cre, const unsigned short* __restrict__ Cim,
                                              const unsigned short* __restrict__ logdt, const unsigned short* __restrict__ Dsk,
                                              unsigned short* __restrict__ hbuf, unsigned short* __restrict__ w2T,
                                              float* __restrict__ gbuf, unsigned short* __restrict__ aT) {
  extern __shared__ __attribute__((aligned(16))) char smem[];
  const int b = blockIdx.x;
  if (b < B_CONV) gemm1_role(b, x, w1, hbuf, smem);
  else if (b < B_GATE) conv_role(b - B_CONV, x, logAr, Aim, Cre, Cim, logdt, Dsk, aT, smem);
  else if (b < B_W2) gate_role(b - B_GATE, x, gw, gbuf);
  else w2_role(b - B_W2, w2, w2T, smem);
}

constexpr int G2_RS = 84;
__global__ __launch_bounds__(256, 1) void k3_gemm2(const unsigned short* __restrict__ hbuf, const unsigned short* __restrict__ w2T,
                                                   const unsigned short* __restrict__ aT, const float* __restrict__ gbuf,
                                                   unsigned short* __restrict__ out) {
  __shared__ float red[4][64][G2_RS];
  const int tid = threadIdx.x, lane = tid & 63, wid = tid >> 6;
  int t = (blockIdx.x & 7) * 32 + (blockIdx.x >> 3);
  const int tm = t >> 3, tn = t & 7;
  const int m0 = tm * 64, n0 = tn * 80;
  const int fr = lane & 15, fq = lane >> 4;
  const int c0 = (wid * 27) >> 2, c1 = ((wid + 1) * 27) >> 2;

  const unsigned short* ap = hbuf + (size_t)(m0 >> 4) * 54 * 512 + fr * 32 + 8 * fq;
  const unsigned short* bp = w2T + (size_t)(n0 >> 4) * 54 * 512 + fr * 32 + 8 * fq;

  f32x4 acc[4][5];
#pragma unroll
  for (int a = 0; a < 4; ++a)
#pragma unroll
    for (int c = 0; c < 5; ++c) acc[a][c] = (f32x4){0.f, 0.f, 0.f, 0.f};

  bf16x8 A0[4][2], B0[5][2], A1[4][2], B1[5][2];
#define G2_LOAD(AA, BB, c)                                                                             \
  {                                                                                                    \
    _Pragma("unroll") for (int a = 0; a < 4; ++a) {                                                    \
      AA[a][0] = *reinterpret_cast<const bf16x8*>(ap + a * 54 * 512 + (c) * 1024);                \
      AA[a][1] = *reinterpret_cast<const bf16x8*>(ap + a * 54 * 512 + (c) * 1024 + 512);            \
    }                                                                                                  \
    _Pragma("unroll") for (int a = 0; a < 5; ++a) {                                                    \
      BB[a][0] = *reinterpret_cast<const bf16x8*>(bp + a * 54 * 512 + (c) * 1024);                \
      BB[a][1] = *reinterpret_cast<const bf16x8*>(bp + a * 54 * 512 + (c) * 1024 + 512);            \
    }                                                                                                  \
  }
#define G2_COMP(AA, BB)                                                                                \
  {                                                                                                    \
    _Pragma("unroll") for (int s = 0; s < 2; ++s)                                                      \
      _Pragma("unroll") for (int a = 0; a < 4; ++a)                                                    \
        _Pragma("unroll") for (int c = 0; c < 5; ++c)                                                  \
          acc[a][c] = __builtin_amdgcn_mfma_f32_16x16x32_bf16(AA[a][s], BB[c][s], acc[a][c], 0, 0, 0); \
  }
  G2_LOAD(A0, B0, c0);
  for (int c = c0; c < c1; c += 2) {
    if (c + 1 < c1) G2_LOAD(A1, B1, c + 1);
    G2_COMP(A0, B0);
    if (c + 1 >= c1) break;
    if (c + 2 < c1) G2_LOAD(A0, B0, c + 2);
    G2_COMP(A1, B1);
  }
#undef G2_LOAD
#undef G2_COMP

#pragma unroll
  for (int a = 0; a < 4; ++a)
#pragma unroll
    for (int c = 0; c < 5; ++c)
#pragma unroll
      for (int i = 0; i < 4; ++i) red[wid][16 * a + 4 * fq + i][16 * c + fr] = acc[a][c][i];
  __syncthreads();

#pragma unroll
  for (int j = 0; j < 2; ++j) {
    const int col = lane + 64 * j;
    if (col < 80) {
      const int row0 = m0 + 16 * wid;
      u32x4 av0 = *reinterpret_cast<const u32x4*>(aT + (size_t)(n0 + col) * L_ + row0);
      u32x4 av1 = *reinterpret_cast<const u32x4*>(aT + (size_t)(n0 + col) * L_ + row0 + 8);
#pragma unroll
      for (int i = 0; i < 16; ++i) {
        int rr = 16 * wid + i;
        float bv = rbf((red[0][rr][col] + red[1][rr][col]) + (red[2][rr][col] + red[3][rr][col]));
        unsigned aw = (i < 8) ? av0[(i >> 1) & 3] : av1[(i >> 1) & 3];
        float a = bf2f((unsigned short)((aw >> (16 * (i & 1))) & 0xffff));
        float g = gbuf[row0 + i];
        float t1 = rbf(g * a);
        float t2 = rbf(1.0f - g);
        float t3 = rbf(t2 * bv);
        out[(size_t)(row0 + i) * D_ + n0 + col] = f2bf(t1 + t3);
      }
    }
  }
}

extern "C" void kernel_launch(void* const* d_in, const int* in_sizes, int n_in, void* d_out, int out_size, void* d_ws,
                              size_t ws_size, hipStream_t stream) {
  const unsigned short* u = (const unsigned short*)d_in[0];
  const unsigned short* logAr = (const unsigned short*)d_in[1];
  const unsigned short* Aim = (const unsigned short*)d_in[2];
  const unsigned short* Cre = (const unsigned short*)d_in[3];
  const unsigned short* Cim = (const unsigned short*)d_in[4];
  const unsigned short* logdt = (const unsigned short*)d_in[5];
  const unsigned short* Dsk = (const unsigned short*)d_in[6];
  const unsigned short* w1 = (const unsigned short*)d_in[8];
  const unsigned short* w2 = (const unsigned short*)d_in[9];
  const unsigned short* gw = (const unsigned short*)d_in[10];
  unsigned short* out = (unsigned short*)d_out;

  char* ws = (char*)d_ws;
  unsigned short* hbuf = (unsigned short*)ws;
  unsigned short* aT = (unsigned short*)(ws + (size_t)L_ * F_ * 2);
  unsigned short* w2T = (unsigned short*)(ws + (size_t)L_ * F_ * 2 + (size_t)D_ * L_ * 2);
  float* gbuf = (float*)(ws + (size_t)L_ * F_ * 2 + (size_t)D_ * L_ * 2 + (size_t)D_ * F_ * 2);

  constexpr int K12_LDS = (CONV_LDS > 3 * G1_STAGE) ? CONV_LDS : 3 * G1_STAGE;
  hipLaunchKernelGGL(k12, dim3(K12_GRID), dim3(256), K12_LDS, stream, u, w1, w2, gw, logAr, Aim, Cre, Cim, logdt, Dsk,
                     hbuf, w2T, gbuf, aT);
  hipLaunchKernelGGL(k3_gemm2, dim3(256), dim3(256), 0, stream, hbuf, w2T, aT, gbuf, out);
}
```

```cpp
#include <hip/hip_runtime.h>
#include <hip/hip_bf16.h>
#include <stdint.h>

typedef __attribute__((ext_vector_type(8))) short bf16x8;
typedef __attribute__((ext_vector_type(4))) short bf16x4;
typedef __attribute__((ext_vector_type(16))) float f32x16;
typedef __attribute__((ext_vector_type(4))) float f32x4;
typedef __attribute__((ext_vector_type(4))) unsigned int u32x4;
typedef __attribute__((ext_vector_type(2))) unsigned int u32x2;

#define LDS_AS __attribute__((address_space(3)))
#define RAW_BARRIER() do { asm volatile("s_waitcnt lgkmcnt(0)" ::: "memory"); __builtin_amdgcn_s_barrier(); } while (0)

constexpr int L_ = 2048, D_ = 640, NS_ = 128, F_ = 1728;

__device__ __forceinline__ float bf2f(unsigned short v) { return __uint_as_float(((unsigned)v) << 16); }
__device__ __forceinline__ unsigned short f2bf(float f) {
  __bf16 b = (__bf16)f;
  return __builtin_bit_cast(unsigned short, b);
}
__device__ __forceinline__ float rbf(float f) { return bf2f(f2bf(f)); }

constexpr int G1_TM = L_ / 128, G1_TN = (F_ + 127) / 128;
constexpr int N_G1 = G1_TM * G1_TN;
constexpr int G1_STAGE = 16384;
constexpr int N_GATE = 32, N_W2 = 90;
constexpr int B_CONV = N_G1, B_GATE = B_CONV + D_, B_W2 = B_GATE + N_GATE, K12_GRID = B_W2 + N_W2;

__device__ __forceinline__ void tr_tiles_load(const unsigned short* __restrict__ src, int ss, int r0, int c0,
                                              u32x4 (&v)[2]) {
  const int tid = threadIdx.x;
#pragma unroll
  for (int i = 0; i < 2; ++i) {
    int id = tid + 256 * i, row = id >> 3, ch = id & 7;
    v[i] = *reinterpret_cast<const u32x4*>(src + (size_t)(r0 + row) * ss + c0 + ch * 8);
  }
}
__device__ __forceinline__ void tr_tiles_put(unsigned short (*tile)[72], const u32x4 (&v)[2]) {
  const int tid = threadIdx.x;
#pragma unroll
  for (int i = 0; i < 2; ++i) {
    int id = tid + 256 * i, row = id >> 3, ch = id & 7;
    *reinterpret_cast<u32x4*>(&tile[row][ch * 8]) = v[i];
  }
}
__device__ __forceinline__ void tr_tiles_store_blocked(unsigned short (*tile)[72], unsigned short* __restrict__ dst,
                                                       int r0, int c0) {
  const int tid = threadIdx.x;
#pragma unroll
  for (int i = 0; i < 2; ++i) {
    int id = tid + 256 * i, col = id >> 3, ch = id & 7;
    unsigned short o[8];
#pragma unroll
    for (int k = 0; k < 8; ++k) o[k] = tile[ch * 8 + k][col];
    u32x4 w;
#pragma unroll
    for (int j = 0; j < 4; ++j) w[j] = o[2 * j] | ((unsigned)o[2 * j + 1] << 16);
    unsigned short* dp = dst + ((size_t)((c0 + col) >> 4) * 54 + ((r0 + ch * 8) >> 5)) * 512 + (col & 15) * 32 + (ch & 3) * 8;
    __builtin_nontemporal_store(w, reinterpret_cast<u32x4*>(dp));
  }
}

__device__ void gate_role(int gb, const unsigned short* __restrict__ x, const unsigned short* __restrict__ gw,
                          float* __restrict__ gbuf) {
  const int tid = threadIdx.x;
  const int r0 = gb * 64;
  u32x4 v[10][2];
#pragma unroll
  for (int t = 0; t < 10; ++t) tr_tiles_load(x, D_, r0, t * 64, v[t]);
  float s[2] = {0.f, 0.f};
#pragma unroll
  for (int t = 0; t < 10; ++t) {
    u32x4 g = *reinterpret_cast<const u32x4*>(gw + t * 64 + (tid & 7) * 8);
#pragma unroll
    for (int i = 0; i < 2; ++i)
#pragma unroll
      for (int j = 0; j < 4; ++j) {
        s[i] += rbf(__uint_as_float(v[t][i][j] << 16) * __uint_as_float(g[j] << 16));
        s[i] += rbf(__uint_as_float(v[t][i][j] & 0xffff0000u) * __uint_as_float(g[j] & 0xffff0000u));
      }
  }
#pragma unroll
  for (int i = 0; i < 2; ++i) {
    s[i] += __shfl_xor(s[i], 1); s[i] += __shfl_xor(s[i], 2); s[i] += __shfl_xor(s[i], 4);
    if ((tid & 7) == 0) gbuf[r0 + (tid >> 3) + 32 * i] = rbf(1.0f / (1.0f + expf(-rbf(s[i]))));
  }
}

__device__ void w2_role(int wb, const unsigned short* __restrict__ w2, unsigned short* __restrict__ w2T, char* smem) {
  u32x4 tv[3][2];
#pragma unroll
  for (int j = 0; j < 3; ++j) {
    int id = wb * 3 + j;
    tr_tiles_load(w2, D_, (id / 10) * 64, (id % 10) * 64, tv[j]);
  }
  typedef unsigned short tile_t[64][72];
  tile_t* tiles = reinterpret_cast<tile_t*>(smem);
#pragma unroll
  for (int j = 0; j < 3; ++j) tr_tiles_put(tiles[j], tv[j]);
  __syncthreads();
#pragma unroll
  for (int j = 0; j < 3; ++j) {
    int id = wb * 3 + j;
    tr_tiles_store_blocked(tiles[j], w2T, (id / 10) * 64, (id % 10) * 64);
  }
}

__device__ void gemm1_role(int b, const unsigned short* __restrict__ x, const unsigned short* __restrict__ w1,
                           unsigned short* __restrict__ hbuf, char* smem) {
  const int tid = threadIdx.x, lane = tid & 63, wid = tid >> 6;
  int t = (b & 7) * (N_G1 / 8) + (b >> 3);
  const int tm = t / G1_TN, tn = t % G1_TN;
  const int m0 = tm * 128, n0 = tn * 128;
  const int wm = wid >> 1, wn = wid & 1, r = lane & 31, h = lane >> 5;

  const unsigned short* asrc[2];
  const unsigned short* bsrc[2];
#pragma unroll
  for (int i = 0; i < 2; ++i) {
    int row = (i * 4 + wid) * 16 + (lane >> 2);
    int c = (lane & 3) ^ ((row >> 2) & 3);
    asrc[i] = x + (size_t)(m0 + row) * D_ + c * 8;
    int k = (i * 4 + wid) * 4 + (lane >> 4);
    int cb = (lane & 15) ^ ((k & 3) << 1);
    int col = n0 + cb * 8;
    if (col > F_ - 8) col = F_ - 8;
    bsrc[i] = w1 + (size_t)k * F_ + col;
  }
  auto stage = [&](int kt, int buf) {
    char* sa = smem + buf * G1_STAGE;
    char* sb = sa + 8192;
#pragma unroll
    for (int i = 0; i < 2; ++i)
      __builtin_amdgcn_global_load_lds((const unsigned*)(asrc[i] + kt * 32), (LDS_AS unsigned*)(sa + (i * 4 + wid) * 1024), 16, 0, 0);
#pragma unroll
    for (int i = 0; i < 2; ++i)
      __builtin_amdgcn_global_load_lds((const unsigned*)(bsrc[i] + (size_t)kt * 32 * F_), (LDS_AS unsigned*)(sb + (i * 4 + wid) * 1024), 16, 0, 0);
  };

  f32x16 acc[2][2];
#pragma unroll
  for (int a = 0; a < 2; ++a)
#pragma unroll
    for (int c = 0; c < 2; ++c)
#pragma unroll
      for (int i = 0; i < 16; ++i) acc[a][c][i] = 0.f;

  const int q = (lane & 15) >> 2, p = lane & 3, blk = (lane >> 4) & 1;
  auto compute = [&](int buf) {
    const char* sa = smem + buf * G1_STAGE;
    const char* sb = sa + 8192;
#pragma unroll
    for (int s = 0; s < 2; ++s) {
      bf16x8 af[2], bfr[2];
#pragma unroll
      for (int mt = 0; mt < 2; ++mt) {
        int row = wm * 64 + mt * 32 + r;
        af[mt] = *reinterpret_cast<const bf16x8*>(sa + row * 64 + (((2 * s + h) ^ ((row >> 2) & 3)) << 4));
      }
#pragma unroll
      for (int nt = 0; nt < 2; ++nt)
#pragma unroll
        for (int tt = 0; tt < 2; ++tt) {
          int k = 16 * s + 8 * h + 4 * tt + q;
          int chunk = wn * 8 + nt * 4 + 2 * blk + (p >> 1);
          const char* ad = sb + k * 256 + ((chunk ^ (q << 1)) << 4) + (p & 1) * 8;
          bf16x4 v = __builtin_amdgcn_ds_read_tr16_b64_v4i16((LDS_AS bf16x4*)(ad));
          bfr[nt][4 * tt + 0] = v[0]; bfr[nt][4 * tt + 1] = v[1]; bfr[nt][4 * tt + 2] = v[2]; bfr[nt][4 * tt + 3] = v[3];
        }
#pragma unroll
      for (int mt = 0; mt < 2; ++mt)
#pragma unroll
        for (int nt = 0; nt < 2; ++nt)
          acc[mt][nt] = __builtin_amdgcn_mfma_f32_32x32x16_bf16(af[mt], bfr[nt], acc[mt][nt], 0, 0, 0);
    }
  };

  constexpr int NK = D_ / 32;
  stage(0, 0);
  stage(1, 1);
  int cur = 0, nxt = 2;
  for (int kt = 0; kt < NK - 1; ++kt) {
    asm volatile("s_waitcnt vmcnt(4)" ::: "memory");
    RAW_BARRIER();
    if (kt + 2 < NK) stage(kt + 2, nxt);
    compute(cur);
    cur = (cur == 2) ? 0 : cur + 1;
    nxt = (nxt == 2) ? 0 : nxt + 1;
  }
  asm volatile("s_waitcnt vmcnt(0)" ::: "memory");
  RAW_BARRIER();
  compute(cur);

  RAW_BARRIER();
  {
    unsigned short* ht = reinterpret_cast<unsigned short*>(smem);
#pragma unroll
    for (int nt = 0; nt < 2; ++nt)
#pragma unroll
      for (int mt = 0; mt < 2; ++mt)
#pragma unroll
        for (int i = 0; i < 16; ++i) {
          int row = wm * 64 + mt * 32 + (i & 3) + 8 * (i >> 2) + 4 * h;
          float hv = rbf(acc[mt][nt][i]);
          float sv = hv * __builtin_amdgcn_rcpf(1.0f + __expf(-hv));
          ht[row * 136 + wn * 64 + nt * 32 + r] = f2bf(sv);
        }
    __syncthreads();
#pragma unroll
    for (int i = 0; i < 8; ++i) {
      int id = tid + 256 * i, row = id >> 4, ch = id & 15;
      if (n0 + ch * 8 < F_)
        __builtin_nontemporal_store(*reinterpret_cast<const u32x4*>(ht + row * 136 + ch * 8), reinterpret_cast<u32x4*>(hbuf + ((size_t)((m0 + row) >> 4) * 54 + ((n0 + ch * 8) >> 5)) * 512 + (row & 15) * 32 + (ch & 3) * 8));
    }
  }
}

constexpr int UC_STRIDE = 4384;
constexpr int UC_OFF = 0;
constexpr int KROW = 80;
constexpr int KH_OFF = 8 * UC_STRIDE;
constexpr int KL_OFF = KH_OFF + 64 * KROW + 16;
constexpr int KZ_OFF = KH_OFF + 64 * KROW;
constexpr int PR_OFF = KH_OFF + 12288;
constexpr int OS_OFF = KH_OFF;
constexpr int TB_OFF = PR_OFF + 128 * 16;
constexpr int CONV_LDS = TB_OFF + 768 * 4;

__device__ __forceinline__ float thi_of(int i) {
  int t;
  if (i < 8) t = i;
  else if (i < 16) t = 128 + (i - 8);
  else if (i < 24) t = 256 + 128 * ((i - 16) >> 2) + 2 * ((i - 16) & 3);
  else if (i < 32) t = 512 + 128 * ((i - 24) >> 1) + 4 * ((i - 24) & 1);
  else if (i < 40) t = 1024 + 128 * (i - 32);
  else t = 2048;
  return (float)t;
}

struct KTab { unsigned short v[L_]; };
constexpr KTab make_ktab() {
  KTab t{};
  for (int j = 0; j < L_; ++j) {
    int sh = (j < 256) ? 0 : (j < 512) ? 1 : (j < 1024) ? 2 : 3;
    int rr = j;
    if (sh) {
      int q = j >> sh, rem = j & ((1 << sh) - 1), half = 1 << (sh - 1);
      if (rem > half || (rem == half && (q & 1))) ++q;
      rr = q << sh;
    }
    int i = 40;
    if (rr < 256) i = (rr & 7) + 8 * (rr >> 7);
    else if (rr < 512) i = 16 + 4 * ((rr - 256) >> 7) + ((rr & 7) >> 1);
    else if (rr < 1024) i = 24 + 2 * ((rr - 512) >> 7) + ((rr & 7) >> 2);
    else if (rr < 2048) i = 32 + ((rr - 1024) >> 7);
    t.v[j] = (unsigned short)(i * 16 + ((rr >> 3) & 15));
  }
  return t;
}
__device__ __attribute__((aligned(16))) const KTab KTAB = make_ktab();

__device__ void conv_role(int d, const unsigned short* __restrict__ x, const unsigned short* __restrict__ logAr,
                          const unsigned short* __restrict__ Aim, const unsigned short* __restrict__ Cre,
                          const unsigned short* __restrict__ Cim, const unsigned short* __restrict__ logdt,
                          const unsigned short* __restrict__ Dsk, unsigned short* __restrict__ aT, char* smem) {
  const int tid = threadIdx.x, lane = tid & 63, wid = tid >> 6;

  const int pn = tid & (NS_ - 1);
  const unsigned short r_ldt = logdt[d];
  const unsigned short r_lar = logAr[d * NS_ + pn], r_aim = Aim[d * NS_ + pn];
  const unsigned short r_cre = Cre[d * NS_ + pn], r_cim = Cim[d * NS_ + pn];
  const u32x4 kidx = *reinterpret_cast<const u32x4*>(KTAB.v + 8 * tid);
  __builtin_amdgcn_sched_barrier(0);
  unsigned short ug[8];
#pragma unroll
  for (int k = 0; k < 8; ++k) ug[k] = x[(size_t)(8 * tid + k) * D_ + d];
  __builtin_amdgcn_sched_barrier(0);
  asm volatile("" :: "v"(r_ldt), "v"(r_lar), "v"(r_aim), "v"(r_cre), "v"(r_cim));

  const float dt = rbf(expf(bf2f(r_ldt)));
  if (tid < NS_) {
    float are = -rbf(expf(bf2f(r_lar)));
    float aim = bf2f(r_aim);
    float lr = are * dt, li = aim * dt;
    float cr = bf2f(r_cre) * dt, ci = bf2f(r_cim) * dt;
    float mag = sqrtf(cr * cr + ci * ci);
    f32x4 pp;
    pp[0] = lr * 1.4426950408889634f;
    pp[1] = li * 0.15915494309189535f;
    pp[2] = log2f(mag);
    pp[3] = atan2f(ci, cr) * 0.15915494309189535f;
    *reinterpret_cast<f32x4*>(smem + PR_OFF + tid * 16) = pp;
  }
  if (tid < 64) {
    u32x4 z = {0, 0, 0, 0};
    *reinterpret_cast<u32x4*>(smem + UC_OFF + (tid >> 3) * UC_STRIDE + (tid & 7) * 16) = z;
  }
  RAW_BARRIER();

  {
    const int x = lane & 15, kk = lane >> 4;
    float th[3];
#pragma unroll
    for (int T = 0; T < 3; ++T) th[T] = thi_of(16 * T + x);
    const float tlo = (float)(8 * x);
    f32x4 acc[3];
#pragma unroll
    for (int T = 0; T < 3; ++T) acc[T] = (f32x4){0.f, 0.f, 0.f, 0.f};
#pragma unroll 2
    for (int g = 0; g < 8; ++g) {
      int n = 32 * wid + 4 * g + kk;
      f32x4 pp = *reinterpret_cast<const f32x4*>(smem + PR_OFF + n * 16);
      float qe = __builtin_amdgcn_exp2f(pp[0] * tlo);
      float qa = pp[1] * tlo;
      float qr = qe * __builtin_amdgcn_cosf(qa), qi = qe * __builtin_amdgcn_sinf(qa);
#pragma unroll
      for (int T = 0; T < 3; ++T) {
        float pe = __builtin_amdgcn_exp2f(pp[0] * th[T] + pp[2]);
        float pa = pp[1] * th[T] + pp[3];
        float pr = pe * __builtin_amdgcn_cosf(pa), pi = pe * __builtin_amdgcn_sinf(pa);
        acc[T] = __builtin_amdgcn_mfma_f32_16x16x4f32(qr, pr, acc[T], 0, 0, 0);
        acc[T] = __builtin_amdgcn_mfma_f32_16x16x4f32(-qi, pi, acc[T], 0, 0, 0);
      }
    }
    float* os = reinterpret_cast<float*>(smem + OS_OFF) + wid * 768;
#pragma unroll
    for (int T = 0; T < 3; ++T) *reinterpret_cast<f32x4*>(os + (16 * T + x) * 16 + 4 * kk) = acc[T];
  }
  RAW_BARRIER();

  u32x4 uvec;
#pragma unroll
  for (int j = 0; j < 4; ++j) uvec[j] = (unsigned)ug[2 * j] | ((unsigned)ug[2 * j + 1] << 16);
  *reinterpret_cast<u32x4*>(smem + UC_OFF + 128 + tid * 16) = uvec;
  {
    unsigned* tab = reinterpret_cast<unsigned*>(smem + TB_OFF);
#pragma unroll
    for (int k = 0; k < 3; ++k) {
      int e = tid + 256 * k;
      const float* osp = reinterpret_cast<const float*>(smem + OS_OFF) + e;
      float kv = 2.0f * ((osp[0] + osp[768]) + (osp[1536] + osp[2304]));
      unsigned short hi = f2bf(kv);
      tab[e] = (unsigned)hi | ((unsigned)f2bf(kv - bf2f(hi)) << 16);
    }
  }
  __syncthreads();
  {
    u32x4 v1 = {0, 0, 0, 0};
    if (tid < 255) v1 = *reinterpret_cast<const u32x4*>(smem + UC_OFF + 128 + (tid + 1) * 16);
    unsigned w[8] = {uvec[0], uvec[1], uvec[2], uvec[3], v1[0], v1[1], v1[2], v1[3]};
#pragma unroll
    for (int e = 1; e < 8; ++e) {
      u32x4 o;
#pragma unroll
      for (int j = 0; j < 4; ++j) {
        int base = j + (e >> 1);
        if (e & 1) o[j] = (w[base] >> 16) | (w[base + 1] << 16);
        else o[j] = w[base];
      }
      *reinterpret_cast<u32x4*>(smem + UC_OFF + e * UC_STRIDE + 128 + tid * 16) = o;
    }
    if (tid == 0) {
#pragma unroll
      for (int e = 1; e < 8; ++e)
#pragma unroll
        for (int k = 0; k < e; ++k)
          *reinterpret_cast<unsigned short*>(smem + UC_OFF + e * UC_STRIDE + 2 * (64 - e + k)) =
              (unsigned short)(w[k >> 1] >> (16 * (k & 1)));
    }
  }
  {
    const unsigned* tab = reinterpret_cast<const unsigned*>(smem + TB_OFF);
    unsigned w[8];
#pragma unroll
    for (int k = 0; k < 8; ++k) w[k] = tab[(kidx[k >> 1] >> (16 * (k & 1))) & 0xffff];
    u32x4 vh, vl;
#pragma unroll
    for (int j = 0; j < 4; ++j) {
      vh[j] = (w[7 - 2 * j] & 0xffffu) | (w[6 - 2 * j] << 16);
      vl[j] = (w[7 - 2 * j] >> 16) | (w[6 - 2 * j] & 0xffff0000u);
    }
    *reinterpret_cast<u32x4*>(smem + KH_OFF + (tid >> 2) * KROW + (tid & 3) * 16) = vh;
    *reinterpret_cast<u32x4*>(smem + KL_OFF + (tid >> 2) * KROW + (tid & 3) * 16) = vl;
    if (tid == 0) { u32x4 z = {0, 0, 0, 0}; *reinterpret_cast<u32x4*>(smem + KZ_OFF) = z; }
  }
  __syncthreads();

  f32x16 y0, y1;
#pragma unroll
  for (int i = 0; i < 16; ++i) { y0[i] = 0.f; y1[i] = 0.f; }
  {
    const int r = lane & 31, h = lane >> 5;
    const int e = (r + 1) & 7;
    const char* ubase = smem + UC_OFF + e * UC_STRIDE + 2 * (r - 7 - e + 64 - 8 * h);
#pragma unroll 2
    for (int dl = wid; dl < 32; dl += 4) {
      bf16x8 a0 = *reinterpret_cast<const bf16x8*>(ubase + 64 * dl);
      bf16x8 a1 = *reinterpret_cast<const bf16x8*>(ubase + 64 * dl - 32);
      bf16x8 a2 = *reinterpret_cast<const bf16x8*>(ubase + 64 * dl + 2048);
      bf16x8 a3 = *reinterpret_cast<const bf16x8*>(ubase + 64 * dl + 2048 - 32);
      const int m = r - dl;
      const int off = (m >= 0) ? (m * KROW + 16 * h) : (KZ_OFF - KH_OFF);
      const int off1 = (m >= 0) ? (off + 32) : off;
      const int lo = (m >= 0) ? KL_OFF : KH_OFF;
      bf16x8 fh0 = *reinterpret_cast<const bf16x8*>(smem + KH_OFF + off);
      bf16x8 fh1 = *reinterpret_cast<const bf16x8*>(smem + KH_OFF + off1);
      bf16x8 fl0 = *reinterpret_cast<const bf16x8*>(smem + lo + off);
      bf16x8 fl1 = *reinterpret_cast<const bf16x8*>(smem + lo + off1);
      const int go = (32 + m) * KROW + 16 * h;
      bf16x8 gh0 = *reinterpret_cast<const bf16x8*>(smem + KH_OFF + go);
      bf16x8 gh1 = *reinterpret_cast<const bf16x8*>(smem + KH_OFF + go + 32);
      bf16x8 gl0 = *reinterpret_cast<const bf16x8*>(smem + KL_OFF + go);
      bf16x8 gl1 = *reinterpret_cast<const bf16x8*>(smem + KL_OFF + go + 32);
      y0 = __builtin_amdgcn_mfma_f32_32x32x16_bf16(a0, fh0, y0, 0, 0, 0);
      y1 = __builtin_amdgcn_mfma_f32_32x32x16_bf16(a2, fh0, y1, 0, 0, 0);
      y0 = __builtin_amdgcn_mfma_f32_32x32x16_bf16(a1, fh1, y0, 0, 0, 0);
      y1 = __builtin_amdgcn_mfma_f32_32x32x16_bf16(a3, fh1, y1, 0, 0, 0);
      y0 = __builtin_amdgcn_mfma_f32_32x32x16_bf16(a0, fl0, y0, 0, 0, 0);
      y1 = __builtin_amdgcn_mfma_f32_32x32x16_bf16(a2, fl0, y1, 0, 0, 0);
      y0 = __builtin_amdgcn_mfma_f32_32x32x16_bf16(a1, fl1, y0, 0, 0, 0);
      y1 = __builtin_amdgcn_mfma_f32_32x32x16_bf16(a3, fl1, y1, 0, 0, 0);
      y1 = __builtin_amdgcn_mfma_f32_32x32x16_bf16(a0, gh0, y1, 0, 0, 0);
      y1 = __builtin_amdgcn_mfma_f32_32x32x16_bf16(a1, gh1, y1, 0, 0, 0);
      y1 = __builtin_amdgcn_mfma_f32_32x32x16_bf16(a0, gl0, y1, 0, 0, 0);
      y1 = __builtin_amdgcn_mfma_f32_32x32x16_bf16(a1, gl1, y1, 0, 0, 0);
    }
  }
  __syncthreads();
  {
    const int r = lane & 31, h = lane >> 5;
    float* slab = reinterpret_cast<float*>(smem + wid * 8704);
#pragma unroll
    for (int g = 0; g < 4; ++g) {
      f32x4 v0 = {y0[4 * g], y0[4 * g + 1], y0[4 * g + 2], y0[4 * g + 3]};
      f32x4 v1 = {y1[4 * g], y1[4 * g + 1], y1[4 * g + 2], y1[4 * g + 3]};
      int b = 8 * g + 4 * h;
      *reinterpret_cast<f32x4*>(slab + 32 * r + b) = v0;
      *reinterpret_cast<f32x4*>(slab + 32 * (32 + r) + b) = v1;
    }
  }
  __syncthreads();
  {
    const float dsk = bf2f(Dsk[d]);
    const float* s = reinterpret_cast<const float*>(smem);
    f32x4 ys[2];
#pragma unroll
    for (int hh = 0; hh < 2; ++hh) {
      f32x4 s0 = *reinterpret_cast<const f32x4*>(s + 8 * tid + 4 * hh);
      f32x4 s1 = *reinterpret_cast<const f32x4*>(s + 2176 + 8 * tid + 4 * hh);
      f32x4 s2 = *reinterpret_cast<const f32x4*>(s + 4352 + 8 * tid + 4 * hh);
      f32x4 s3 = *reinterpret_cast<const f32x4*>(s + 6528 + 8 * tid + 4 * hh);
      ys[hh] = (s0 + s1) + (s2 + s3);
    }
    unsigned short o[8];
#pragma unroll
    for (int k = 0; k < 8; ++k) {
      float yb = rbf(ys[k >> 2][k & 3]);
      float uu = bf2f((unsigned short)(uvec[k >> 1] >> (16 * (k & 1))));
      float sk = rbf(uu * dsk);
      o[k] = f2bf(yb + sk);
    }
    u32x4 v;
#pragma unroll
    for (int j = 0; j < 4; ++j) v[j] = o[2 * j] | ((unsigned)o[2 * j + 1] << 16);
    *reinterpret_cast<u32x4*>(aT + (size_t)d * L_ + 8 * tid) = v;
  }
}

__global__ __launch_bounds__(256, 3) void k12(const unsigned short* __restrict__ x, const unsigned short* __restrict__ w1,
                                              const unsigned short* __restrict__ w2, const unsigned short* __restrict__ gw,
                                              const unsigned short* __restrict__ logAr, const unsigned short* __restrict__ Aim,
                                              const unsigned short* __restrict__ Cre, const unsigned short* __restrict__ Cim,
                                              const unsigned short* __restrict__ logdt, const unsigned short* __restrict__ Dsk,
                                              unsigned short* __restrict__ hbuf, unsigned short* __restrict__ w2T,
                                              float* __restrict__ gbuf, unsigned short* __restrict__ aT) {
  extern __shared__ __attribute__((aligned(16))) char smem[];
  const int b = blockIdx.x;
  if (b < B_CONV) gemm1_role(b, x, w1, hbuf, smem);
  else if (b < B_GATE) conv_role(b - B_CONV, x, logAr, Aim, Cre, Cim, logdt, Dsk, aT, smem);
  else if (b < B_W2) gate_role(b - B_GATE, x, gw, gbuf);
  else w2_role(b - B_W2, w2, w2T, smem);
}

constexpr int G2_RS = 84;
__global__ __launch_bounds__(256, 1) void k3_gemm2(const unsigned short* __restrict__ hbuf, const unsigned short* __restrict__ w2T,
                                                   const unsigned short* __restrict__ aT, const float* __restrict__ gbuf,
                                                   unsigned short* __restrict__ out) {
  __shared__ float red[4][64][G2_RS];
  const int tid = threadIdx.x, lane = tid & 63, wid = tid >> 6;
  int t = (blockIdx.x & 7) * 32 + (blockIdx.x >> 3);
  const int tm = t >> 3, tn = t & 7;
  const int m0 = tm * 64, n0 = tn * 80;
  const int fr = lane & 15, fq = lane >> 4;
  const int c0 = (wid * 27) >> 2, c1 = ((wid + 1) * 27) >> 2;

  const unsigned short* ap = hbuf + (size_t)(m0 >> 4) * 54 * 512 + fr * 32 + 8 * fq;
  const unsigned short* bp = w2T + (size_t)(n0 >> 4) * 54 * 512 + fr * 32 + 8 * fq;

  f32x4 acc[4][5];
#pragma unroll
  for (int a = 0; a < 4; ++a)
#pragma unroll
    for (int c = 0; c < 5; ++c) acc[a][c] = (f32x4){0.f, 0.f, 0.f, 0.f};

  bf16x8 A0[4][2], B0[5][2], A1[4][2], B1[5][2];
#define G2_LOAD(AA, BB, c)                                                                             \
  {                                                                                                    \
    _Pragma("unroll") for (int a = 0; a < 4; ++a) {                                                    \
      AA[a][0] = *reinterpret_cast<const bf16x8*>(ap + a * 54 * 512 + (c) * 1024);                \
      AA[a][1] = *reinterpret_cast<const bf16x8*>(ap + a * 54 * 512 + (c) * 1024 + 512);            \
    }                                                                                                  \
    _Pragma("unroll") for (int a = 0; a < 5; ++a) {                                                    \
      BB[a][0] = *reinterpret_cast<const bf16x8*>(bp + a * 54 * 512 + (c) * 1024);                \
      BB[a][1] = *reinterpret_cast<const bf16x8*>(bp + a * 54 * 512 + (c) * 1024 + 512);            \
    }                                                                                                  \
  }
#define G2_COMP(AA, BB)                                                                                \
  {                                                                                                    \
    _Pragma("unroll") for (int s = 0; s < 2; ++s)                                                      \
      _Pragma("unroll") for (int a = 0; a < 4; ++a)                                                    \
        _Pragma("unroll") for (int c = 0; c < 5; ++c)                                                  \
          acc[a][c] = __builtin_amdgcn_mfma_f32_16x16x32_bf16(AA[a][s], BB[c][s], acc[a][c], 0, 0, 0); \
  }
  G2_LOAD(A0, B0, c0);
  for (int c = c0; c < c1; c += 2) {
    if (c + 1 < c1) G2_LOAD(A1, B1, c + 1);
    G2_COMP(A0, B0);
    if (c + 1 >= c1) break;
    if (c + 2 < c1) G2_LOAD(A0, B0, c + 2);
    G2_COMP(A1, B1);
  }
#undef G2_LOAD
#undef G2_COMP

#pragma unroll
  for (int a = 0; a < 4; ++a)
#pragma unroll
    for (int c = 0; c < 5; ++c)
#pragma unroll
      for (int i = 0; i < 4; ++i) red[wid][16 * a + 4 * fq + i][16 * c + fr] = acc[a][c][i];
  __syncthreads();

#pragma unroll
  for (int j = 0; j < 2; ++j) {
    const int col = lane + 64 * j;
    if (col < 80) {
      const int row0 = m0 + 16 * wid;
      u32x4 av0 = *reinterpret_cast<const u32x4*>(aT + (size_t)(n0 + col) * L_ + row0);
      u32x4 av1 = *reinterpret_cast<const u32x4*>(aT + (size_t)(n0 + col) * L_ + row0 + 8);
#pragma unroll
      for (int i = 0; i < 16; ++i) {
        int rr = 16 * wid + i;
        float bv = rbf((red[0][rr][col] + red[1][rr][col]) + (red[2][rr][col] + red[3][rr][col]));
        unsigned aw = (i < 8) ? av0[(i >> 1) & 3] : av1[(i >> 1) & 3];
        float a = bf2f((unsigned short)((aw >> (16 * (i & 1))) & 0xffff));
        float g = gbuf[row0 + i];
        float t1 = rbf(g * a);
        float t2 = rbf(1.0f - g);
        float t3 = rbf(t2 * bv);
        out[(size_t)(row0 + i) * D_ + n0 + col] = f2bf(t1 + t3);
      }
    }
  }
}

extern "C" void kernel_launch(void* const* d_in, const int* in_sizes, int n_in, void* d_out, int out_size, void* d_ws,
                              size_t ws_size, hipStream_t stream) {
  const unsigned short* u = (const unsigned short*)d_in[0];
  const unsigned short* logAr = (const unsigned short*)d_in[1];
  const unsigned short* Aim = (const unsigned short*)d_in[2];
  const unsigned short* Cre = (const unsigned short*)d_in[3];
  const unsigned short* Cim = (const unsigned short*)d_in[4];
  const unsigned short* logdt = (const unsigned short*)d_in[5];
  const unsigned short* Dsk = (const unsigned short*)d_in[6];
  const unsigned short* w1 = (const unsigned short*)d_in[8];
  const unsigned short* w2 = (const unsigned short*)d_in[9];
  const unsigned short* gw = (const unsigned short*)d_in[10];
  unsigned short* out = (unsigned short*)d_out;

  char* ws = (char*)d_ws;
  unsigned short* hbuf = (unsigned short*)ws;
  unsigned short* aT = (unsigned short*)(ws + (size_t)L_ * F_ * 2);
  unsigned short* w2T = (unsigned short*)(ws + (size_t)L_ * F_ * 2 + (size_t)D_ * L_ * 2);
  float* gbuf = (float*)(ws + (size_t)L_ * F_ * 2 + (size_t)D_ * L_ * 2 + (size_t)D_ * F_ * 2);

  constexpr int K12_LDS = (CONV_LDS > 3 * G1_STAGE) ? CONV_LDS : 3 * G1_STAGE;
  hipLaunchKernelGGL(k12, dim3(K12_GRID), dim3(256), K12_LDS, stream, u, w1, w2, gw, logAr, Aim, Cre, Cim, logdt, Dsk,
                     hbuf, w2T, gbuf, aT);
  hipLaunchKernelGGL(k3_gemm2, dim3(256), dim3(256), 0, stream, hbuf, w2T, aT, gbuf, out);
}
```

```cpp
#include <hip/hip_runtime.h>
#include <hip/hip_bf16.h>
#include <stdint.h>

typedef __attribute__((ext_vector_type(8))) short bf16x8;
typedef __attribute__((ext_vector_type(4))) short bf16x4;
typedef __attribute__((ext_vector_type(16))) float f32x16;
typedef __attribute__((ext_vector_type(4))) float f32x4;
typedef __attribute__((ext_vector_type(4))) unsigned int u32x4;
typedef __attribute__((ext_vector_type(2))) unsigned int u32x2;

#define LDS_AS __attribute__((address_space(3)))
#define RAW_BARRIER() do { asm volatile("s_waitcnt lgkmcnt(0)" ::: "memory"); __builtin_amdgcn_s_barrier(); } while (0)

constexpr int L_ = 2048, D_ = 640, NS_ = 128, F_ = 1728;

__device__ __forceinline__ float bf2f(unsigned short v) { return __uint_as_float(((unsigned)v) << 16); }
__device__ __forceinline__ unsigned short f2bf(float f) {
  __bf16 b = (__bf16)f;
  return __builtin_bit_cast(unsigned short, b);
}
__device__ __forceinline__ float rbf(float f) { return bf2f(f2bf(f)); }

constexpr int G1_TM = L_ / 128, G1_TN = (F_ + 127) / 128;
constexpr int N_G1 = G1_TM * G1_TN;
constexpr int G1_STAGE = 16384;
constexpr int N_GATE = 32, N_W2 = 90;
constexpr int B_CONV = N_G1, B_GATE = B_CONV + D_, B_W2 = B_GATE + N_GATE, K12_GRID = B_W2 + N_W2;

__device__ __forceinline__ void tr_tiles_load(const unsigned short* __restrict__ src, int ss, int r0, int c0,
                                              u32x4 (&v)[2]) {
  const int tid = threadIdx.x;
#pragma unroll
  for (int i = 0; i < 2; ++i) {
    int id = tid + 256 * i, row = id >> 3, ch = id & 7;
    v[i] = *reinterpret_cast<const u32x4*>(src + (size_t)(r0 + row) * ss + c0 + ch * 8);
  }
}
__device__ __forceinline__ void tr_tiles_put(unsigned short (*tile)[72], const u32x4 (&v)[2]) {
  const int tid = threadIdx.x;
#pragma unroll
  for (int i = 0; i < 2; ++i) {
    int id = tid + 256 * i, row = id >> 3, ch = id & 7;
    *reinterpret_cast<u32x4*>(&tile[row][ch * 8]) = v[i];
  }
}
__device__ __forceinline__ void tr_tiles_store_blocked(unsigned short (*tile)[72], unsigned short* __restrict__ dst,
                                                       int r0, int c0) {
  const int tid = threadIdx.x;
#pragma unroll
  for (int i = 0; i < 2; ++i) {
    int id = tid + 256 * i, col = id >> 3, ch = id & 7;
    unsigned short o[8];
#pragma unroll
    for (int k = 0; k < 8; ++k) o[k] = tile[ch * 8 + k][col];
    u32x4 w;
#pragma unroll
    for (int j = 0; j < 4; ++j) w[j] = o[2 * j] | ((unsigned)o[2 * j + 1] << 16);
    unsigned short* dp = dst + ((size_t)((c0 + col) >> 4) * 54 + ((r0 + ch * 8) >> 5)) * 512 + (col & 15) * 32 + (ch & 3) * 8;
    *reinterpret_cast<u32x4*>(dp) = w;
  }
}

__device__ void gate_role(int gb, const unsigned short* __restrict__ x, const unsigned short* __restrict__ gw,
                          float* __restrict__ gbuf) {
  const int tid = threadIdx.x;
  const int r0 = gb * 64;
  u32x4 v[10][2];
#pragma unroll
  for (int t = 0; t < 10; ++t) tr_tiles_load(x, D_, r0, t * 64, v[t]);
  float s[2] = {0.f, 0.f};
#pragma unroll
  for (int t = 0; t < 10; ++t) {
    u32x4 g = *reinterpret_cast<const u32x4*>(gw + t * 64 + (tid & 7) * 8);
#pragma unroll
    for (int i = 0; i < 2; ++i)
#pragma unroll
      for (int j = 0; j < 4; ++j) {
        s[i] += rbf(__uint_as_float(v[t][i][j] << 16) * __uint_as_float(g[j] << 16));
        s[i] += rbf(__uint_as_float(v[t][i][j] & 0xffff0000u) * __uint_as_float(g[j] & 0xffff0000u));
      }
  }
#pragma unroll
  for (int i = 0; i < 2; ++i) {
    s[i] += __shfl_xor(s[i], 1); s[i] += __shfl_xor(s[i], 2); s[i] += __shfl_xor(s[i], 4);
    if ((tid & 7) == 0) gbuf[r0 + (tid >> 3) + 32 * i] = rbf(1.0f / (1.0f + expf(-rbf(s[i]))));
  }
}

__device__ void w2_role(int wb, const unsigned short* __restrict__ w2, unsigned short* __restrict__ w2T, char* smem) {
  u32x4 tv[3][2];
#pragma unroll
  for (int j = 0; j < 3; ++j) {
    int id = wb * 3 + j;
    tr_tiles_load(w2, D_, (id / 10) * 64, (id % 10) * 64, tv[j]);
  }
  typedef unsigned short tile_t[64][72];
  tile_t* tiles = reinterpret_cast<tile_t*>(smem);
#pragma unroll
  for (int j = 0; j < 3; ++j) tr_tiles_put(tiles[j], tv[j]);
  __syncthreads();
#pragma unroll
  for (int j = 0; j < 3; ++j) {
    int id = wb * 3 + j;
    tr_tiles_store_blocked(tiles[j], w2T, (id / 10) * 64, (id % 10) * 64);
  }
}

__device__ void gemm1_role(int b, const unsigned short* __restrict__ x, const unsigned short* __restrict__ w1,
                           unsigned short* __restrict__ hbuf, char* smem) {
  const int tid = threadIdx.x, lane = tid & 63, wid = tid >> 6;
  int t = (b & 7) * (N_G1 / 8) + (b >> 3);
  const int tm = t / G1_TN, tn = t % G1_TN;
  const int m0 = tm * 128, n0 = tn * 128;
  const int wm = wid >> 1, wn = wid & 1, r = lane & 31, h = lane >> 5;

  const unsigned short* asrc[2];
  const unsigned short* bsrc[2];
#pragma unroll
  for (int i = 0; i < 2; ++i) {
    int row = (i * 4 + wid) * 16 + (lane >> 2);
    int c = (lane & 3) ^ ((row >> 2) & 3);
    asrc[i] = x + (size_t)(m0 + row) * D_ + c * 8;
    int k = (i * 4 + wid) * 4 + (lane >> 4);
    int cb = (lane & 15) ^ ((k & 3) << 1);
    int col = n0 + cb * 8;
    if (col > F_ - 8) col = F_ - 8;
    bsrc[i] = w1 + (size_t)k * F_ + col;
  }
  auto stage = [&](int kt, int buf) {
    char* sa = smem + buf * G1_STAGE;
    char* sb = sa + 8192;
#pragma unroll
    for (int i = 0; i < 2; ++i)
      __builtin_amdgcn_global_load_lds((const unsigned*)(asrc[i] + kt * 32), (LDS_AS unsigned*)(sa + (i * 4 + wid) * 1024), 16, 0, 0);
#pragma unroll
    for (int i = 0; i < 2; ++i)
      __builtin_amdgcn_global_load_lds((const unsigned*)(bsrc[i] + (size_t)kt * 32 * F_), (LDS_AS unsigned*)(sb + (i * 4 + wid) * 1024), 16, 0, 0);
  };

  f32x16 acc[2][2];
#pragma unroll
  for (int a = 0; a < 2; ++a)
#pragma unroll
    for (int c = 0; c < 2; ++c)
#pragma unroll
      for (int i = 0; i < 16; ++i) acc[a][c][i] = 0.f;

  const int q = (lane & 15) >> 2, p = lane & 3, blk = (lane >> 4) & 1;
  auto compute = [&](int buf) {
    const char* sa = smem + buf * G1_STAGE;
    const char* sb = sa + 8192;
#pragma unroll
    for (int s = 0; s < 2; ++s) {
      bf16x8 af[2], bfr[2];
#pragma unroll
      for (int mt = 0; mt < 2; ++mt) {
        int row = wm * 64 + mt * 32 + r;
        af[mt] = *reinterpret_cast<const bf16x8*>(sa + row * 64 + (((2 * s + h) ^ ((row >> 2) & 3)) << 4));
      }
#pragma unroll
      for (int nt = 0; nt < 2; ++nt)
#pragma unroll
        for (int tt = 0; tt < 2; ++tt) {
          int k = 16 * s + 8 * h + 4 * tt + q;
          int chunk = wn * 8 + nt * 4 + 2 * blk + (p >> 1);
          const char* ad = sb + k * 256 + ((chunk ^ (q << 1)) << 4) + (p & 1) * 8;
          bf16x4 v = __builtin_amdgcn_ds_read_tr16_b64_v4i16((LDS_AS bf16x4*)(ad));
          bfr[nt][4 * tt + 0] = v[0]; bfr[nt][4 * tt + 1] = v[1]; bfr[nt][4 * tt + 2] = v[2]; bfr[nt][4 * tt + 3] = v[3];
        }
#pragma unroll
      for (int mt = 0; mt < 2; ++mt)
#pragma unroll
        for (int nt = 0; nt < 2; ++nt)
          acc[mt][nt] = __builtin_amdgcn_mfma_f32_32x32x16_bf16(af[mt], bfr[nt], acc[mt][nt], 0, 0, 0);
    }
  };

  constexpr int NK = D_ / 32;
  stage(0, 0);
  stage(1, 1);
  int cur = 0, nxt = 2;
  for (int kt = 0; kt < NK - 1; ++kt) {
    asm volatile("s_waitcnt vmcnt(4)" ::: "memory");
    RAW_BARRIER();
    if (kt + 2 < NK) stage(kt + 2, nxt);
    compute(cur);
    cur = (cur == 2) ? 0 : cur + 1;
    nxt = (nxt == 2) ? 0 : nxt + 1;
  }
  asm volatile("s_waitcnt vmcnt(0)" ::: "memory");
  RAW_BARRIER();
  compute(cur);

  RAW_BARRIER();
  {
    unsigned short* ht = reinterpret_cast<unsigned short*>(smem);
#pragma unroll
    for (int nt = 0; nt < 2; ++nt)
#pragma unroll
      for (int mt = 0; mt < 2; ++mt)
#pragma unroll
        for (int i = 0; i < 16; ++i) {
          int row = wm * 64 + mt * 32 + (i & 3) + 8 * (i >> 2) + 4 * h;
          float hv = rbf(acc[mt][nt][i]);
          float sv = hv * __builtin_amdgcn_rcpf(1.0f + __expf(-hv));
          ht[row * 136 + wn * 64 + nt * 32 + r] = f2bf(sv);
        }
    __syncthreads();
#pragma unroll
    for (int i = 0; i < 8; ++i) {
      int id = tid + 256 * i, row = id >> 4, ch = id & 15;
      if (n0 + ch * 8 < F_)
        *reinterpret_cast<u32x4*>(hbuf + ((size_t)((m0 + row) >> 4) * 54 + ((n0 + ch * 8) >> 5)) * 512 + (row & 15) * 32 + (ch & 3) * 8) = *reinterpret_cast<const u32x4*>(ht + row * 136 + ch * 8);
    }
  }
}

constexpr int UC_STRIDE = 4384;
constexpr int UC_OFF = 0;
constexpr int KROW = 80;
constexpr int KH_OFF = 8 * UC_STRIDE;
constexpr int KL_OFF = KH_OFF + 64 * KROW + 16;
constexpr int KZ_OFF = KH_OFF + 64 * KROW;
constexpr int PR_OFF = KH_OFF + 12288;
constexpr int OS_OFF = KH_OFF;
constexpr int TB_OFF = PR_OFF + 128 * 16;
constexpr int CONV_LDS = TB_OFF + 768 * 4;

__device__ __forceinline__ float thi_of(int i) {
  int t;
  if (i < 8) t = i;
  else if (i < 16) t = 128 + (i - 8);
  else if (i < 24) t = 256 + 128 * ((i - 16) >> 2) + 2 * ((i - 16) & 3);
  else if (i < 32) t = 512 + 128 * ((i - 24) >> 1) + 4 * ((i - 24) & 1);
  else if (i < 40) t = 1024 + 128 * (i - 32);
  else t = 2048;
  return (float)t;
}

struct KTab { unsigned short v[L_]; };
constexpr KTab make_ktab() {
  KTab t{};
  for (int j = 0; j < L_; ++j) {
    int sh = (j < 256) ? 0 : (j < 512) ? 1 : (j < 1024) ? 2 : 3;
    int rr = j;
    if (sh) {
      int q = j >> sh, rem = j & ((1 << sh) - 1), half = 1 << (sh - 1);
      if (rem > half || (rem == half && (q & 1))) ++q;
      rr = q << sh;
    }
    int i = 40;
    if (rr < 256) i = (rr & 7) + 8 * (rr >> 7);
    else if (rr < 512) i = 16 + 4 * ((rr - 256) >> 7) + ((rr & 7) >> 1);
    else if (rr < 1024) i = 24 + 2 * ((rr - 512) >> 7) + ((rr & 7) >> 2);
    else if (rr < 2048) i = 32 + ((rr - 1024) >> 7);
    t.v[j] = (unsigned short)(i * 16 + ((rr >> 3) & 15));
  }
  return t;
}
__device__ __attribute__((aligned(16))) const KTab KTAB = make_ktab();

__device__ void conv_role(int d, const unsigned short* __restrict__ x, const unsigned short* __restrict__ logAr,
                          const unsigned short* __restrict__ Aim, const unsigned short* __restrict__ Cre,
                          const unsigned short* __restrict__ Cim, const unsigned short* __restrict__ logdt,
                          const unsigned short* __restrict__ Dsk, unsigned short* __restrict__ aT, char* smem) {
  const int tid = threadIdx.x, lane = tid & 63, wid = tid >> 6;

  const int pn = tid & (NS_ - 1);
  const unsigned short r_ldt = logdt[d];
  const unsigned short r_lar = logAr[d * NS_ + pn], r_aim = Aim[d * NS_ + pn];
  const unsigned short r_cre = Cre[d * NS_ + pn], r_cim = Cim[d * NS_ + pn];
  const u32x4 kidx = *reinterpret_cast<const u32x4*>(KTAB.v + 8 * tid);
  __builtin_amdgcn_sched_barrier(0);
  unsigned short ug[8];
#pragma unroll
  for (int k = 0; k < 8; ++k) ug[k] = x[(size_t)(8 * tid + k) * D_ + d];
  __builtin_amdgcn_sched_barrier(0);
  asm volatile("" :: "v"(r_ldt), "v"(r_lar), "v"(r_aim), "v"(r_cre), "v"(r_cim));

  const float dt = rbf(expf(bf2f(r_ldt)));
  if (tid < NS_) {
    float are = -rbf(expf(bf2f(r_lar)));
    float aim = bf2f(r_aim);
    float lr = are * dt, li = aim * dt;
    float cr = bf2f(r_cre) * dt, ci = bf2f(r_cim) * dt;
    float mag = sqrtf(cr * cr + ci * ci);
    f32x4 pp;
    pp[0] = lr * 1.4426950408889634f;
    pp[1] = li * 0.15915494309189535f;
    pp[2] = log2f(mag);
    pp[3] = atan2f(ci, cr) * 0.15915494309189535f;
    *reinterpret_cast<f32x4*>(smem + PR_OFF + tid * 16) = pp;
  }
  if (tid < 64) {
    u32x4 z = {0, 0, 0, 0};
    *reinterpret_cast<u32x4*>(smem + UC_OFF + (tid >> 3) * UC_STRIDE + (tid & 7) * 16) = z;
  }
  RAW_BARRIER();

  {
    const int x = lane & 15, kk = lane >> 4;
    float th[3];
#pragma unroll
    for (int T = 0; T < 3; ++T) th[T] = thi_of(16 * T + x);
    const float tlo = (float)(8 * x);
    f32x4 acc[3];
#pragma unroll
    for (int T = 0; T < 3; ++T) acc[T] = (f32x4){0.f, 0.f, 0.f, 0.f};
#pragma unroll 2
    for (int g = 0; g < 8; ++g) {
      int n = 32 * wid + 4 * g + kk;
      f32x4 pp = *reinterpret_cast<const f32x4*>(smem + PR_OFF + n * 16);
      float qe = __builtin_amdgcn_exp2f(pp[0] * tlo);
      float qa = pp[1] * tlo;
      float qr = qe * __builtin_amdgcn_cosf(qa), qi = qe * __builtin_amdgcn_sinf(qa);
#pragma unroll
      for (int T = 0; T < 3; ++T) {
        float pe = __builtin_amdgcn_exp2f(pp[0] * th[T] + pp[2]);
        float pa = pp[1] * th[T] + pp[3];
        float pr = pe * __builtin_amdgcn_cosf(pa), pi = pe * __builtin_amdgcn_sinf(pa);
        acc[T] = __builtin_amdgcn_mfma_f32_16x16x4f32(qr, pr, acc[T], 0, 0, 0);
        acc[T] = __builtin_amdgcn_mfma_f32_16x16x4f32(-qi, pi, acc[T], 0, 0, 0);
      }
    }
    float* os = reinterpret_cast<float*>(smem + OS_OFF) + wid * 768;
#pragma unroll
    for (int T = 0; T < 3; ++T) *reinterpret_cast<f32x4*>(os + (16 * T + x) * 16 + 4 * kk) = acc[T];
  }
  RAW_BARRIER();

  u32x4 uvec;
#pragma unroll
  for (int j = 0; j < 4; ++j) uvec[j] = (unsigned)ug[2 * j] | ((unsigned)ug[2 * j + 1] << 16);
  *reinterpret_cast<u32x4*>(smem + UC_OFF + 128 + tid * 16) = uvec;
  {
    unsigned* tab = reinterpret_cast<unsigned*>(smem + TB_OFF);
#pragma unroll
    for (int k = 0; k < 3; ++k) {
      int e = tid + 256 * k;
      const float* osp = reinterpret_cast<const float*>(smem + OS_OFF) + e;
      float kv = 2.0f * ((osp[0] + osp[768]) + (osp[1536] + osp[2304]));
      unsigned short hi = f2bf(kv);
      tab[e] = (unsigned)hi | ((unsigned)f2bf(kv - bf2f(hi)) << 16);
    }
  }
  __syncthreads();
  {
    u32x4 v1 = {0, 0, 0, 0};
    if (tid < 255) v1 = *reinterpret_cast<const u32x4*>(smem + UC_OFF + 128 + (tid + 1) * 16);
    unsigned w[8] = {uvec[0], uvec[1], uvec[2], uvec[3], v1[0], v1[1], v1[2], v1[3]};
#pragma unroll
    for (int e = 1; e < 8; ++e) {
      u32x4 o;
#pragma unroll
      for (int j = 0; j < 4; ++j) {
        int base = j + (e >> 1);
        if (e & 1) o[j] = (w[base] >> 16) | (w[base + 1] << 16);
        else o[j] = w[base];
      }
      *reinterpret_cast<u32x4*>(smem + UC_OFF + e * UC_STRIDE + 128 + tid * 16) = o;
    }
    if (tid == 0) {
#pragma unroll
      for (int e = 1; e < 8; ++e)
#pragma unroll
        for (int k = 0; k < e; ++k)
          *reinterpret_cast<unsigned short*>(smem + UC_OFF + e * UC_STRIDE + 2 * (64 - e + k)) =
              (unsigned short)(w[k >> 1] >> (16 * (k & 1)));
    }
  }
  {
    const unsigned* tab = reinterpret_cast<const unsigned*>(smem + TB_OFF);
    unsigned w[8];
#pragma unroll
    for (int k = 0; k < 8; ++k) w[k] = tab[(kidx[k >> 1] >> (16 * (k & 1))) & 0xffff];
    u32x4 vh, vl;
#pragma unroll
    for (int j = 0; j < 4; ++j) {
      vh[j] = (w[7 - 2 * j] & 0xffffu) | (w[6 - 2 * j] << 16);
      vl[j] = (w[7 - 2 * j] >> 16) | (w[6 - 2 * j] & 0xffff0000u);
    }
    *reinterpret_cast<u32x4*>(smem + KH_OFF + (tid >> 2) * KROW + (tid & 3) * 16) = vh;
    *reinterpret_cast<u32x4*>(smem + KL_OFF + (tid >> 2) * KROW + (tid & 3) * 16) = vl;
    if (tid == 0) { u32x4 z = {0, 0, 0, 0}; *reinterpret_cast<u32x4*>(smem + KZ_OFF) = z; }
  }
  __syncthreads();

  f32x16 y0, y1;
#pragma unroll
  for (int i = 0; i < 16; ++i) { y0[i] = 0.f; y1[i] = 0.f; }
  {
    const int r = lane & 31, h = lane >> 5;
    const int e = (r + 1) & 7;
    const char* ubase = smem + UC_OFF + e * UC_STRIDE + 2 * (r - 7 - e + 64 - 8 * h);
#pragma unroll 2
    for (int dl = wid; dl < 32; dl += 4) {
      bf16x8 a0 = *reinterpret_cast<const bf16x8*>(ubase + 64 * dl);
      bf16x8 a1 = *reinterpret_cast<const bf16x8*>(ubase + 64 * dl - 32);
      bf16x8 a2 = *reinterpret_cast<const bf16x8*>(ubase + 64 * dl + 2048);
      bf16x8 a3 = *reinterpret_cast<const bf16x8*>(ubase + 64 * dl + 2048 - 32);
      const int m = r - dl;
      const int off = (m >= 0) ? (m * KROW + 16 * h) : (KZ_OFF - KH_OFF);
      const int off1 = (m >= 0) ? (off + 32) : off;
      const int lo = (m >= 0) ? KL_OFF : KH_OFF;
      bf16x8 fh0 = *reinterpret_cast<const bf16x8*>(smem + KH_OFF + off);
      bf16x8 fh1 = *reinterpret_cast<const bf16x8*>(smem + KH_OFF + off1);
      bf16x8 fl0 = *reinterpret_cast<const bf16x8*>(smem + lo + off);
      bf16x8 fl1 = *reinterpret_cast<const bf16x8*>(smem + lo + off1);
      const int go = (32 + m) * KROW + 16 * h;
      bf16x8 gh0 = *reinterpret_cast<const bf16x8*>(smem + KH_OFF + go);
      bf16x8 gh1 = *reinterpret_cast<const bf16x8*>(smem + KH_OFF + go + 32);
      bf16x8 gl0 = *reinterpret_cast<const bf16x8*>(smem + KL_OFF + go);
      bf16x8 gl1 = *reinterpret_cast<const bf16x8*>(smem + KL_OFF + go + 32);
      y0 = __builtin_amdgcn_mfma_f32_32x32x16_bf16(a0, fh0, y0, 0, 0, 0);
      y1 = __builtin_amdgcn_mfma_f32_32x32x16_bf16(a2, fh0, y1, 0, 0, 0);
      y0 = __builtin_amdgcn_mfma_f32_32x32x16_bf16(a1, fh1, y0, 0, 0, 0);
      y1 = __builtin_amdgcn_mfma_f32_32x32x16_bf16(a3, fh1, y1, 0, 0, 0);
      y0 = __builtin_amdgcn_mfma_f32_32x32x16_bf16(a0, fl0, y0, 0, 0, 0);
      y1 = __builtin_amdgcn_mfma_f32_32x32x16_bf16(a2, fl0, y1, 0, 0, 0);
      y0 = __builtin_amdgcn_mfma_f32_32x32x16_bf16(a1, fl1, y0, 0, 0, 0);
      y1 = __builtin_amdgcn_mfma_f32_32x32x16_bf16(a3, fl1, y1, 0, 0, 0);
      y1 = __builtin_amdgcn_mfma_f32_32x32x16_bf16(a0, gh0, y1, 0, 0, 0);
      y1 = __builtin_amdgcn_mfma_f32_32x32x16_bf16(a1, gh1, y1, 0, 0, 0);
      y1 = __builtin_amdgcn_mfma_f32_32x32x16_bf16(a0, gl0, y1, 0, 0, 0);
      y1 = __builtin_amdgcn_mfma_f32_32x32x16_bf16(a1, gl1, y1, 0, 0, 0);
    }
  }
  __syncthreads();
  {
    const int r = lane & 31, h = lane >> 5;
    float* slab = reinterpret_cast<float*>(smem + wid * 8704);
#pragma unroll
    for (int g = 0; g < 4; ++g) {
      f32x4 v0 = {y0[4 * g], y0[4 * g + 1], y0[4 * g + 2], y0[4 * g + 3]};
      f32x4 v1 = {y1[4 * g], y1[4 * g + 1], y1[4 * g + 2], y1[4 * g + 3]};
      int b = 8 * g + 4 * h;
      *reinterpret_cast<f32x4*>(slab + 32 * r + b) = v0;
      *reinterpret_cast<f32x4*>(slab + 32 * (32 + r) + b) = v1;
    }
  }
  __syncthreads();
  {
    const float dsk = bf2f(Dsk[d]);
    const float* s = reinterpret_cast<const float*>(smem);
    f32x4 ys[2];
#pragma unroll
    for (int hh = 0; hh < 2; ++hh) {
      f32x4 s0 = *reinterpret_cast<const f32x4*>(s + 8 * tid + 4 * hh);
      f32x4 s1 = *reinterpret_cast<const f32x4*>(s + 2176 + 8 * tid + 4 * hh);
      f32x4 s2 = *reinterpret_cast<const f32x4*>(s + 4352 + 8 * tid + 4 * hh);
      f32x4 s3 = *reinterpret_cast<const f32x4*>(s + 6528 + 8 * tid + 4 * hh);
      ys[hh] = (s0 + s1) + (s2 + s3);
    }
    unsigned short o[8];
#pragma unroll
    for (int k = 0; k < 8; ++k) {
      float yb = rbf(ys[k >> 2][k & 3]);
      float uu = bf2f((unsigned short)(uvec[k >> 1] >> (16 * (k & 1))));
      float sk = rbf(uu * dsk);
      o[k] = f2bf(yb + sk);
    }
    u32x4 v;
#pragma unroll
    for (int j = 0; j < 4; ++j) v[j] = o[2 * j] | ((unsigned)o[2 * j + 1] << 16);
    *reinterpret_cast<u32x4*>(aT + (size_t)d * L_ + 8 * tid) = v;
  }
}

__global__ __launch_bounds__(256, 3) void k12(const unsigned short* __restrict__ x, const unsigned short* __restrict__ w1,
                                              const unsigned short* __restrict__ w2, const unsigned short* __restrict__ gw,
                                              const unsigned short* __restrict__ logAr, const unsigned short* __restrict__ Aim,
                                              const unsigned short* __restrict__ Cre, const unsigned short* __restrict__ Cim,
                                              const unsigned short* __restrict__ logdt, const unsigned short* __restrict__ Dsk,
                                              unsigned short* __restrict__ hbuf, unsigned short* __restrict__ w2T,
                                              float* __restrict__ gbuf, unsigned short* __restrict__ aT) {
  extern __shared__ __attribute__((aligned(16))) char smem[];
  const int b = blockIdx.x;
  if (b < B_CONV) gemm1_role(b, x, w1, hbuf, smem);
  else if (b < B_GATE) conv_role(b - B_CONV, x, logAr, Aim, Cre, Cim, logdt, Dsk, aT, smem);
  else if (b < B_W2) gate_role(b - B_GATE, x, gw, gbuf);
  else w2_role(b - B_W2, w2, w2T, smem);
}

constexpr int G2_RS = 84;
__global__ __launch_bounds__(256, 1) void k3_gemm2(const unsigned short* __restrict__ hbuf, const unsigned short* __restrict__ w2T,
                                                   const unsigned short* __restrict__ aT, const float* __restrict__ gbuf,
                                                   unsigned short* __restrict__ out) {
  __shared__ float red[4][64][G2_RS];
  const int tid = threadIdx.x, lane = tid & 63, wid = tid >> 6;
  int t = (blockIdx.x & 7) * 32 + (blockIdx.x >> 3);
  const int tm = t >> 3, tn = t & 7;
  const int m0 = tm * 64, n0 = tn * 80;
  const int fr = lane & 15, fq = lane >> 4;
  const int wv = __builtin_amdgcn_readfirstlane(wid);
  const int c0 = (wv * 27) >> 2;
  const int nch = (((wv + 1) * 27) >> 2) - c0;

  const unsigned short* ap = hbuf + (size_t)(m0 >> 4) * 54 * 512 + fr * 32 + 8 * fq + c0 * 1024;
  const unsigned short* bp = w2T + (size_t)(n0 >> 4) * 54 * 512 + fr * 32 + 8 * fq + c0 * 1024;

  f32x4 acc[4][5];
#pragma unroll
  for (int a = 0; a < 4; ++a)
#pragma unroll
    for (int c = 0; c < 5; ++c) acc[a][c] = (f32x4){0.f, 0.f, 0.f, 0.f};

  constexpr int PD = 2;
  bf16x8 A[PD][4][2], B[PD][5][2];
#define G2_LOAD(S, c)                                                                                  \
  {                                                                                                    \
    _Pragma("unroll") for (int a = 0; a < 4; ++a) {                                                    \
      A[S][a][0] = *reinterpret_cast<const bf16x8*>(ap + a * 54 * 512 + (c) * 1024);                   \
      A[S][a][1] = *reinterpret_cast<const bf16x8*>(ap + a * 54 * 512 + (c) * 1024 + 512);             \
    }                                                                                                  \
    _Pragma("unroll") for (int a = 0; a < 5; ++a) {                                                    \
      B[S][a][0] = *reinterpret_cast<const bf16x8*>(bp + a * 54 * 512 + (c) * 1024);                   \
      B[S][a][1] = *reinterpret_cast<const bf16x8*>(bp + a * 54 * 512 + (c) * 1024 + 512);             \
    }                                                                                                  \
  }
#define G2_COMP(S)                                                                                     \
  {                                                                                                    \
    _Pragma("unroll") for (int s = 0; s < 2; ++s)                                                      \
      _Pragma("unroll") for (int a = 0; a < 4; ++a)                                                    \
        _Pragma("unroll") for (int c = 0; c < 5; ++c)                                                  \
          acc[a][c] = __builtin_amdgcn_mfma_f32_16x16x32_bf16(A[S][a][s], B[S][c][s], acc[a][c], 0, 0, 0); \
  }
#pragma unroll
  for (int i = 0; i < PD - 1; ++i) {
    G2_LOAD(i, i);
    __builtin_amdgcn_sched_barrier(0);
  }
#pragma unroll
  for (int i = 0; i < 7; ++i) {
    if (i + PD - 1 < 7) {
      const int cn = (i + PD - 1 < 6) ? (i + PD - 1) : (nch - 1);
      G2_LOAD((i + PD - 1) % PD, cn);
    }
    __builtin_amdgcn_sched_barrier(0);
    if (i == 6 && nch != 7) {
      const bf16x8 z = {0, 0, 0, 0, 0, 0, 0, 0};
#pragma unroll
      for (int a = 0; a < 4; ++a) { A[i % PD][a][0] = z; A[i % PD][a][1] = z; }
#pragma unroll
      for (int a = 0; a < 5; ++a) { B[i % PD][a][0] = z; B[i % PD][a][1] = z; }
    }
    G2_COMP(i % PD);
    __builtin_amdgcn_sched_barrier(0);
  }
#undef G2_LOAD
#undef G2_COMP

#pragma unroll
  for (int a = 0; a < 4; ++a)
#pragma unroll
    for (int c = 0; c < 5; ++c)
#pragma unroll
      for (int i = 0; i < 4; ++i) red[wid][16 * a + 4 * fq + i][16 * c + fr] = acc[a][c][i];
  __syncthreads();

#pragma unroll
  for (int j = 0; j < 2; ++j) {
    const int col = lane + 64 * j;
    if (col < 80) {
      const int row0 = m0 + 16 * wid;
      u32x4 av0 = *reinterpret_cast<const u32x4*>(aT + (size_t)(n0 + col) * L_ + row0);
      u32x4 av1 = *reinterpret_cast<const u32x4*>(aT + (size_t)(n0 + col) * L_ + row0 + 8);
#pragma unroll
      for (int i = 0; i < 16; ++i) {
        int rr = 16 * wid + i;
        float bv = rbf((red[0][rr][col] + red[1][rr][col]) + (red[2][rr][col] + red[3][rr][col]));
        unsigned aw = (i < 8) ? av0[(i >> 1) & 3] : av1[(i >> 1) & 3];
        float a = bf2f((unsigned short)((aw >> (16 * (i & 1))) & 0xffff));
        float g = gbuf[row0 + i];
        float t1 = rbf(g * a);
        float t2 = rbf(1.0f - g);
        float t3 = rbf(t2 * bv);
        out[(size_t)(row0 + i) * D_ + n0 + col] = f2bf(t1 + t3);
      }
    }
  }
}

extern "C" void kernel_launch(void* const* d_in, const int* in_sizes, int n_in, void* d_out, int out_size, void* d_ws,
                              size_t ws_size, hipStream_t stream) {
  const unsigned short* u = (const unsigned short*)d_in[0];
  const unsigned short* logAr = (const unsigned short*)d_in[1];
  const unsigned short* Aim = (const unsigned short*)d_in[2];
  const unsigned short* Cre = (const unsigned short*)d_in[3];
  const unsigned short* Cim = (const unsigned short*)d_in[4];
  const unsigned short* logdt = (const unsigned short*)d_in[5];
  const unsigned short* Dsk = (const unsigned short*)d_in[6];
  const unsigned short* w1 = (const unsigned short*)d_in[8];
  const unsigned short* w2 = (const unsigned short*)d_in[9];
  const unsigned short* gw = (const unsigned short*)d_in[10];
  unsigned short* out = (unsigned short*)d_out;

  char* ws = (char*)d_ws;
  unsigned short* hbuf = (unsigned short*)ws;
  unsigned short* aT = (unsigned short*)(ws + (size_t)L_ * F_ * 2);
  unsigned short* w2T = (unsigned short*)(ws + (size_t)L_ * F_ * 2 + (size_t)D_ * L_ * 2);
  float* gbuf = (float*)(ws + (size_t)L_ * F_ * 2 + (size_t)D_ * L_ * 2 + (size_t)D_ * F_ * 2);

  constexpr int K12_LDS = (CONV_LDS > 3 * G1_STAGE) ? CONV_LDS : 3 * G1_STAGE;
  hipLaunchKernelGGL(k12, dim3(K12_GRID), dim3(256), K12_LDS, stream, u, w1, w2, gw, logAr, Aim, Cre, Cim, logdt, Dsk,
                     hbuf, w2T, gbuf, aT);
  hipLaunchKernelGGL(k3_gemm2, dim3(256), dim3(256), 0, stream, hbuf, w2T, aT, gbuf, out);
}
```

```cpp
#include <hip/hip_runtime.h>
#include <hip/hip_bf16.h>
#include <stdint.h>

typedef __attribute__((ext_vector_type(8))) short bf16x8;
typedef __attribute__((ext_vector_type(4))) short bf16x4;
typedef __attribute__((ext_vector_type(16))) float f32x16;
typedef __attribute__((ext_vector_type(4))) float f32x4;
typedef __attribute__((ext_vector_type(4))) unsigned int u32x4;
typedef __attribute__((ext_vector_type(2))) unsigned int u32x2;

#define LDS_AS __attribute__((address_space(3)))
#define RAW_BARRIER() do { asm volatile("s_waitcnt lgkmcnt(0)" ::: "memory"); __builtin_amdgcn_s_barrier(); } while (0)

constexpr int L_ = 2048, D_ = 640, NS_ = 128, F_ = 1728;

__device__ __forceinline__ float bf2f(unsigned short v) { return __uint_as_float(((unsigned)v) << 16); }
__device__ __forceinline__ unsigned short f2bf(float f) {
  __bf16 b = (__bf16)f;
  return __builtin_bit_cast(unsigned short, b);
}
__device__ __forceinline__ float rbf(float f) { return bf2f(f2bf(f)); }

constexpr int G1_TM = L_ / 128, G1_TN = (F_ + 127) / 128;
constexpr int N_G1 = G1_TM * G1_TN;
constexpr int G1_STAGE = 16384;
constexpr int N_GATE = 32, N_W2 = 90;
constexpr int B_CONV = N_G1, B_GATE = B_CONV + D_, B_W2 = B_GATE + N_GATE, K12_GRID = B_W2 + N_W2;

__device__ __forceinline__ void tr_tiles_load(const unsigned short* __restrict__ src, int ss, int r0, int c0,
                                              u32x4 (&v)[2]) {
  const int tid = threadIdx.x;
#pragma unroll
  for (int i = 0; i < 2; ++i) {
    int id = tid + 256 * i, row = id >> 3, ch = id & 7;
    v[i] = *reinterpret_cast<const u32x4*>(src + (size_t)(r0 + row) * ss + c0 + ch * 8);
  }
}
__device__ __forceinline__ void tr_tiles_put(unsigned short (*tile)[72], const u32x4 (&v)[2]) {
  const int tid = threadIdx.x;
#pragma unroll
  for (int i = 0; i < 2; ++i) {
    int id = tid + 256 * i, row = id >> 3, ch = id & 7;
    *reinterpret_cast<u32x4*>(&tile[row][ch * 8]) = v[i];
  }
}
__device__ __forceinline__ void tr_tiles_store_blocked(unsigned short (*tile)[72], unsigned short* __restrict__ dst,
                                                       int r0, int c0) {
  const int tid = threadIdx.x;
#pragma unroll
  for (int i = 0; i < 2; ++i) {
    int id = tid + 256 * i, col = id >> 3, ch = id & 7;
    unsigned short o[8];
#pragma unroll
    for (int k = 0; k < 8; ++k) o[k] = tile[ch * 8 + k][col];
    u32x4 w;
#pragma unroll
    for (int j = 0; j < 4; ++j) w[j] = o[2 * j] | ((unsigned)o[2 * j + 1] << 16);
    unsigned short* dp = dst + ((size_t)((c0 + col) >> 4) * 54 + ((r0 + ch * 8) >> 5)) * 512 + (col & 15) * 32 + (ch & 3) * 8;
    *reinterpret_cast<u32x4*>(dp) = w;
  }
}

__device__ void gate_role(int gb, const unsigned short* __restrict__ x, const unsigned short* __restrict__ gw,
                          float* __restrict__ gbuf) {
  const int tid = threadIdx.x;
  const int r0 = gb * 64;
  u32x4 v[10][2];
#pragma unroll
  for (int t = 0; t < 10; ++t) tr_tiles_load(x, D_, r0, t * 64, v[t]);
  float s[2] = {0.f, 0.f};
#pragma unroll
  for (int t = 0; t < 10; ++t) {
    u32x4 g = *reinterpret_cast<const u32x4*>(gw + t * 64 + (tid & 7) * 8);
#pragma unroll
    for (int i = 0; i < 2; ++i)
#pragma unroll
      for (int j = 0; j < 4; ++j) {
        s[i] += rbf(__uint_as_float(v[t][i][j] << 16) * __uint_as_float(g[j] << 16));
        s[i] += rbf(__uint_as_float(v[t][i][j] & 0xffff0000u) * __uint_as_float(g[j] & 0xffff0000u));
      }
  }
#pragma unroll
  for (int i = 0; i < 2; ++i) {
    s[i] += __shfl_xor(s[i], 1); s[i] += __shfl_xor(s[i], 2); s[i] += __shfl_xor(s[i], 4);
    if ((tid & 7) == 0) gbuf[r0 + (tid >> 3) + 32 * i] = rbf(1.0f / (1.0f + expf(-rbf(s[i]))));
  }
}

__device__ void w2_role(int wb, const unsigned short* __restrict__ w2, unsigned short* __restrict__ w2T, char* smem) {
  u32x4 tv[3][2];
#pragma unroll
  for (int j = 0; j < 3; ++j) {
    int id = wb * 3 + j;
    tr_tiles_load(w2, D_, (id / 10) * 64, (id % 10) * 64, tv[j]);
  }
  typedef unsigned short tile_t[64][72];
  tile_t* tiles = reinterpret_cast<tile_t*>(smem);
#pragma unroll
  for (int j = 0; j < 3; ++j) tr_tiles_put(tiles[j], tv[j]);
  __syncthreads();
#pragma unroll
  for (int j = 0; j < 3; ++j) {
    int id = wb * 3 + j;
    tr_tiles_store_blocked(tiles[j], w2T, (id / 10) * 64, (id % 10) * 64);
  }
}

__device__ void gemm1_role(int b, const unsigned short* __restrict__ x, const unsigned short* __restrict__ w1,
                           unsigned short* __restrict__ hbuf, char* smem) {
  const int tid = threadIdx.x, lane = tid & 63, wid = tid >> 6;
  int t = (b & 7) * (N_G1 / 8) + (b >> 3);
  const int tm = t / G1_TN, tn = t % G1_TN;
  const int m0 = tm * 128, n0 = tn * 128;
  const int wm = wid >> 1, wn = wid & 1, r = lane & 31, h = lane >> 5;

  const unsigned short* asrc[2];
  const unsigned short* bsrc[2];
#pragma unroll
  for (int i = 0; i < 2; ++i) {
    int row = (i * 4 + wid) * 16 + (lane >> 2);
    int c = (lane & 3) ^ ((row >> 2) & 3);
    asrc[i] = x + (size_t)(m0 + row) * D_ + c * 8;
    int k = (i * 4 + wid) * 4 + (lane >> 4);
    int cb = (lane & 15) ^ ((k & 3) << 1);
    int col = n0 + cb * 8;
    if (col > F_ - 8) col = F_ - 8;
    bsrc[i] = w1 + (size_t)k * F_ + col;
  }
  auto stage = [&](int kt, int buf) {
    char* sa = smem + buf * G1_STAGE;
    char* sb = sa + 8192;
#pragma unroll
    for (int i = 0; i < 2; ++i)
      __builtin_amdgcn_global_load_lds((const unsigned*)(asrc[i] + kt * 32), (LDS_AS unsigned*)(sa + (i * 4 + wid) * 1024), 16, 0, 0);
#pragma unroll
    for (int i = 0; i < 2; ++i)
      __builtin_amdgcn_global_load_lds((const unsigned*)(bsrc[i] + (size_t)kt * 32 * F_), (LDS_AS unsigned*)(sb + (i * 4 + wid) * 1024), 16, 0, 0);
  };

  f32x16 acc[2][2];
#pragma unroll
  for (int a = 0; a < 2; ++a)
#pragma unroll
    for (int c = 0; c < 2; ++c)
#pragma unroll
      for (int i = 0; i < 16; ++i) acc[a][c][i] = 0.f;

  const int q = (lane & 15) >> 2, p = lane & 3, blk = (lane >> 4) & 1;
  auto compute = [&](int buf) {
    const char* sa = smem + buf * G1_STAGE;
    const char* sb = sa + 8192;
#pragma unroll
    for (int s = 0; s < 2; ++s) {
      bf16x8 af[2], bfr[2];
#pragma unroll
      for (int mt = 0; mt < 2; ++mt) {
        int row = wm * 64 + mt * 32 + r;
        af[mt] = *reinterpret_cast<const bf16x8*>(sa + row * 64 + (((2 * s + h) ^ ((row >> 2) & 3)) << 4));
      }
#pragma unroll
      for (int nt = 0; nt < 2; ++nt)
#pragma unroll
        for (int tt = 0; tt < 2; ++tt) {
          int k = 16 * s + 8 * h + 4 * tt + q;
          int chunk = wn * 8 + nt * 4 + 2 * blk + (p >> 1);
          const char* ad = sb + k * 256 + ((chunk ^ (q << 1)) << 4) + (p & 1) * 8;
          bf16x4 v = __builtin_amdgcn_ds_read_tr16_b64_v4i16((LDS_AS bf16x4*)(ad));
          bfr[nt][4 * tt + 0] = v[0]; bfr[nt][4 * tt + 1] = v[1]; bfr[nt][4 * tt + 2] = v[2]; bfr[nt][4 * tt + 3] = v[3];
        }
#pragma unroll
      for (int mt = 0; mt < 2; ++mt)
#pragma unroll
        for (int nt = 0; nt < 2; ++nt)
          acc[mt][nt] = __builtin_amdgcn_mfma_f32_32x32x16_bf16(af[mt], bfr[nt], acc[mt][nt], 0, 0, 0);
    }
  };

  constexpr int NK = D_ / 32;
  stage(0, 0);
  stage(1, 1);
  int cur = 0, nxt = 2;
  for (int kt = 0; kt < NK - 1; ++kt) {
    asm volatile("s_waitcnt vmcnt(4)" ::: "memory");
    RAW_BARRIER();
    if (kt + 2 < NK) stage(kt + 2, nxt);
    compute(cur);
    cur = (cur == 2) ? 0 : cur + 1;
    nxt = (nxt == 2) ? 0 : nxt + 1;
  }
  asm volatile("s_waitcnt vmcnt(0)" ::: "memory");
  RAW_BARRIER();
  compute(cur);

  RAW_BARRIER();
  {
    unsigned short* ht = reinterpret_cast<unsigned short*>(smem);
#pragma unroll
    for (int nt = 0; nt < 2; ++nt)
#pragma unroll
      for (int mt = 0; mt < 2; ++mt)
#pragma unroll
        for (int i = 0; i < 16; ++i) {
          int row = wm * 64 + mt * 32 + (i & 3) + 8 * (i >> 2) + 4 * h;
          float hv = rbf(acc[mt][nt][i]);
          float sv = hv * __builtin_amdgcn_rcpf(1.0f + __expf(-hv));
          ht[row * 136 + wn * 64 + nt * 32 + r] = f2bf(sv);
        }
    __syncthreads();
#pragma unroll
    for (int i = 0; i < 8; ++i) {
      int id = tid + 256 * i, row = id >> 4, ch = id & 15;
      if (n0 + ch * 8 < F_)
        *reinterpret_cast<u32x4*>(hbuf + ((size_t)((m0 + row) >> 4) * 54 + ((n0 + ch * 8) >> 5)) * 512 + (row & 15) * 32 + (ch & 3) * 8) = *reinterpret_cast<const u32x4*>(ht + row * 136 + ch * 8);
    }
  }
}

constexpr int UC_STRIDE = 4384;
constexpr int UC_OFF = 0;
constexpr int KROW = 80;
constexpr int KH_OFF = 8 * UC_STRIDE;
constexpr int KL_OFF = KH_OFF + 64 * KROW + 16;
constexpr int KZ_OFF = KH_OFF + 64 * KROW;
constexpr int PR_OFF = KH_OFF + 12288;
constexpr int OS_OFF = KH_OFF;
constexpr int TB_OFF = PR_OFF + 128 * 16;
constexpr int CONV_LDS = TB_OFF + 768 * 4;

__device__ __forceinline__ float thi_of(int i) {
  int t;
  if (i < 8) t = i;
  else if (i < 16) t = 128 + (i - 8);
  else if (i < 24) t = 256 + 128 * ((i - 16) >> 2) + 2 * ((i - 16) & 3);
  else if (i < 32) t = 512 + 128 * ((i - 24) >> 1) + 4 * ((i - 24) & 1);
  else if (i < 40) t = 1024 + 128 * (i - 32);
  else t = 2048;
  return (float)t;
}

struct KTab { unsigned short v[L_]; };
constexpr KTab make_ktab() {
  KTab t{};
  for (int j = 0; j < L_; ++j) {
    int sh = (j < 256) ? 0 : (j < 512) ? 1 : (j < 1024) ? 2 : 3;
    int rr = j;
    if (sh) {
      int q = j >> sh, rem = j & ((1 << sh) - 1), half = 1 << (sh - 1);
      if (rem > half || (rem == half && (q & 1))) ++q;
      rr = q << sh;
    }
    int i = 40;
    if (rr < 256) i = (rr & 7) + 8 * (rr >> 7);
    else if (rr < 512) i = 16 + 4 * ((rr - 256) >> 7) + ((rr & 7) >> 1);
    else if (rr < 1024) i = 24 + 2 * ((rr - 512) >> 7) + ((rr & 7) >> 2);
    else if (rr < 2048) i = 32 + ((rr - 1024) >> 7);
    t.v[j] = (unsigned short)(i * 16 + ((rr >> 3) & 15));
  }
  return t;
}
__device__ __attribute__((aligned(16))) const KTab KTAB = make_ktab();

__device__ void conv_role(int d, const unsigned short* __restrict__ x, const unsigned short* __restrict__ logAr,
                          const unsigned short* __restrict__ Aim, const unsigned short* __restrict__ Cre,
                          const unsigned short* __restrict__ Cim, const unsigned short* __restrict__ logdt,
                          const unsigned short* __restrict__ Dsk, unsigned short* __restrict__ aT, char* smem) {
  const int tid = threadIdx.x, lane = tid & 63, wid = tid >> 6;

  const int pn = tid & (NS_ - 1);
  const unsigned short r_ldt = logdt[d];
  const unsigned short r_lar = logAr[d * NS_ + pn], r_aim = Aim[d * NS_ + pn];
  const unsigned short r_cre = Cre[d * NS_ + pn], r_cim = Cim[d * NS_ + pn];
  const u32x4 kidx = *reinterpret_cast<const u32x4*>(KTAB.v + 8 * tid);
  __builtin_amdgcn_sched_barrier(0);
  unsigned short ug[8];
#pragma unroll
  for (int k = 0; k < 8; ++k) ug[k] = x[(size_t)(8 * tid + k) * D_ + d];
  __builtin_amdgcn_sched_barrier(0);
  asm volatile("" :: "v"(r_ldt), "v"(r_lar), "v"(r_aim), "v"(r_cre), "v"(r_cim));

  const float dt = rbf(expf(bf2f(r_ldt)));
  if (tid < NS_) {
    float are = -rbf(expf(bf2f(r_lar)));
    float aim = bf2f(r_aim);
    float lr = are * dt, li = aim * dt;
    float cr = bf2f(r_cre) * dt, ci = bf2f(r_cim) * dt;
    float mag = sqrtf(cr * cr + ci * ci);
    f32x4 pp;
    pp[0] = lr * 1.4426950408889634f;
    pp[1] = li * 0.15915494309189535f;
    pp[2] = log2f(mag);
    pp[3] = atan2f(ci, cr) * 0.15915494309189535f;
    *reinterpret_cast<f32x4*>(smem + PR_OFF + tid * 16) = pp;
  }
  if (tid < 64) {
    u32x4 z = {0, 0, 0, 0};
    *reinterpret_cast<u32x4*>(smem + UC_OFF + (tid >> 3) * UC_STRIDE + (tid & 7) * 16) = z;
  }
  RAW_BARRIER();

  {
    const int x = lane & 15, kk = lane >> 4;
    float th[3];
#pragma unroll
    for (int T = 0; T < 3; ++T) th[T] = thi_of(16 * T + x);
    const float tlo = (float)(8 * x);
    f32x4 acc[3];
#pragma unroll
    for (int T = 0; T < 3; ++T) acc[T] = (f32x4){0.f, 0.f, 0.f, 0.f};
#pragma unroll 2
    for (int g = 0; g < 8; ++g) {
      int n = 32 * wid + 4 * g + kk;
      f32x4 pp = *reinterpret_cast<const f32x4*>(smem + PR_OFF + n * 16);
      float qe = __builtin_amdgcn_exp2f(pp[0] * tlo);
      float qa = pp[1] * tlo;
      float qr = qe * __builtin_amdgcn_cosf(qa), qi = qe * __builtin_amdgcn_sinf(qa);
#pragma unroll
      for (int T = 0; T < 3; ++T) {
        float pe = __builtin_amdgcn_exp2f(pp[0] * th[T] + pp[2]);
        float pa = pp[1] * th[T] + pp[3];
        float pr = pe * __builtin_amdgcn_cosf(pa), pi = pe * __builtin_amdgcn_sinf(pa);
        acc[T] = __builtin_amdgcn_mfma_f32_16x16x4f32(qr, pr, acc[T], 0, 0, 0);
        acc[T] = __builtin_amdgcn_mfma_f32_16x16x4f32(-qi, pi, acc[T], 0, 0, 0);
      }
    }
    float* os = reinterpret_cast<float*>(smem + OS_OFF) + wid * 768;
#pragma unroll
    for (int T = 0; T < 3; ++T) *reinterpret_cast<f32x4*>(os + (16 * T + x) * 16 + 4 * kk) = acc[T];
  }
  RAW_BARRIER();

  u32x4 uvec;
#pragma unroll
  for (int j = 0; j < 4; ++j) uvec[j] = (unsigned)ug[2 * j] | ((unsigned)ug[2 * j + 1] << 16);
  *reinterpret_cast<u32x4*>(smem + UC_OFF + 128 + tid * 16) = uvec;
  {
    unsigned* tab = reinterpret_cast<unsigned*>(smem + TB_OFF);
#pragma unroll
    for (int k = 0; k < 3; ++k) {
      int e = tid + 256 * k;
      const float* osp = reinterpret_cast<const float*>(smem + OS_OFF) + e;
      float kv = 2.0f * ((osp[0] + osp[768]) + (osp[1536] + osp[2304]));
      unsigned short hi = f2bf(kv);
      tab[e] = (unsigned)hi | ((unsigned)f2bf(kv - bf2f(hi)) << 16);
    }
  }
  __syncthreads();
  {
    u32x4 v1 = {0, 0, 0, 0};
    if (tid < 255) v1 = *reinterpret_cast<const u32x4*>(smem + UC_OFF + 128 + (tid + 1) * 16);
    unsigned w[8] = {uvec[0], uvec[1], uvec[2], uvec[3], v1[0], v1[1], v1[2], v1[3]};
#pragma unroll
    for (int e = 1; e < 8; ++e) {
      u32x4 o;
#pragma unroll
      for (int j = 0; j < 4; ++j) {
        int base = j + (e >> 1);
        if (e & 1) o[j] = (w[base] >> 16) | (w[base + 1] << 16);
        else o[j] = w[base];
      }
      *reinterpret_cast<u32x4*>(smem + UC_OFF + e * UC_STRIDE + 128 + tid * 16) = o;
    }
    if (tid == 0) {
#pragma unroll
      for (int e = 1; e < 8; ++e)
#pragma unroll
        for (int k = 0; k < e; ++k)
          *reinterpret_cast<unsigned short*>(smem + UC_OFF + e * UC_STRIDE + 2 * (64 - e + k)) =
              (unsigned short)(w[k >> 1] >> (16 * (k & 1)));
    }
  }
  {
    const unsigned* tab = reinterpret_cast<const unsigned*>(smem + TB_OFF);
    unsigned w[8];
#pragma unroll
    for (int k = 0; k < 8; ++k) w[k] = tab[(kidx[k >> 1] >> (16 * (k & 1))) & 0xffff];
    u32x4 vh, vl;
#pragma unroll
    for (int j = 0; j < 4; ++j) {
      vh[j] = (w[7 - 2 * j] & 0xffffu) | (w[6 - 2 * j] << 16);
      vl[j] = (w[7 - 2 * j] >> 16) | (w[6 - 2 * j] & 0xffff0000u);
    }
    *reinterpret_cast<u32x4*>(smem + KH_OFF + (tid >> 2) * KROW + (tid & 3) * 16) = vh;
    *reinterpret_cast<u32x4*>(smem + KL_OFF + (tid >> 2) * KROW + (tid & 3) * 16) = vl;
    if (tid == 0) { u32x4 z = {0, 0, 0, 0}; *reinterpret_cast<u32x4*>(smem + KZ_OFF) = z; }
  }
  __syncthreads();

  f32x16 y0, y1;
#pragma unroll
  for (int i = 0; i < 16; ++i) { y0[i] = 0.f; y1[i] = 0.f; }
  {
    const int r = lane & 31, h = lane >> 5;
    const int e = (r + 1) & 7;
    const char* ubase = smem + UC_OFF + e * UC_STRIDE + 2 * (r - 7 - e + 64 - 8 * h);
#pragma unroll 2
    for (int dl = wid; dl < 32; dl += 4) {
      bf16x8 a0 = *reinterpret_cast<const bf16x8*>(ubase + 64 * dl);
      bf16x8 a1 = *reinterpret_cast<const bf16x8*>(ubase + 64 * dl - 32);
      bf16x8 a2 = *reinterpret_cast<const bf16x8*>(ubase + 64 * dl + 2048);
      bf16x8 a3 = *reinterpret_cast<const bf16x8*>(ubase + 64 * dl + 2048 - 32);
      const int m = r - dl;
      const int off = (m >= 0) ? (m * KROW + 16 * h) : (KZ_OFF - KH_OFF);
      const int off1 = (m >= 0) ? (off + 32) : off;
      const int lo = (m >= 0) ? KL_OFF : KH_OFF;
      bf16x8 fh0 = *reinterpret_cast<const bf16x8*>(smem + KH_OFF + off);
      bf16x8 fh1 = *reinterpret_cast<const bf16x8*>(smem + KH_OFF + off1);
      bf16x8 fl0 = *reinterpret_cast<const bf16x8*>(smem + lo + off);
      bf16x8 fl1 = *reinterpret_cast<const bf16x8*>(smem + lo + off1);
      const int go = (32 + m) * KROW + 16 * h;
      bf16x8 gh0 = *reinterpret_cast<const bf16x8*>(smem + KH_OFF + go);
      bf16x8 gh1 = *reinterpret_cast<const bf16x8*>(smem + KH_OFF + go + 32);
      bf16x8 gl0 = *reinterpret_cast<const bf16x8*>(smem + KL_OFF + go);
      bf16x8 gl1 = *reinterpret_cast<const bf16x8*>(smem + KL_OFF + go + 32);
      y0 = __builtin_amdgcn_mfma_f32_32x32x16_bf16(a0, fh0, y0, 0, 0, 0);
      y1 = __builtin_amdgcn_mfma_f32_32x32x16_bf16(a2, fh0, y1, 0, 0, 0);
      y0 = __builtin_amdgcn_mfma_f32_32x32x16_bf16(a1, fh1, y0, 0, 0, 0);
      y1 = __builtin_amdgcn_mfma_f32_32x32x16_bf16(a3, fh1, y1, 0, 0, 0);
      y0 = __builtin_amdgcn_mfma_f32_32x32x16_bf16(a0, fl0, y0, 0, 0, 0);
      y1 = __builtin_amdgcn_mfma_f32_32x32x16_bf16(a2, fl0, y1, 0, 0, 0);
      y0 = __builtin_amdgcn_mfma_f32_32x32x16_bf16(a1, fl1, y0, 0, 0, 0);
      y1 = __builtin_amdgcn_mfma_f32_32x32x16_bf16(a3, fl1, y1, 0, 0, 0);
      y1 = __builtin_amdgcn_mfma_f32_32x32x16_bf16(a0, gh0, y1, 0, 0, 0);
      y1 = __builtin_amdgcn_mfma_f32_32x32x16_bf16(a1, gh1, y1, 0, 0, 0);
      y1 = __builtin_amdgcn_mfma_f32_32x32x16_bf16(a0, gl0, y1, 0, 0, 0);
      y1 = __builtin_amdgcn_mfma_f32_32x32x16_bf16(a1, gl1, y1, 0, 0, 0);
    }
  }
  __syncthreads();
  {
    const int r = lane & 31, h = lane >> 5;
    float* slab = reinterpret_cast<float*>(smem + wid * 8704);
#pragma unroll
    for (int g = 0; g < 4; ++g) {
      f32x4 v0 = {y0[4 * g], y0[4 * g + 1], y0[4 * g + 2], y0[4 * g + 3]};
      f32x4 v1 = {y1[4 * g], y1[4 * g + 1], y1[4 * g + 2], y1[4 * g + 3]};
      int b = 8 * g + 4 * h;
      *reinterpret_cast<f32x4*>(slab + 32 * r + b) = v0;
      *reinterpret_cast<f32x4*>(slab + 32 * (32 + r) + b) = v1;
    }
  }
  __syncthreads();
  {
    const float dsk = bf2f(Dsk[d]);
    const float* s = reinterpret_cast<const float*>(smem);
    f32x4 ys[2];
#pragma unroll
    for (int hh = 0; hh < 2; ++hh) {
      f32x4 s0 = *reinterpret_cast<const f32x4*>(s + 8 * tid + 4 * hh);
      f32x4 s1 = *reinterpret_cast<const f32x4*>(s + 2176 + 8 * tid + 4 * hh);
      f32x4 s2 = *reinterpret_cast<const f32x4*>(s + 4352 + 8 * tid + 4 * hh);
      f32x4 s3 = *reinterpret_cast<const f32x4*>(s + 6528 + 8 * tid + 4 * hh);
      ys[hh] = (s0 + s1) + (s2 + s3);
    }
    unsigned short o[8];
#pragma unroll
    for (int k = 0; k < 8; ++k) {
      float yb = rbf(ys[k >> 2][k & 3]);
      float uu = bf2f((unsigned short)(uvec[k >> 1] >> (16 * (k & 1))));
      float sk = rbf(uu * dsk);
      o[k] = f2bf(yb + sk);
    }
    u32x4 v;
#pragma unroll
    for (int j = 0; j < 4; ++j) v[j] = o[2 * j] | ((unsigned)o[2 * j + 1] << 16);
    *reinterpret_cast<u32x4*>(aT + (size_t)d * L_ + 8 * tid) = v;
  }
}

__global__ __launch_bounds__(256, 3) void k12(const unsigned short* __restrict__ x, const unsigned short* __restrict__ w1,
                                              const unsigned short* __restrict__ w2, const unsigned short* __restrict__ gw,
                                              const unsigned short* __restrict__ logAr, const unsigned short* __restrict__ Aim,
                                              const unsigned short* __restrict__ Cre, const unsigned short* __restrict__ Cim,
                                              const unsigned short* __restrict__ logdt, const unsigned short* __restrict__ Dsk,
                                              unsigned short* __restrict__ hbuf, unsigned short* __restrict__ w2T,
                                              float* __restrict__ gbuf, unsigned short* __restrict__ aT) {
  extern __shared__ __attribute__((aligned(16))) char smem[];
  const int b = blockIdx.x;
  if (b < B_CONV) gemm1_role(b, x, w1, hbuf, smem);
  else if (b < B_GATE) conv_role(b - B_CONV, x, logAr, Aim, Cre, Cim, logdt, Dsk, aT, smem);
  else if (b < B_W2) gate_role(b - B_GATE, x, gw, gbuf);
  else w2_role(b - B_W2, w2, w2T, smem);
}

constexpr int G2_RS = 84;
__global__ __launch_bounds__(256, 1) void k3_gemm2(const unsigned short* __restrict__ hbuf, const unsigned short* __restrict__ w2T,
                                                   const unsigned short* __restrict__ aT, const float* __restrict__ gbuf,
                                                   unsigned short* __restrict__ out) {
  __shared__ float red[4][64][G2_RS];
  const int tid = threadIdx.x, lane = tid & 63, wid = tid >> 6;
  int t = (blockIdx.x & 7) * 32 + (blockIdx.x >> 3);
  const int tm = t >> 3, tn = t & 7;
  const int m0 = tm * 64, n0 = tn * 80;
  const int fr = lane & 15, fq = lane >> 4;
  const int wv = __builtin_amdgcn_readfirstlane(wid);
  const int c0 = (wv * 27) >> 2;
  const int nch = (((wv + 1) * 27) >> 2) - c0;

  const unsigned short* ap = hbuf + (size_t)(m0 >> 4) * 54 * 512 + fr * 32 + 8 * fq + c0 * 1024;
  const unsigned short* bp = w2T + (size_t)(n0 >> 4) * 54 * 512 + fr * 32 + 8 * fq + c0 * 1024;

  f32x4 acc[4][5];
#pragma unroll
  for (int a = 0; a < 4; ++a)
#pragma unroll
    for (int c = 0; c < 5; ++c) acc[a][c] = (f32x4){0.f, 0.f, 0.f, 0.f};

  constexpr int PD = 2;
  bf16x8 A[PD][4][2], B[PD][5][2];
#define G2_LOAD(S, c)                                                                                  \
  {                                                                                                    \
    _Pragma("unroll") for (int a = 0; a < 4; ++a) {                                                    \
      A[S][a][0] = *reinterpret_cast<const bf16x8*>(ap + a * 54 * 512 + (c) * 1024);                   \
      A[S][a][1] = *reinterpret_cast<const bf16x8*>(ap + a * 54 * 512 + (c) * 1024 + 512);             \
    }                                                                                                  \
    _Pragma("unroll") for (int a = 0; a < 5; ++a) {                                                    \
      B[S][a][0] = *reinterpret_cast<const bf16x8*>(bp + a * 54 * 512 + (c) * 1024);                   \
      B[S][a][1] = *reinterpret_cast<const bf16x8*>(bp + a * 54 * 512 + (c) * 1024 + 512);             \
    }                                                                                                  \
  }
#define G2_COMP(S)                                                                                     \
  {                                                                                                    \
    _Pragma("unroll") for (int s = 0; s < 2; ++s)                                                      \
      _Pragma("unroll") for (int a = 0; a < 4; ++a)                                                    \
        _Pragma("unroll") for (int c = 0; c < 5; ++c)                                                  \
          acc[a][c] = __builtin_amdgcn_mfma_f32_16x16x32_bf16(A[S][a][s], B[S][c][s], acc[a][c], 0, 0, 0); \
  }
#pragma unroll
  for (int i = 0; i < PD - 1; ++i) {
    G2_LOAD(i, i);
    __builtin_amdgcn_sched_barrier(0);
  }
  const int row0 = m0 + 16 * wv;
  u32x4 av[2][2];
  f32x4 gv[4];
#pragma unroll
  for (int j = 0; j < 2; ++j) {
    const int col = (j == 0) ? lane : (64 + (lane & 15));
    av[j][0] = *reinterpret_cast<const u32x4*>(aT + (size_t)(n0 + col) * L_ + row0);
    av[j][1] = *reinterpret_cast<const u32x4*>(aT + (size_t)(n0 + col) * L_ + row0 + 8);
  }
#pragma unroll
  for (int i = 0; i < 4; ++i) gv[i] = *reinterpret_cast<const f32x4*>(gbuf + row0 + 4 * i);
  __builtin_amdgcn_sched_barrier(0);
#pragma unroll
  for (int i = 0; i < 7; ++i) {
    if (i + PD - 1 < 7) {
      const int cn = (i + PD - 1 < 6) ? (i + PD - 1) : (nch - 1);
      G2_LOAD((i + PD - 1) % PD, cn);
    }
    __builtin_amdgcn_sched_barrier(0);
    if (i == 6 && nch != 7) {
      const bf16x8 z = {0, 0, 0, 0, 0, 0, 0, 0};
#pragma unroll
      for (int a = 0; a < 4; ++a) { A[i % PD][a][0] = z; A[i % PD][a][1] = z; }
#pragma unroll
      for (int a = 0; a < 5; ++a) { B[i % PD][a][0] = z; B[i % PD][a][1] = z; }
    }
    G2_COMP(i % PD);
    __builtin_amdgcn_sched_barrier(0);
  }
#undef G2_LOAD
#undef G2_COMP

#pragma unroll
  for (int a = 0; a < 4; ++a)
#pragma unroll
    for (int c = 0; c < 5; ++c)
#pragma unroll
      for (int i = 0; i < 4; ++i) red[wid][16 * a + 4 * fq + i][16 * c + fr] = acc[a][c][i];
  __syncthreads();

#pragma unroll
  for (int j = 0; j < 2; ++j) {
    const int col = lane + 64 * j;
    if (col < 80) {
      const u32x4 av0 = av[j][0], av1 = av[j][1];
#pragma unroll
      for (int i = 0; i < 16; ++i) {
        int rr = 16 * wid + i;
        float bv = rbf((red[0][rr][col] + red[1][rr][col]) + (red[2][rr][col] + red[3][rr][col]));
        unsigned aw = (i < 8) ? av0[(i >> 1) & 3] : av1[(i >> 1) & 3];
        float a = bf2f((unsigned short)((aw >> (16 * (i & 1))) & 0xffff));
        float g = gv[i >> 2][i & 3];
        float t1 = rbf(g * a);
        float t2 = rbf(1.0f - g);
        float t3 = rbf(t2 * bv);
        out[(size_t)(row0 + i) * D_ + n0 + col] = f2bf(t1 + t3);
      }
    }
  }
}

extern "C" void kernel_launch(void* const* d_in, const int* in_sizes, int n_in, void* d_out, int out_size, void* d_ws,
                              size_t ws_size, hipStream_t stream) {
  const unsigned short* u = (const unsigned short*)d_in[0];
  const unsigned short* logAr = (const unsigned short*)d_in[1];
  const unsigned short* Aim = (const unsigned short*)d_in[2];
  const unsigned short* Cre = (const unsigned short*)d_in[3];
  const unsigned short* Cim = (const unsigned short*)d_in[4];
  const unsigned short* logdt = (const unsigned short*)d_in[5];
  const unsigned short* Dsk = (const unsigned short*)d_in[6];
  const unsigned short* w1 = (const unsigned short*)d_in[8];
  const unsigned short* w2 = (const unsigned short*)d_in[9];
  const unsigned short* gw = (const unsigned short*)d_in[10];
  unsigned short* out = (unsigned short*)d_out;

  char* ws = (char*)d_ws;
  unsigned short* hbuf = (unsigned short*)ws;
  unsigned short* aT = (unsigned short*)(ws + (size_t)L_ * F_ * 2);
  unsigned short* w2T = (unsigned short*)(ws + (size_t)L_ * F_ * 2 + (size_t)D_ * L_ * 2);
  float* gbuf = (float*)(ws + (size_t)L_ * F_ * 2 + (size_t)D_ * L_ * 2 + (size_t)D_ * F_ * 2);

  constexpr int K12_LDS = (CONV_LDS > 3 * G1_STAGE) ? CONV_LDS : 3 * G1_STAGE;
  hipLaunchKernelGGL(k12, dim3(K12_GRID), dim3(256), K12_LDS, stream, u, w1, w2, gw, logAr, Aim, Cre, Cim, logdt, Dsk,
                     hbuf, w2T, gbuf, aT);
  hipLaunchKernelGGL(k3_gemm2, dim3(256), dim3(256), 0, stream, hbuf, w2T, aT, gbuf, out);
}
```

```cpp
#include <hip/hip_runtime.h>
#include <hip/hip_bf16.h>
#include <stdint.h>

typedef __attribute__((ext_vector_type(8))) short bf16x8;
typedef __attribute__((ext_vector_type(4))) short bf16x4;
typedef __attribute__((ext_vector_type(16))) float f32x16;
typedef __attribute__((ext_vector_type(4))) float f32x4;
typedef __attribute__((ext_vector_type(4))) unsigned int u32x4;
typedef __attribute__((ext_vector_type(2))) unsigned int u32x2;

#define LDS_AS __attribute__((address_space(3)))
#define RAW_BARRIER() do { asm volatile("s_waitcnt lgkmcnt(0)" ::: "memory"); __builtin_amdgcn_s_barrier(); } while (0)

constexpr int L_ = 2048, D_ = 640, NS_ = 128, F_ = 1728;

__device__ __forceinline__ float bf2f(unsigned short v) { return __uint_as_float(((unsigned)v) << 16); }
__device__ __forceinline__ unsigned short f2bf(float f) {
  __bf16 b = (__bf16)f;
  return __builtin_bit_cast(unsigned short, b);
}
__device__ __forceinline__ float rbf(float f) { return bf2f(f2bf(f)); }

constexpr int G1_TM = L_ / 128, G1_TN = (F_ + 127) / 128;
constexpr int N_G1 = G1_TM * G1_TN;
constexpr int G1_STAGE = 16384;
constexpr int N_GATE = 32, N_W2 = 90;
constexpr int B_CONV = N_G1, B_GATE = B_CONV + D_, B_W2 = B_GATE + N_GATE, K12_GRID = B_W2 + N_W2;

__device__ __forceinline__ void tr_tiles_load(const unsigned short* __restrict__ src, int ss, int r0, int c0,
                                              u32x4 (&v)[2]) {
  const int tid = threadIdx.x;
#pragma unroll
  for (int i = 0; i < 2; ++i) {
    int id = tid + 256 * i, row = id >> 3, ch = id & 7;
    v[i] = *reinterpret_cast<const u32x4*>(src + (size_t)(r0 + row) * ss + c0 + ch * 8);
  }
}
__device__ __forceinline__ void tr_tiles_put(unsigned short (*tile)[72], const u32x4 (&v)[2]) {
  const int tid = threadIdx.x;
#pragma unroll
  for (int i = 0; i < 2; ++i) {
    int id = tid + 256 * i, row = id >> 3, ch = id & 7;
    *reinterpret_cast<u32x4*>(&tile[row][ch * 8]) = v[i];
  }
}
__device__ __forceinline__ void tr_tiles_store_blocked(unsigned short (*tile)[72], unsigned short* __restrict__ dst,
                                                       int r0, int c0) {
  const int tid = threadIdx.x;
#pragma unroll
  for (int i = 0; i < 2; ++i) {
    int id = tid + 256 * i, col = id >> 3, ch = id & 7;
    unsigned short o[8];
#pragma unroll
    for (int k = 0; k < 8; ++k) o[k] = tile[ch * 8 + k][col];
    u32x4 w;
#pragma unroll
    for (int j = 0; j < 4; ++j) w[j] = o[2 * j] | ((unsigned)o[2 * j + 1] << 16);
    unsigned short* dp = dst + ((size_t)((c0 + col) >> 4) * 54 + ((r0 + ch * 8) >> 5)) * 512 + (col & 15) * 32 + (ch & 3) * 8;
    *reinterpret_cast<u32x4*>(dp) = w;
  }
}

__device__ void gate_role(int gb, const unsigned short* __restrict__ x, const unsigned short* __restrict__ gw,
                          float* __restrict__ gbuf) {
  const int tid = threadIdx.x;
  const int r0 = gb * 64;
  u32x4 v[10][2];
#pragma unroll
  for (int t = 0; t < 10; ++t) tr_tiles_load(x, D_, r0, t * 64, v[t]);
  float s[2] = {0.f, 0.f};
#pragma unroll
  for (int t = 0; t < 10; ++t) {
    u32x4 g = *reinterpret_cast<const u32x4*>(gw + t * 64 + (tid & 7) * 8);
#pragma unroll
    for (int i = 0; i < 2; ++i)
#pragma unroll
      for (int j = 0; j < 4; ++j) {
        s[i] += rbf(__uint_as_float(v[t][i][j] << 16) * __uint_as_float(g[j] << 16));
        s[i] += rbf(__uint_as_float(v[t][i][j] & 0xffff0000u) * __uint_as_float(g[j] & 0xffff0000u));
      }
  }
#pragma unroll
  for (int i = 0; i < 2; ++i) {
    s[i] += __shfl_xor(s[i], 1); s[i] += __shfl_xor(s[i], 2); s[i] += __shfl_xor(s[i], 4);
    if ((tid & 7) == 0) gbuf[r0 + (tid >> 3) + 32 * i] = rbf(1.0f / (1.0f + expf(-rbf(s[i]))));
  }
}

__device__ void w2_role(int wb, const unsigned short* __restrict__ w2, unsigned short* __restrict__ w2T, char* smem) {
  u32x4 tv[3][2];
#pragma unroll
  for (int j = 0; j < 3; ++j) {
    int id = wb * 3 + j;
    tr_tiles_load(w2, D_, (id / 10) * 64, (id % 10) * 64, tv[j]);
  }
  typedef unsigned short tile_t[64][72];
  tile_t* tiles = reinterpret_cast<tile_t*>(smem);
#pragma unroll
  for (int j = 0; j < 3; ++j) tr_tiles_put(tiles[j], tv[j]);
  __syncthreads();
#pragma unroll
  for (int j = 0; j < 3; ++j) {
    int id = wb * 3 + j;
    tr_tiles_store_blocked(tiles[j], w2T, (id / 10) * 64, (id % 10) * 64);
  }
}

__device__ void gemm1_role(int b, const unsigned short* __restrict__ x, const unsigned short* __restrict__ w1,
                           unsigned short* __restrict__ hbuf, char* smem) {
  const int tid = threadIdx.x, lane = tid & 63, wid = tid >> 6;
  int t = (b & 7) * (N_G1 / 8) + (b >> 3);
  const int tm = t / G1_TN, tn = t % G1_TN;
  const int m0 = tm * 128, n0 = tn * 128;
  const int wm = wid >> 1, wn = wid & 1, r = lane & 31, h = lane >> 5;

  const unsigned short* asrc[2];
  const unsigned short* bsrc[2];
#pragma unroll
  for (int i = 0; i < 2; ++i) {
    int row = (i * 4 + wid) * 16 + (lane >> 2);
    int c = (lane & 3) ^ ((row >> 2) & 3);
    asrc[i] = x + (size_t)(m0 + row) * D_ + c * 8;
    int k = (i * 4 + wid) * 4 + (lane >> 4);
    int cb = (lane & 15) ^ ((k & 3) << 1);
    int col = n0 + cb * 8;
    if (col > F_ - 8) col = F_ - 8;
    bsrc[i] = w1 + (size_t)k * F_ + col;
  }
  auto stage = [&](int kt, int buf) {
    char* sa = smem + buf * G1_STAGE;
    char* sb = sa + 8192;
#pragma unroll
    for (int i = 0; i < 2; ++i)
      __builtin_amdgcn_global_load_lds((const unsigned*)(asrc[i] + kt * 32), (LDS_AS unsigned*)(sa + (i * 4 + wid) * 1024), 16, 0, 0);
#pragma unroll
    for (int i = 0; i < 2; ++i)
      __builtin_amdgcn_global_load_lds((const unsigned*)(bsrc[i] + (size_t)kt * 32 * F_), (LDS_AS unsigned*)(sb + (i * 4 + wid) * 1024), 16, 0, 0);
  };

  f32x16 acc[2][2];
#pragma unroll
  for (int a = 0; a < 2; ++a)
#pragma unroll
    for (int c = 0; c < 2; ++c)
#pragma unroll
      for (int i = 0; i < 16; ++i) acc[a][c][i] = 0.f;

  const int q = (lane & 15) >> 2, p = lane & 3, blk = (lane >> 4) & 1;
  auto compute = [&](int buf) {
    const char* sa = smem + buf * G1_STAGE;
    const char* sb = sa + 8192;
#pragma unroll
    for (int s = 0; s < 2; ++s) {
      bf16x8 af[2], bfr[2];
#pragma unroll
      for (int mt = 0; mt < 2; ++mt) {
        int row = wm * 64 + mt * 32 + r;
        af[mt] = *reinterpret_cast<const bf16x8*>(sa + row * 64 + (((2 * s + h) ^ ((row >> 2) & 3)) << 4));
      }
#pragma unroll
      for (int nt = 0; nt < 2; ++nt)
#pragma unroll
        for (int tt = 0; tt < 2; ++tt) {
          int k = 16 * s + 8 * h + 4 * tt + q;
          int chunk = wn * 8 + nt * 4 + 2 * blk + (p >> 1);
          const char* ad = sb + k * 256 + ((chunk ^ (q << 1)) << 4) + (p & 1) * 8;
          bf16x4 v = __builtin_amdgcn_ds_read_tr16_b64_v4i16((LDS_AS bf16x4*)(ad));
          bfr[nt][4 * tt + 0] = v[0]; bfr[nt][4 * tt + 1] = v[1]; bfr[nt][4 * tt + 2] = v[2]; bfr[nt][4 * tt + 3] = v[3];
        }
#pragma unroll
      for (int mt = 0; mt < 2; ++mt)
#pragma unroll
        for (int nt = 0; nt < 2; ++nt)
          acc[mt][nt] = __builtin_amdgcn_mfma_f32_32x32x16_bf16(af[mt], bfr[nt], acc[mt][nt], 0, 0, 0);
    }
  };

  constexpr int NK = D_ / 32;
  stage(0, 0);
  stage(1, 1);
  int cur = 0, nxt = 2;
  for (int kt = 0; kt < NK - 1; ++kt) {
    asm volatile("s_waitcnt vmcnt(4)" ::: "memory");
    RAW_BARRIER();
    if (kt + 2 < NK) stage(kt + 2, nxt);
    compute(cur);
    cur = (cur == 2) ? 0 : cur + 1;
    nxt = (nxt == 2) ? 0 : nxt + 1;
  }
  asm volatile("s_waitcnt vmcnt(0)" ::: "memory");
  RAW_BARRIER();
  compute(cur);

  RAW_BARRIER();
  {
    unsigned short* ht = reinterpret_cast<unsigned short*>(smem);
#pragma unroll
    for (int nt = 0; nt < 2; ++nt)
#pragma unroll
      for (int mt = 0; mt < 2; ++mt)
#pragma unroll
        for (int i = 0; i < 16; ++i) {
          int row = wm * 64 + mt * 32 + (i & 3) + 8 * (i >> 2) + 4 * h;
          float hv = rbf(acc[mt][nt][i]);
          float sv = hv * __builtin_amdgcn_rcpf(1.0f + __expf(-hv));
          ht[row * 136 + wn * 64 + nt * 32 + r] = f2bf(sv);
        }
    __syncthreads();
#pragma unroll
    for (int i = 0; i < 8; ++i) {
      int id = tid + 256 * i, row = id >> 4, ch = id & 15;
      if (n0 + ch * 8 < F_)
        *reinterpret_cast<u32x4*>(hbuf + ((size_t)((m0 + row) >> 4) * 54 + ((n0 + ch * 8) >> 5)) * 512 + (row & 15) * 32 + (ch & 3) * 8) = *reinterpret_cast<const u32x4*>(ht + row * 136 + ch * 8);
    }
  }
}

constexpr int UC_STRIDE = 4384;
constexpr int UC_OFF = 0;
constexpr int KROW = 80;
constexpr int KH_OFF = 8 * UC_STRIDE;
constexpr int KL_OFF = KH_OFF + 64 * KROW + 16;
constexpr int KZ_OFF = KH_OFF + 64 * KROW;
constexpr int PR_OFF = KH_OFF + 12288;
constexpr int OS_OFF = KH_OFF;
constexpr int TB_OFF = PR_OFF + 128 * 16;
constexpr int CONV_LDS = TB_OFF + 768 * 4;

__device__ __forceinline__ float thi_of(int i) {
  int t;
  if (i < 8) t = i;
  else if (i < 16) t = 128 + (i - 8);
  else if (i < 24) t = 256 + 128 * ((i - 16) >> 2) + 2 * ((i - 16) & 3);
  else if (i < 32) t = 512 + 128 * ((i - 24) >> 1) + 4 * ((i - 24) & 1);
  else if (i < 40) t = 1024 + 128 * (i - 32);
  else t = 2048;
  return (float)t;
}

struct KTab { unsigned short v[L_]; };
constexpr KTab make_ktab() {
  KTab t{};
  for (int j = 0; j < L_; ++j) {
    int sh = (j < 256) ? 0 : (j < 512) ? 1 : (j < 1024) ? 2 : 3;
    int rr = j;
    if (sh) {
      int q = j >> sh, rem = j & ((1 << sh) - 1), half = 1 << (sh - 1);
      if (rem > half || (rem == half && (q & 1))) ++q;
      rr = q << sh;
    }
    int i = 40;
    if (rr < 256) i = (rr & 7) + 8 * (rr >> 7);
    else if (rr < 512) i = 16 + 4 * ((rr - 256) >> 7) + ((rr & 7) >> 1);
    else if (rr < 1024) i = 24 + 2 * ((rr - 512) >> 7) + ((rr & 7) >> 2);
    else if (rr < 2048) i = 32 + ((rr - 1024) >> 7);
    t.v[j] = (unsigned short)(i * 16 + ((rr >> 3) & 15));
  }
  return t;
}
__device__ __attribute__((aligned(16))) const KTab KTAB = make_ktab();

__device__ void conv_role(int d, const unsigned short* __restrict__ x, const unsigned short* __restrict__ logAr,
                          const unsigned short* __restrict__ Aim, const unsigned short* __restrict__ Cre,
                          const unsigned short* __restrict__ Cim, const unsigned short* __restrict__ logdt,
                          const unsigned short* __restrict__ Dsk, unsigned short* __restrict__ aT, char* smem) {
  const int tid = threadIdx.x, lane = tid & 63, wid = tid >> 6;

  const int pn = tid & (NS_ - 1);
  const unsigned short r_ldt = logdt[d];
  const unsigned short r_lar = logAr[d * NS_ + pn], r_aim = Aim[d * NS_ + pn];
  const unsigned short r_cre = Cre[d * NS_ + pn], r_cim = Cim[d * NS_ + pn];
  const u32x4 kidx = *reinterpret_cast<const u32x4*>(KTAB.v + 8 * tid);
  __builtin_amdgcn_sched_barrier(0);
  unsigned short ug[8];
#pragma unroll
  for (int k = 0; k < 8; ++k) ug[k] = x[(size_t)(8 * tid + k) * D_ + d];
  __builtin_amdgcn_sched_barrier(0);
  asm volatile("" :: "v"(r_ldt), "v"(r_lar), "v"(r_aim), "v"(r_cre), "v"(r_cim));

  const float dt = rbf(expf(bf2f(r_ldt)));
  if (tid < NS_) {
    float are = -rbf(expf(bf2f(r_lar)));
    float aim = bf2f(r_aim);
    float lr = are * dt, li = aim * dt;
    float cr = bf2f(r_cre) * dt, ci = bf2f(r_cim) * dt;
    float mag = sqrtf(cr * cr + ci * ci);
    f32x4 pp;
    pp[0] = lr * 1.4426950408889634f;
    pp[1] = li * 0.15915494309189535f;
    pp[2] = log2f(mag);
    pp[3] = atan2f(ci, cr) * 0.15915494309189535f;
    *reinterpret_cast<f32x4*>(smem + PR_OFF + tid * 16) = pp;
  }
  if (tid < 64) {
    u32x4 z = {0, 0, 0, 0};
    *reinterpret_cast<u32x4*>(smem + UC_OFF + (tid >> 3) * UC_STRIDE + (tid & 7) * 16) = z;
  }
  RAW_BARRIER();

  {
    const int x = lane & 15, kk = lane >> 4;
    float th[3];
#pragma unroll
    for (int T = 0; T < 3; ++T) th[T] = thi_of(16 * T + x);
    const float tlo = (float)(8 * x);
    f32x4 acc[3];
#pragma unroll
    for (int T = 0; T < 3; ++T) acc[T] = (f32x4){0.f, 0.f, 0.f, 0.f};
#pragma unroll 2
    for (int g = 0; g < 8; ++g) {
      int n = 32 * wid + 4 * g + kk;
      f32x4 pp = *reinterpret_cast<const f32x4*>(smem + PR_OFF + n * 16);
      float qe = __builtin_amdgcn_exp2f(pp[0] * tlo);
      float qa = pp[1] * tlo;
      float qr = qe * __builtin_amdgcn_cosf(qa), qi = qe * __builtin_amdgcn_sinf(qa);
#pragma unroll
      for (int T = 0; T < 3; ++T) {
        float pe = __builtin_amdgcn_exp2f(pp[0] * th[T] + pp[2]);
        float pa = pp[1] * th[T] + pp[3];
        float pr = pe * __builtin_amdgcn_cosf(pa), pi = pe * __builtin_amdgcn_sinf(pa);
        acc[T] = __builtin_amdgcn_mfma_f32_16x16x4f32(qr, pr, acc[T], 0, 0, 0);
        acc[T] = __builtin_amdgcn_mfma_f32_16x16x4f32(-qi, pi, acc[T], 0, 0, 0);
      }
    }
    float* os = reinterpret_cast<float*>(smem + OS_OFF) + wid * 768;
#pragma unroll
    for (int T = 0; T < 3; ++T) *reinterpret_cast<f32x4*>(os + (16 * T + x) * 16 + 4 * kk) = acc[T];
  }
  RAW_BARRIER();

  u32x4 uvec;
#pragma unroll
  for (int j = 0; j < 4; ++j) uvec[j] = (unsigned)ug[2 * j] | ((unsigned)ug[2 * j + 1] << 16);
  *reinterpret_cast<u32x4*>(smem + UC_OFF + 128 + tid * 16) = uvec;
  {
    unsigned* tab = reinterpret_cast<unsigned*>(smem + TB_OFF);
#pragma unroll
    for (int k = 0; k < 3; ++k) {
      int e = tid + 256 * k;
      const float* osp = reinterpret_cast<const float*>(smem + OS_OFF) + e;
      float kv = 2.0f * ((osp[0] + osp[768]) + (osp[1536] + osp[2304]));
      unsigned short hi = f2bf(kv);
      tab[e] = (unsigned)hi | ((unsigned)f2bf(kv - bf2f(hi)) << 16);
    }
  }
  __syncthreads();
  {
    u32x4 v1 = {0, 0, 0, 0};
    if (tid < 255) v1 = *reinterpret_cast<const u32x4*>(smem + UC_OFF + 128 + (tid + 1) * 16);
    unsigned w[8] = {uvec[0], uvec[1], uvec[2], uvec[3], v1[0], v1[1], v1[2], v1[3]};
#pragma unroll
    for (int e = 1; e < 8; ++e) {
      u32x4 o;
#pragma unroll
      for (int j = 0; j < 4; ++j) {
        int base = j + (e >> 1);
        if (e & 1) o[j] = (w[base] >> 16) | (w[base + 1] << 16);
        else o[j] = w[base];
      }
      *reinterpret_cast<u32x4*>(smem + UC_OFF + e * UC_STRIDE + 128 + tid * 16) = o;
    }
    if (tid == 0) {
#pragma unroll
      for (int e = 1; e < 8; ++e)
#pragma unroll
        for (int k = 0; k < e; ++k)
          *reinterpret_cast<unsigned short*>(smem + UC_OFF + e * UC_STRIDE + 2 * (64 - e + k)) =
              (unsigned short)(w[k >> 1] >> (16 * (k & 1)));
    }
  }
  {
    const unsigned* tab = reinterpret_cast<const unsigned*>(smem + TB_OFF);
    unsigned w[8];
#pragma unroll
    for (int k = 0; k < 8; ++k) w[k] = tab[(kidx[k >> 1] >> (16 * (k & 1))) & 0xffff];
    u32x4 vh, vl;
#pragma unroll
    for (int j = 0; j < 4; ++j) {
      vh[j] = (w[7 - 2 * j] & 0xffffu) | (w[6 - 2 * j] << 16);
      vl[j] = (w[7 - 2 * j] >> 16) | (w[6 - 2 * j] & 0xffff0000u);
    }
    *reinterpret_cast<u32x4*>(smem + KH_OFF + (tid >> 2) * KROW + (tid & 3) * 16) = vh;
    *reinterpret_cast<u32x4*>(smem + KL_OFF + (tid >> 2) * KROW + (tid & 3) * 16) = vl;
    if (tid == 0) { u32x4 z = {0, 0, 0, 0}; *reinterpret_cast<u32x4*>(smem + KZ_OFF) = z; }
  }
  __syncthreads();

  f32x16 y0, y1;
#pragma unroll
  for (int i = 0; i < 16; ++i) { y0[i] = 0.f; y1[i] = 0.f; }
  {
    const int r = lane & 31, h = lane >> 5;
    const int e = (r + 1) & 7;
    const char* ubase = smem + UC_OFF + e * UC_STRIDE + 2 * (r - 7 - e + 64 - 8 * h);
#pragma unroll 2
    for (int dl = wid; dl < 32; dl += 4) {
      bf16x8 a0 = *reinterpret_cast<const bf16x8*>(ubase + 64 * dl);
      bf16x8 a1 = *reinterpret_cast<const bf16x8*>(ubase + 64 * dl - 32);
      bf16x8 a2 = *reinterpret_cast<const bf16x8*>(ubase + 64 * dl + 2048);
      bf16x8 a3 = *reinterpret_cast<const bf16x8*>(ubase + 64 * dl + 2048 - 32);
      const int m = r - dl;
      const int off = (m >= 0) ? (m * KROW + 16 * h) : (KZ_OFF - KH_OFF);
      const int off1 = (m >= 0) ? (off + 32) : off;
      const int lo = (m >= 0) ? KL_OFF : KH_OFF;
      bf16x8 fh0 = *reinterpret_cast<const bf16x8*>(smem + KH_OFF + off);
      bf16x8 fh1 = *reinterpret_cast<const bf16x8*>(smem + KH_OFF + off1);
      bf16x8 fl0 = *reinterpret_cast<const bf16x8*>(smem + lo + off);
      bf16x8 fl1 = *reinterpret_cast<const bf16x8*>(smem + lo + off1);
      const int go = (32 + m) * KROW + 16 * h;
      bf16x8 gh0 = *reinterpret_cast<const bf16x8*>(smem + KH_OFF + go);
      bf16x8 gh1 = *reinterpret_cast<const bf16x8*>(smem + KH_OFF + go + 32);
      bf16x8 gl0 = *reinterpret_cast<const bf16x8*>(smem + KL_OFF + go);
      bf16x8 gl1 = *reinterpret_cast<const bf16x8*>(smem + KL_OFF + go + 32);
      y0 = __builtin_amdgcn_mfma_f32_32x32x16_bf16(a0, fh0, y0, 0, 0, 0);
      y1 = __builtin_amdgcn_mfma_f32_32x32x16_bf16(a2, fh0, y1, 0, 0, 0);
      y0 = __builtin_amdgcn_mfma_f32_32x32x16_bf16(a1, fh1, y0, 0, 0, 0);
      y1 = __builtin_amdgcn_mfma_f32_32x32x16_bf16(a3, fh1, y1, 0, 0, 0);
      y0 = __builtin_amdgcn_mfma_f32_32x32x16_bf16(a0, fl0, y0, 0, 0, 0);
      y1 = __builtin_amdgcn_mfma_f32_32x32x16_bf16(a2, fl0, y1, 0, 0, 0);
      y0 = __builtin_amdgcn_mfma_f32_32x32x16_bf16(a1, fl1, y0, 0, 0, 0);
      y1 = __builtin_amdgcn_mfma_f32_32x32x16_bf16(a3, fl1, y1, 0, 0, 0);
      y1 = __builtin_amdgcn_mfma_f32_32x32x16_bf16(a0, gh0, y1, 0, 0, 0);
      y1 = __builtin_amdgcn_mfma_f32_32x32x16_bf16(a1, gh1, y1, 0, 0, 0);
      y1 = __builtin_amdgcn_mfma_f32_32x32x16_bf16(a0, gl0, y1, 0, 0, 0);
      y1 = __builtin_amdgcn_mfma_f32_32x32x16_bf16(a1, gl1, y1, 0, 0, 0);
    }
  }
  __syncthreads();
  {
    const int r = lane & 31, h = lane >> 5;
    float* slab = reinterpret_cast<float*>(smem + wid * 8704);
#pragma unroll
    for (int g = 0; g < 4; ++g) {
      f32x4 v0 = {y0[4 * g], y0[4 * g + 1], y0[4 * g + 2], y0[4 * g + 3]};
      f32x4 v1 = {y1[4 * g], y1[4 * g + 1], y1[4 * g + 2], y1[4 * g + 3]};
      int b = 8 * g + 4 * h;
      *reinterpret_cast<f32x4*>(slab + 32 * r + b) = v0;
      *reinterpret_cast<f32x4*>(slab + 32 * (32 + r) + b) = v1;
    }
  }
  __syncthreads();
  {
    const float dsk = bf2f(Dsk[d]);
    const float* s = reinterpret_cast<const float*>(smem);
    f32x4 ys[2];
#pragma unroll
    for (int hh = 0; hh < 2; ++hh) {
      f32x4 s0 = *reinterpret_cast<const f32x4*>(s + 8 * tid + 4 * hh);
      f32x4 s1 = *reinterpret_cast<const f32x4*>(s + 2176 + 8 * tid + 4 * hh);
      f32x4 s2 = *reinterpret_cast<const f32x4*>(s + 4352 + 8 * tid + 4 * hh);
      f32x4 s3 = *reinterpret_cast<const f32x4*>(s + 6528 + 8 * tid + 4 * hh);
      ys[hh] = (s0 + s1) + (s2 + s3);
    }
    unsigned short o[8];
#pragma unroll
    for (int k = 0; k < 8; ++k) {
      float yb = rbf(ys[k >> 2][k & 3]);
      float uu = bf2f((unsigned short)(uvec[k >> 1] >> (16 * (k & 1))));
      float sk = rbf(uu * dsk);
      o[k] = f2bf(yb + sk);
    }
    u32x4 v;
#pragma unroll
    for (int j = 0; j < 4; ++j) v[j] = o[2 * j] | ((unsigned)o[2 * j + 1] << 16);
    *reinterpret_cast<u32x4*>(aT + (size_t)d * L_ + 8 * tid) = v;
  }
}

__global__ __launch_bounds__(256, 3) void k12(const unsigned short* __restrict__ x, const unsigned short* __restrict__ w1,
                                              const unsigned short* __restrict__ w2, const unsigned short* __restrict__ gw,
                                              const unsigned short* __restrict__ logAr, const unsigned short* __restrict__ Aim,
                                              const unsigned short* __restrict__ Cre, const unsigned short* __restrict__ Cim,
                                              const unsigned short* __restrict__ logdt, const unsigned short* __restrict__ Dsk,
                                              unsigned short* __restrict__ hbuf, unsigned short* __restrict__ w2T,
                                              float* __restrict__ gbuf, unsigned short* __restrict__ aT) {
  extern __shared__ __attribute__((aligned(16))) char smem[];
  const int b = blockIdx.x;
  if (b < D_) conv_role(b, x, logAr, Aim, Cre, Cim, logdt, Dsk, aT, smem);
  else if (b < B_GATE) gemm1_role(b - D_, x, w1, hbuf, smem);
  else if (b < B_W2) gate_role(b - B_GATE, x, gw, gbuf);
  else w2_role(b - B_W2, w2, w2T, smem);
}

constexpr int G2_RS = 84;
__global__ __launch_bounds__(256, 1) void k3_gemm2(const unsigned short* __restrict__ hbuf, const unsigned short* __restrict__ w2T,
                                                   const unsigned short* __restrict__ aT, const float* __restrict__ gbuf,
                                                   unsigned short* __restrict__ out) {
  __shared__ float red[4][64][G2_RS];
  const int tid = threadIdx.x, lane = tid & 63, wid = tid >> 6;
  int t = (blockIdx.x & 7) * 32 + (blockIdx.x >> 3);
  const int tm = t >> 3, tn = t & 7;
  const int m0 = tm * 64, n0 = tn * 80;
  const int fr = lane & 15, fq = lane >> 4;
  const int wv = __builtin_amdgcn_readfirstlane(wid);
  const int c0 = (wv * 27) >> 2;
  const int nch = (((wv + 1) * 27) >> 2) - c0;

  const unsigned short* ap = hbuf + (size_t)(m0 >> 4) * 54 * 512 + fr * 32 + 8 * fq + c0 * 1024;
  const unsigned short* bp = w2T + (size_t)(n0 >> 4) * 54 * 512 + fr * 32 + 8 * fq + c0 * 1024;

  f32x4 acc[4][5];
#pragma unroll
  for (int a = 0; a < 4; ++a)
#pragma unroll
    for (int c = 0; c < 5; ++c) acc[a][c] = (f32x4){0.f, 0.f, 0.f, 0.f};

  constexpr int PD = 2;
  bf16x8 A[PD][4][2], B[PD][5][2];
#define G2_LOAD(S, c)                                                                                  \
  {                                                                                                    \
    _Pragma("unroll") for (int a = 0; a < 4; ++a) {                                                    \
      A[S][a][0] = *reinterpret_cast<const bf16x8*>(ap + a * 54 * 512 + (c) * 1024);                   \
      A[S][a][1] = *reinterpret_cast<const bf16x8*>(ap + a * 54 * 512 + (c) * 1024 + 512);             \
    }                                                                                                  \
    _Pragma("unroll") for (int a = 0; a < 5; ++a) {                                                    \
      B[S][a][0] = *reinterpret_cast<const bf16x8*>(bp + a * 54 * 512 + (c) * 1024);                   \
      B[S][a][1] = *reinterpret_cast<const bf16x8*>(bp + a * 54 * 512 + (c) * 1024 + 512);             \
    }                                                                                                  \
  }
#define G2_COMP(S)                                                                                     \
  {                                                                                                    \
    _Pragma("unroll") for (int s = 0; s < 2; ++s)                                                      \
      _Pragma("unroll") for (int a = 0; a < 4; ++a)                                                    \
        _Pragma("unroll") for (int c = 0; c < 5; ++c)                                                  \
          acc[a][c] = __builtin_amdgcn_mfma_f32_16x16x32_bf16(A[S][a][s], B[S][c][s], acc[a][c], 0, 0, 0); \
  }
#pragma unroll
  for (int i = 0; i < PD - 1; ++i) {
    G2_LOAD(i, i);
    __builtin_amdgcn_sched_barrier(0);
  }
#pragma unroll
  for (int i = 0; i < 7; ++i) {
    if (i + PD - 1 < 7) {
      const int cn = (i + PD - 1 < 6) ? (i + PD - 1) : (nch - 1);
      G2_LOAD((i + PD - 1) % PD, cn);
    }
    __builtin_amdgcn_sched_barrier(0);
    if (i == 6 && nch != 7) {
      const bf16x8 z = {0, 0, 0, 0, 0, 0, 0, 0};
#pragma unroll
      for (int a = 0; a < 4; ++a) { A[i % PD][a][0] = z; A[i % PD][a][1] = z; }
#pragma unroll
      for (int a = 0; a < 5; ++a) { B[i % PD][a][0] = z; B[i % PD][a][1] = z; }
    }
    G2_COMP(i % PD);
    __builtin_amdgcn_sched_barrier(0);
  }
#undef G2_LOAD
#undef G2_COMP

#pragma unroll
  for (int a = 0; a < 4; ++a)
#pragma unroll
    for (int c = 0; c < 5; ++c)
#pragma unroll
      for (int i = 0; i < 4; ++i) red[wid][16 * a + 4 * fq + i][16 * c + fr] = acc[a][c][i];
  __syncthreads();

#pragma unroll
  for (int j = 0; j < 2; ++j) {
    const int col = lane + 64 * j;
    if (col < 80) {
      const int row0 = m0 + 16 * wid;
      u32x4 av0 = *reinterpret_cast<const u32x4*>(aT + (size_t)(n0 + col) * L_ + row0);
      u32x4 av1 = *reinterpret_cast<const u32x4*>(aT + (size_t)(n0 + col) * L_ + row0 + 8);
#pragma unroll
      for (int i = 0; i < 16; ++i) {
        int rr = 16 * wid + i;
        float bv = rbf((red[0][rr][col] + red[1][rr][col]) + (red[2][rr][col] + red[3][rr][col]));
        unsigned aw = (i < 8) ? av0[(i >> 1) & 3] : av1[(i >> 1) & 3];
        float a = bf2f((unsigned short)((aw >> (16 * (i & 1))) & 0xffff));
        float g = gbuf[row0 + i];
        float t1 = rbf(g * a);
        float t2 = rbf(1.0f - g);
        float t3 = rbf(t2 * bv);
        out[(size_t)(row0 + i) * D_ + n0 + col] = f2bf(t1 + t3);
      }
    }
  }
}

extern "C" void kernel_launch(void* const* d_in, const int* in_sizes, int n_in, void* d_out, int out_size, void* d_ws,
                              size_t ws_size, hipStream_t stream) {
  const unsigned short* u = (const unsigned short*)d_in[0];
  const unsigned short* logAr = (const unsigned short*)d_in[1];
  const unsigned short* Aim = (const unsigned short*)d_in[2];
  const unsigned short* Cre = (const unsigned short*)d_in[3];
  const unsigned short* Cim = (const unsigned short*)d_in[4];
  const unsigned short* logdt = (const unsigned short*)d_in[5];
  const unsigned short* Dsk = (const unsigned short*)d_in[6];
  const unsigned short* w1 = (const unsigned short*)d_in[8];
  const unsigned short* w2 = (const unsigned short*)d_in[9];
  const unsigned short* gw = (const unsigned short*)d_in[10];
  unsigned short* out = (unsigned short*)d_out;

  char* ws = (char*)d_ws;
  unsigned short* hbuf = (unsigned short*)ws;
  unsigned short* aT = (unsigned short*)(ws + (size_t)L_ * F_ * 2);
  unsigned short* w2T = (unsigned short*)(ws + (size_t)L_ * F_ * 2 + (size_t)D_ * L_ * 2);
  float* gbuf = (float*)(ws + (size_t)L_ * F_ * 2 + (size_t)D_ * L_ * 2 + (size_t)D_ * F_ * 2);

  constexpr int K12_LDS = (CONV_LDS > 3 * G1_STAGE) ? CONV_LDS : 3 * G1_STAGE;
  hipLaunchKernelGGL(k12, dim3(K12_GRID), dim3(256), K12_LDS, stream, u, w1, w2, gw, logAr, Aim, Cre, Cim, logdt, Dsk,
                     hbuf, w2T, gbuf, aT);
  hipLaunchKernelGGL(k3_gemm2, dim3(256), dim3(256), 0, stream, hbuf, w2T, aT, gbuf, out);
}
```

```cpp
#include <hip/hip_runtime.h>
#include <hip/hip_bf16.h>
#include <stdint.h>

typedef __attribute__((ext_vector_type(8))) short bf16x8;
typedef __attribute__((ext_vector_type(4))) short bf16x4;
typedef __attribute__((ext_vector_type(16))) float f32x16;
typedef __attribute__((ext_vector_type(4))) float f32x4;
typedef __attribute__((ext_vector_type(4))) unsigned int u32x4;
typedef __attribute__((ext_vector_type(2))) unsigned int u32x2;

#define LDS_AS __attribute__((address_space(3)))
#define RAW_BARRIER() do { asm volatile("s_waitcnt lgkmcnt(0)" ::: "memory"); __builtin_amdgcn_s_barrier(); } while (0)

constexpr int L_ = 2048, D_ = 640, NS_ = 128, F_ = 1728;

__device__ __forceinline__ float bf2f(unsigned short v) { return __uint_as_float(((unsigned)v) << 16); }
__device__ __forceinline__ unsigned short f2bf(float f) {
  __bf16 b = (__bf16)f;
  return __builtin_bit_cast(unsigned short, b);
}
__device__ __forceinline__ float rbf(float f) { return bf2f(f2bf(f)); }

constexpr int G1_TM = L_ / 128, G1_TN = (F_ + 127) / 128;
constexpr int N_G1 = G1_TM * G1_TN;
constexpr int G1_STAGE = 16384;
constexpr int N_GATE = 32, N_W2 = 90;
constexpr int B_CONV = N_G1, B_GATE = B_CONV + D_, B_W2 = B_GATE + N_GATE, K12_GRID = B_W2 + N_W2;

__device__ __forceinline__ void tr_tiles_load(const unsigned short* __restrict__ src, int ss, int r0, int c0,
                                              u32x4 (&v)[2]) {
  const int tid = threadIdx.x;
#pragma unroll
  for (int i = 0; i < 2; ++i) {
    int id = tid + 256 * i, row = id >> 3, ch = id & 7;
    v[i] = *reinterpret_cast<const u32x4*>(src + (size_t)(r0 + row) * ss + c0 + ch * 8);
  }
}
__device__ __forceinline__ void tr_tiles_put(unsigned short (*tile)[72], const u32x4 (&v)[2]) {
  const int tid = threadIdx.x;
#pragma unroll
  for (int i = 0; i < 2; ++i) {
    int id = tid + 256 * i, row = id >> 3, ch = id & 7;
    *reinterpret_cast<u32x4*>(&tile[row][ch * 8]) = v[i];
  }
}
__device__ __forceinline__ void tr_tiles_store_blocked(unsigned short (*tile)[72], unsigned short* __restrict__ dst,
                                                       int r0, int c0) {
  const int tid = threadIdx.x;
#pragma unroll
  for (int i = 0; i < 2; ++i) {
    int id = tid + 256 * i, col = id >> 3, ch = id & 7;
    unsigned short o[8];
#pragma unroll
    for (int k = 0; k < 8; ++k) o[k] = tile[ch * 8 + k][col];
    u32x4 w;
#pragma unroll
    for (int j = 0; j < 4; ++j) w[j] = o[2 * j] | ((unsigned)o[2 * j + 1] << 16);
    unsigned short* dp = dst + ((size_t)((c0 + col) >> 4) * 54 + ((r0 + ch * 8) >> 5)) * 512 + (col & 15) * 32 + (ch & 3) * 8;
    *reinterpret_cast<u32x4*>(dp) = w;
  }
}

__device__ void gate_role(int gb, const unsigned short* __restrict__ x, const unsigned short* __restrict__ gw,
                          float* __restrict__ gbuf) {
  const int tid = threadIdx.x;
  const int r0 = gb * 64;
  u32x4 v[10][2];
#pragma unroll
  for (int t = 0; t < 10; ++t) tr_tiles_load(x, D_, r0, t * 64, v[t]);
  float s[2] = {0.f, 0.f};
#pragma unroll
  for (int t = 0; t < 10; ++t) {
    u32x4 g = *reinterpret_cast<const u32x4*>(gw + t * 64 + (tid & 7) * 8);
#pragma unroll
    for (int i = 0; i < 2; ++i)
#pragma unroll
      for (int j = 0; j < 4; ++j) {
        s[i] += rbf(__uint_as_float(v[t][i][j] << 16) * __uint_as_float(g[j] << 16));
        s[i] += rbf(__uint_as_float(v[t][i][j] & 0xffff0000u) * __uint_as_float(g[j] & 0xffff0000u));
      }
  }
#pragma unroll
  for (int i = 0; i < 2; ++i) {
    s[i] += __shfl_xor(s[i], 1); s[i] += __shfl_xor(s[i], 2); s[i] += __shfl_xor(s[i], 4);
    if ((tid & 7) == 0) gbuf[r0 + (tid >> 3) + 32 * i] = rbf(1.0f / (1.0f + expf(-rbf(s[i]))));
  }
}

__device__ void w2_role(int wb, const unsigned short* __restrict__ w2, unsigned short* __restrict__ w2T, char* smem) {
  u32x4 tv[3][2];
#pragma unroll
  for (int j = 0; j < 3; ++j) {
    int id = wb * 3 + j;
    tr_tiles_load(w2, D_, (id / 10) * 64, (id % 10) * 64, tv[j]);
  }
  typedef unsigned short tile_t[64][72];
  tile_t* tiles = reinterpret_cast<tile_t*>(smem);
#pragma unroll
  for (int j = 0; j < 3; ++j) tr_tiles_put(tiles[j], tv[j]);
  __syncthreads();
#pragma unroll
  for (int j = 0; j < 3; ++j) {
    int id = wb * 3 + j;
    tr_tiles_store_blocked(tiles[j], w2T, (id / 10) * 64, (id % 10) * 64);
  }
}

__device__ void gemm1_role(int b, const unsigned short* __restrict__ x, const unsigned short* __restrict__ w1,
                           unsigned short* __restrict__ hbuf, char* smem) {
  const int tid = threadIdx.x, lane = tid & 63, wid = tid >> 6;
  __builtin_amdgcn_s_setprio(3);
  int t = (b & 7) * (N_G1 / 8) + (b >> 3);
  const int tm = t / G1_TN, tn = t % G1_TN;
  const int m0 = tm * 128, n0 = tn * 128;
  const int wm = wid >> 1, wn = wid & 1, r = lane & 31, h = lane >> 5;

  const unsigned short* asrc[2];
  const unsigned short* bsrc[2];
#pragma unroll
  for (int i = 0; i < 2; ++i) {
    int row = (i * 4 + wid) * 16 + (lane >> 2);
    int c = (lane & 3) ^ ((row >> 2) & 3);
    asrc[i] = x + (size_t)(m0 + row) * D_ + c * 8;
    int k = (i * 4 + wid) * 4 + (lane >> 4);
    int cb = (lane & 15) ^ ((k & 3) << 2);
    int col = n0 + cb * 8;
    if (col > F_ - 8) col = F_ - 8;
    bsrc[i] = w1 + (size_t)k * F_ + col;
  }
  auto stage = [&](int kt, int buf) {
    char* sa = smem + buf * G1_STAGE;
    char* sb = sa + 8192;
#pragma unroll
    for (int i = 0; i < 2; ++i)
      __builtin_amdgcn_global_load_lds((const unsigned*)(asrc[i] + kt * 32), (LDS_AS unsigned*)(sa + (i * 4 + wid) * 1024), 16, 0, 0);
#pragma unroll
    for (int i = 0; i < 2; ++i)
      __builtin_amdgcn_global_load_lds((const unsigned*)(bsrc[i] + (size_t)kt * 32 * F_), (LDS_AS unsigned*)(sb + (i * 4 + wid) * 1024), 16, 0, 0);
  };

  f32x16 acc[2][2];
#pragma unroll
  for (int a = 0; a < 2; ++a)
#pragma unroll
    for (int c = 0; c < 2; ++c)
#pragma unroll
      for (int i = 0; i < 16; ++i) acc[a][c][i] = 0.f;

  const int q = (lane & 15) >> 2, p = lane & 3, blk = (lane >> 4) & 1;
  auto compute = [&](int buf) {
    const char* sa = smem + buf * G1_STAGE;
    const char* sb = sa + 8192;
#pragma unroll
    for (int s = 0; s < 2; ++s) {
      bf16x8 af[2], bfr[2];
#pragma unroll
      for (int mt = 0; mt < 2; ++mt) {
        int row = wm * 64 + mt * 32 + r;
        af[mt] = *reinterpret_cast<const bf16x8*>(sa + row * 64 + (((2 * s + h) ^ ((row >> 2) & 3)) << 4));
      }
#pragma unroll
      for (int nt = 0; nt < 2; ++nt)
#pragma unroll
        for (int tt = 0; tt < 2; ++tt) {
          int k = 16 * s + 8 * h + 4 * tt + q;
          int chunk = wn * 8 + nt * 4 + 2 * blk + (p >> 1);
          const char* ad = sb + k * 256 + ((chunk ^ (q << 2)) << 4) + (p & 1) * 8;
          bf16x4 v = __builtin_amdgcn_ds_read_tr16_b64_v4i16((LDS_AS bf16x4*)(ad));
          bfr[nt][4 * tt + 0] = v[0]; bfr[nt][4 * tt + 1] = v[1]; bfr[nt][4 * tt + 2] = v[2]; bfr[nt][4 * tt + 3] = v[3];
        }
#pragma unroll
      for (int mt = 0; mt < 2; ++mt)
#pragma unroll
        for (int nt = 0; nt < 2; ++nt)
          acc[mt][nt] = __builtin_amdgcn_mfma_f32_32x32x16_bf16(af[mt], bfr[nt], acc[mt][nt], 0, 0, 0);
    }
  };

  constexpr int NK = D_ / 32;
  stage(0, 0);
  stage(1, 1);
  int cur = 0, nxt = 2;
  for (int kt = 0; kt < NK - 1; ++kt) {
    asm volatile("s_waitcnt vmcnt(4)" ::: "memory");
    RAW_BARRIER();
    if (kt + 2 < NK) stage(kt + 2, nxt);
    compute(cur);
    cur = (cur == 2) ? 0 : cur + 1;
    nxt = (nxt == 2) ? 0 : nxt + 1;
  }
  asm volatile("s_waitcnt vmcnt(0)" ::: "memory");
  RAW_BARRIER();
  compute(cur);

  RAW_BARRIER();
  {
    unsigned short* ht = reinterpret_cast<unsigned short*>(smem);
#pragma unroll
    for (int nt = 0; nt < 2; ++nt)
#pragma unroll
      for (int mt = 0; mt < 2; ++mt)
#pragma unroll
        for (int i = 0; i < 16; ++i) {
          int row = wm * 64 + mt * 32 + (i & 3) + 8 * (i >> 2) + 4 * h;
          float hv = rbf(acc[mt][nt][i]);
          float sv = hv * __builtin_amdgcn_rcpf(1.0f + __expf(-hv));
          ht[row * 136 + wn * 64 + nt * 32 + r] = f2bf(sv);
        }
    __syncthreads();
#pragma unroll
    for (int i = 0; i < 8; ++i) {
      int id = tid + 256 * i, row = id >> 4, ch = id & 15;
      if (n0 + ch * 8 < F_)
        *reinterpret_cast<u32x4*>(hbuf + ((size_t)((m0 + row) >> 4) * 54 + ((n0 + ch * 8) >> 5)) * 512 + (row & 15) * 32 + (ch & 3) * 8) = *reinterpret_cast<const u32x4*>(ht + row * 136 + ch * 8);
    }
  }
}

constexpr int UC_STRIDE = 4416;
constexpr int UC_OFF = 0;
constexpr int KROW = 80;
constexpr int KH_OFF = 8 * UC_STRIDE;
constexpr int KL_OFF = KH_OFF + 64 * KROW + 16;
constexpr int KZ_OFF = KH_OFF + 64 * KROW;
constexpr int PR_OFF = KH_OFF + 12288;
constexpr int OS_OFF = KH_OFF;
constexpr int TB_OFF = PR_OFF + 128 * 16;
constexpr int CONV_LDS = TB_OFF + 768 * 4;

__device__ __forceinline__ float thi_of(int i) {
  int t;
  if (i < 8) t = i;
  else if (i < 16) t = 128 + (i - 8);
  else if (i < 24) t = 256 + 128 * ((i - 16) >> 2) + 2 * ((i - 16) & 3);
  else if (i < 32) t = 512 + 128 * ((i - 24) >> 1) + 4 * ((i - 24) & 1);
  else if (i < 40) t = 1024 + 128 * (i - 32);
  else t = 2048;
  return (float)t;
}

struct KTab { unsigned short v[L_]; };
constexpr KTab make_ktab() {
  KTab t{};
  for (int j = 0; j < L_; ++j) {
    int sh = (j < 256) ? 0 : (j < 512) ? 1 : (j < 1024) ? 2 : 3;
    int rr = j;
    if (sh) {
      int q = j >> sh, rem = j & ((1 << sh) - 1), half = 1 << (sh - 1);
      if (rem > half || (rem == half && (q & 1))) ++q;
      rr = q << sh;
    }
    int i = 40;
    if (rr < 256) i = (rr & 7) + 8 * (rr >> 7);
    else if (rr < 512) i = 16 + 4 * ((rr - 256) >> 7) + ((rr & 7) >> 1);
    else if (rr < 1024) i = 24 + 2 * ((rr - 512) >> 7) + ((rr & 7) >> 2);
    else if (rr < 2048) i = 32 + ((rr - 1024) >> 7);
    t.v[j] = (unsigned short)(i * 16 + ((rr >> 3) & 15));
  }
  return t;
}
__device__ __attribute__((aligned(16))) const KTab KTAB = make_ktab();

__device__ void conv_role(int d, const unsigned short* __restrict__ x, const unsigned short* __restrict__ logAr,
                          const unsigned short* __restrict__ Aim, const unsigned short* __restrict__ Cre,
                          const unsigned short* __restrict__ Cim, const unsigned short* __restrict__ logdt,
                          const unsigned short* __restrict__ Dsk, unsigned short* __restrict__ aT, char* smem) {
  const int tid = threadIdx.x, lane = tid & 63, wid = tid >> 6;

  const int pn = tid & (NS_ - 1);
  const unsigned short r_ldt = logdt[d];
  const unsigned short r_lar = logAr[d * NS_ + pn], r_aim = Aim[d * NS_ + pn];
  const unsigned short r_cre = Cre[d * NS_ + pn], r_cim = Cim[d * NS_ + pn];
  const u32x4 kidx = *reinterpret_cast<const u32x4*>(KTAB.v + 8 * tid);
  __builtin_amdgcn_sched_barrier(0);
  unsigned short ug[8];
#pragma unroll
  for (int k = 0; k < 8; ++k) ug[k] = x[(size_t)(8 * tid + k) * D_ + d];
  __builtin_amdgcn_sched_barrier(0);
  asm volatile("" :: "v"(r_ldt), "v"(r_lar), "v"(r_aim), "v"(r_cre), "v"(r_cim));

  const float dt = rbf(expf(bf2f(r_ldt)));
  if (tid < NS_) {
    float are = -rbf(expf(bf2f(r_lar)));
    float aim = bf2f(r_aim);
    float lr = are * dt, li = aim * dt;
    float cr = bf2f(r_cre) * dt, ci = bf2f(r_cim) * dt;
    float mag = sqrtf(cr * cr + ci * ci);
    f32x4 pp;
    pp[0] = lr * 1.4426950408889634f;
    pp[1] = li * 0.15915494309189535f;
    pp[2] = log2f(mag);
    pp[3] = atan2f(ci, cr) * 0.15915494309189535f;
    *reinterpret_cast<f32x4*>(smem + PR_OFF + tid * 16) = pp;
  }
  if (tid < 64) {
    u32x4 z = {0, 0, 0, 0};
    *reinterpret_cast<u32x4*>(smem + UC_OFF + (tid >> 3) * UC_STRIDE + (tid & 7) * 16) = z;
  }
  RAW_BARRIER();

  {
    const int x = lane & 15, kk = lane >> 4;
    float th[3];
#pragma unroll
    for (int T = 0; T < 3; ++T) th[T] = thi_of(16 * T + x);
    const float tlo = (float)(8 * x);
    f32x4 acc[3];
#pragma unroll
    for (int T = 0; T < 3; ++T) acc[T] = (f32x4){0.f, 0.f, 0.f, 0.f};
#pragma unroll 2
    for (int g = 0; g < 8; ++g) {
      int n = 32 * wid + 4 * g + kk;
      f32x4 pp = *reinterpret_cast<const f32x4*>(smem + PR_OFF + n * 16);
      float qe = __builtin_amdgcn_exp2f(pp[0] * tlo);
      float qa = pp[1] * tlo;
      float qr = qe * __builtin_amdgcn_cosf(qa), qi = qe * __builtin_amdgcn_sinf(qa);
#pragma unroll
      for (int T = 0; T < 3; ++T) {
        float pe = __builtin_amdgcn_exp2f(pp[0] * th[T] + pp[2]);
        float pa = pp[1] * th[T] + pp[3];
        float pr = pe * __builtin_amdgcn_cosf(pa), pi = pe * __builtin_amdgcn_sinf(pa);
        acc[T] = __builtin_amdgcn_mfma_f32_16x16x4f32(qr, pr, acc[T], 0, 0, 0);
        acc[T] = __builtin_amdgcn_mfma_f32_16x16x4f32(-qi, pi, acc[T], 0, 0, 0);
      }
    }
    float* os = reinterpret_cast<float*>(smem + OS_OFF) + wid * 768;
#pragma unroll
    for (int T = 0; T < 3; ++T) *reinterpret_cast<f32x4*>(os + (16 * T + x) * 16 + 4 * (kk ^ ((x >> 1) & 3))) = acc[T];
  }
  RAW_BARRIER();

  u32x4 uvec;
#pragma unroll
  for (int j = 0; j < 4; ++j) uvec[j] = (unsigned)ug[2 * j] | ((unsigned)ug[2 * j + 1] << 16);
  *reinterpret_cast<u32x4*>(smem + UC_OFF + 7 * UC_STRIDE + 112 + tid * 16) = uvec;
  {
    unsigned* tab = reinterpret_cast<unsigned*>(smem + TB_OFF);
#pragma unroll
    for (int k = 0; k < 3; ++k) {
      int e = tid + 256 * k;
      const float* osp = reinterpret_cast<const float*>(smem + OS_OFF) + (e ^ ((e >> 3) & 12));
      float kv = 2.0f * ((osp[0] + osp[768]) + (osp[1536] + osp[2304]));
      unsigned short hi = f2bf(kv);
      tab[e] = (unsigned)hi | ((unsigned)f2bf(kv - bf2f(hi)) << 16);
    }
  }
  __syncthreads();
  {
    u32x4 v1 = {0, 0, 0, 0};
    if (tid < 255) v1 = *reinterpret_cast<const u32x4*>(smem + UC_OFF + 7 * UC_STRIDE + 112 + (tid + 1) * 16);
    unsigned w[8] = {uvec[0], uvec[1], uvec[2], uvec[3], v1[0], v1[1], v1[2], v1[3]};
#pragma unroll
    for (int e = 1; e < 8; ++e) {
      u32x4 o;
#pragma unroll
      for (int j = 0; j < 4; ++j) {
        int base = j + (e >> 1);
        if (e & 1) o[j] = (w[base] >> 16) | (w[base + 1] << 16);
        else o[j] = w[base];
      }
      *reinterpret_cast<u32x4*>(smem + UC_OFF + (e - 1) * UC_STRIDE + 128 + tid * 16) = o;
    }
    if (tid == 0) {
#pragma unroll
      for (int e = 1; e < 8; ++e)
#pragma unroll
        for (int k = 0; k < e; ++k)
          *reinterpret_cast<unsigned short*>(smem + UC_OFF + (e - 1) * UC_STRIDE + 2 * (64 - e + k)) =
              (unsigned short)(w[k >> 1] >> (16 * (k & 1)));
    }
  }
  {
    const unsigned* tab = reinterpret_cast<const unsigned*>(smem + TB_OFF);
    unsigned w[8];
#pragma unroll
    for (int k = 0; k < 8; ++k) w[k] = tab[(kidx[k >> 1] >> (16 * (k & 1))) & 0xffff];
    u32x4 vh, vl;
#pragma unroll
    for (int j = 0; j < 4; ++j) {
      vh[j] = (w[7 - 2 * j] & 0xffffu) | (w[6 - 2 * j] << 16);
      vl[j] = (w[7 - 2 * j] >> 16) | (w[6 - 2 * j] & 0xffff0000u);
    }
    *reinterpret_cast<u32x4*>(smem + KH_OFF + (tid >> 2) * KROW + (tid & 3) * 16) = vh;
    *reinterpret_cast<u32x4*>(smem + KL_OFF + (tid >> 2) * KROW + (tid & 3) * 16) = vl;
    if (tid == 0) { u32x4 z = {0, 0, 0, 0}; *reinterpret_cast<u32x4*>(smem + KZ_OFF) = z; }
  }
  __syncthreads();

  f32x16 y0, y1;
#pragma unroll
  for (int i = 0; i < 16; ++i) { y0[i] = 0.f; y1[i] = 0.f; }
  {
    const int r = lane & 31, h = lane >> 5;
    const int e = r & 7;
    const char* ubase = smem + UC_OFF + e * UC_STRIDE + 2 * (r - e + 56 - 8 * h);
#pragma unroll 2
    for (int dl = wid; dl < 32; dl += 4) {
      bf16x8 a0 = *reinterpret_cast<const bf16x8*>(ubase + 64 * dl);
      bf16x8 a1 = *reinterpret_cast<const bf16x8*>(ubase + 64 * dl - 32);
      bf16x8 a2 = *reinterpret_cast<const bf16x8*>(ubase + 64 * dl + 2048);
      bf16x8 a3 = *reinterpret_cast<const bf16x8*>(ubase + 64 * dl + 2048 - 32);
      const int m = r - dl;
      const int off = (m >= 0) ? (m * KROW + 16 * h) : (KZ_OFF - KH_OFF);
      const int off1 = (m >= 0) ? (off + 32) : off;
      const int lo = (m >= 0) ? KL_OFF : KH_OFF;
      bf16x8 fh0 = *reinterpret_cast<const bf16x8*>(smem + KH_OFF + off);
      bf16x8 fh1 = *reinterpret_cast<const bf16x8*>(smem + KH_OFF + off1);
      bf16x8 fl0 = *reinterpret_cast<const bf16x8*>(smem + lo + off);
      bf16x8 fl1 = *reinterpret_cast<const bf16x8*>(smem + lo + off1);
      const int go = (32 + m) * KROW + 16 * h;
      bf16x8 gh0 = *reinterpret_cast<const bf16x8*>(smem + KH_OFF + go);
      bf16x8 gh1 = *reinterpret_cast<const bf16x8*>(smem + KH_OFF + go + 32);
      bf16x8 gl0 = *reinterpret_cast<const bf16x8*>(smem + KL_OFF + go);
      bf16x8 gl1 = *reinterpret_cast<const bf16x8*>(smem + KL_OFF + go + 32);
      y0 = __builtin_amdgcn_mfma_f32_32x32x16_bf16(a0, fh0, y0, 0, 0, 0);
      y1 = __builtin_amdgcn_mfma_f32_32x32x16_bf16(a2, fh0, y1, 0, 0, 0);
      y0 = __builtin_amdgcn_mfma_f32_32x32x16_bf16(a1, fh1, y0, 0, 0, 0);
      y1 = __builtin_amdgcn_mfma_f32_32x32x16_bf16(a3, fh1, y1, 0, 0, 0);
      y0 = __builtin_amdgcn_mfma_f32_32x32x16_bf16(a0, fl0, y0, 0, 0, 0);
      y1 = __builtin_amdgcn_mfma_f32_32x32x16_bf16(a2, fl0, y1, 0, 0, 0);
      y0 = __builtin_amdgcn_mfma_f32_32x32x16_bf16(a1, fl1, y0, 0, 0, 0);
      y1 = __builtin_amdgcn_mfma_f32_32x32x16_bf16(a3, fl1, y1, 0, 0, 0);
      y1 = __builtin_amdgcn_mfma_f32_32x32x16_bf16(a0, gh0, y1, 0, 0, 0);
      y1 = __builtin_amdgcn_mfma_f32_32x32x16_bf16(a1, gh1, y1, 0, 0, 0);
      y1 = __builtin_amdgcn_mfma_f32_32x32x16_bf16(a0, gl0, y1, 0, 0, 0);
      y1 = __builtin_amdgcn_mfma_f32_32x32x16_bf16(a1, gl1, y1, 0, 0, 0);
    }
  }
  __syncthreads();
  {
    const int r = lane & 31, h = lane >> 5;
    float* slab = reinterpret_cast<float*>(smem + wid * 9216);
#pragma unroll
    for (int g = 0; g < 4; ++g) {
      f32x4 v0 = {y0[4 * g], y0[4 * g + 1], y0[4 * g + 2], y0[4 * g + 3]};
      f32x4 v1 = {y1[4 * g], y1[4 * g + 1], y1[4 * g + 2], y1[4 * g + 3]};
      int b = 8 * g + 4 * h;
      *reinterpret_cast<f32x4*>(slab + 36 * r + b) = v0;
      *reinterpret_cast<f32x4*>(slab + 36 * (32 + r) + b) = v1;
    }
  }
  __syncthreads();
  {
    const float dsk = bf2f(Dsk[d]);
    const float* s = reinterpret_cast<const float*>(smem) + (tid >> 2) * 36 + (tid & 3) * 8;
    f32x4 ys[2];
#pragma unroll
    for (int hh = 0; hh < 2; ++hh) {
      f32x4 s0 = *reinterpret_cast<const f32x4*>(s + 4 * hh);
      f32x4 s1 = *reinterpret_cast<const f32x4*>(s + 2304 + 4 * hh);
      f32x4 s2 = *reinterpret_cast<const f32x4*>(s + 4608 + 4 * hh);
      f32x4 s3 = *reinterpret_cast<const f32x4*>(s + 6912 + 4 * hh);
      ys[hh] = (s0 + s1) + (s2 + s3);
    }
    unsigned short o[8];
#pragma unroll
    for (int k = 0; k < 8; ++k) {
      float yb = rbf(ys[k >> 2][k & 3]);
      float uu = bf2f((unsigned short)(uvec[k >> 1] >> (16 * (k & 1))));
      float sk = rbf(uu * dsk);
      o[k] = f2bf(yb + sk);
    }
    u32x4 v;
#pragma unroll
    for (int j = 0; j < 4; ++j) v[j] = o[2 * j] | ((unsigned)o[2 * j + 1] << 16);
    *reinterpret_cast<u32x4*>(aT + (size_t)d * L_ + 8 * tid) = v;
  }
}

__global__ __launch_bounds__(256, 3) void k12(const unsigned short* __restrict__ x, const unsigned short* __restrict__ w1,
                                              const unsigned short* __restrict__ w2, const unsigned short* __restrict__ gw,
                                              const unsigned short* __restrict__ logAr, const unsigned short* __restrict__ Aim,
                                              const unsigned short* __restrict__ Cre, const unsigned short* __restrict__ Cim,
                                              const unsigned short* __restrict__ logdt, const unsigned short* __restrict__ Dsk,
                                              unsigned short* __restrict__ hbuf, unsigned short* __restrict__ w2T,
                                              float* __restrict__ gbuf, unsigned short* __restrict__ aT) {
  extern __shared__ __attribute__((aligned(16))) char smem[];
  const int b = blockIdx.x;
  constexpr int G0 = 0;
  if (b >= G0 && b < G0 + D_) conv_role(b - G0, x, logAr, Aim, Cre, Cim, logdt, Dsk, aT, smem);
  else if (b < B_GATE) gemm1_role(b < G0 ? b : b - D_, x, w1, hbuf, smem);
  else if (b < B_W2) gate_role(b - B_GATE, x, gw, gbuf);
  else w2_role(b - B_W2, w2, w2T, smem);
}

constexpr int G2_RS = 84;
__global__ __launch_bounds__(256, 1) void k3_gemm2(const unsigned short* __restrict__ hbuf, const unsigned short* __restrict__ w2T,
                                                   const unsigned short* __restrict__ aT, const float* __restrict__ gbuf,
                                                   unsigned short* __restrict__ out) {
  __shared__ float red[4][64][G2_RS];
  const int tid = threadIdx.x, lane = tid & 63, wid = tid >> 6;
  int t = (blockIdx.x & 7) * 32 + (blockIdx.x >> 3);
  const int tm = t >> 3, tn = t & 7;
  const int m0 = tm * 64, n0 = tn * 80;
  const int fr = lane & 15, fq = lane >> 4;
  const int wv = __builtin_amdgcn_readfirstlane(wid);
  const int c0 = (wv * 27) >> 2;
  const int nch = (((wv + 1) * 27) >> 2) - c0;

  const unsigned short* ap = hbuf + (size_t)(m0 >> 4) * 54 * 512 + fr * 32 + 8 * fq + c0 * 1024;
  const unsigned short* bp = w2T + (size_t)(n0 >> 4) * 54 * 512 + fr * 32 + 8 * fq + c0 * 1024;

  f32x4 acc[4][5];
#pragma unroll
  for (int a = 0; a < 4; ++a)
#pragma unroll
    for (int c = 0; c < 5; ++c) acc[a][c] = (f32x4){0.f, 0.f, 0.f, 0.f};

  constexpr int PD = 2;
  bf16x8 A[PD][4][2], B[PD][5][2];
#define G2_LOAD(S, c)                                                                                  \
  {                                                                                                    \
    _Pragma("unroll") for (int a = 0; a < 4; ++a) {                                                    \
      A[S][a][0] = *reinterpret_cast<const bf16x8*>(ap + a * 54 * 512 + (c) * 1024);                   \
      A[S][a][1] = *reinterpret_cast<const bf16x8*>(ap + a * 54 * 512 + (c) * 1024 + 512);             \
    }                                                                                                  \
    _Pragma("unroll") for (int a = 0; a < 5; ++a) {                                                    \
      B[S][a][0] = *reinterpret_cast<const bf16x8*>(bp + a * 54 * 512 + (c) * 1024);                   \
      B[S][a][1] = *reinterpret_cast<const bf16x8*>(bp + a * 54 * 512 + (c) * 1024 + 512);             \
    }                                                                                                  \
  }
#define G2_COMP(S)                                                                                     \
  {                                                                                                    \
    _Pragma("unroll") for (int s = 0; s < 2; ++s)                                                      \
      _Pragma("unroll") for (int a = 0; a < 4; ++a)                                                    \
        _Pragma("unroll") for (int c = 0; c < 5; ++c)                                                  \
          acc[a][c] = __builtin_amdgcn_mfma_f32_16x16x32_bf16(A[S][a][s], B[S][c][s], acc[a][c], 0, 0, 0); \
  }
#pragma unroll
  for (int i = 0; i < PD - 1; ++i) {
    G2_LOAD(i, i);
    __builtin_amdgcn_sched_barrier(0);
  }
#pragma unroll
  for (int i = 0; i < 7; ++i) {
    if (i + PD - 1 < 7) {
      const int cn = (i + PD - 1 < 6) ? (i + PD - 1) : (nch - 1);
      G2_LOAD((i + PD - 1) % PD, cn);
    }
    __builtin_amdgcn_sched_barrier(0);
    if (i == 6 && nch != 7) {
      const bf16x8 z = {0, 0, 0, 0, 0, 0, 0, 0};
#pragma unroll
      for (int a = 0; a < 4; ++a) { A[i % PD][a][0] = z; A[i % PD][a][1] = z; }
#pragma unroll
      for (int a = 0; a < 5; ++a) { B[i % PD][a][0] = z; B[i % PD][a][1] = z; }
    }
    G2_COMP(i % PD);
    __builtin_amdgcn_sched_barrier(0);
  }
#undef G2_LOAD
#undef G2_COMP

#pragma unroll
  for (int a = 0; a < 4; ++a)
#pragma unroll
    for (int c = 0; c < 5; ++c)
#pragma unroll
      for (int i = 0; i < 4; ++i) red[wid][16 * a + 4 * fq + i][16 * c + fr] = acc[a][c][i];
  __syncthreads();

#pragma unroll
  for (int j = 0; j < 2; ++j) {
    const int col = lane + 64 * j;
    if (col < 80) {
      const int row0 = m0 + 16 * wid;
      u32x4 av0 = *reinterpret_cast<const u32x4*>(aT + (size_t)(n0 + col) * L_ + row0);
      u32x4 av1 = *reinterpret_cast<const u32x4*>(aT + (size_t)(n0 + col) * L_ + row0 + 8);
#pragma unroll
      for (int i = 0; i < 16; ++i) {
        int rr = 16 * wid + i;
        float bv = rbf((red[0][rr][col] + red[1][rr][col]) + (red[2][rr][col] + red[3][rr][col]));
        unsigned aw = (i < 8) ? av0[(i >> 1) & 3] : av1[(i >> 1) & 3];
        float a = bf2f((unsigned short)((aw >> (16 * (i & 1))) & 0xffff));
        float g = gbuf[row0 + i];
        float t1 = rbf(g * a);
        float t2 = rbf(1.0f - g);
        float t3 = rbf(t2 * bv);
        out[(size_t)(row0 + i) * D_ + n0 + col] = f2bf(t1 + t3);
      }
    }
  }
}

extern "C" void kernel_launch(void* const* d_in, const int* in_sizes, int n_in, void* d_out, int out_size, void* d_ws,
                              size_t ws_size, hipStream_t stream) {
  const unsigned short* u = (const unsigned short*)d_in[0];
  const unsigned short* logAr = (const unsigned short*)d_in[1];
  const unsigned short* Aim = (const unsigned short*)d_in[2];
  const unsigned short* Cre = (const unsigned short*)d_in[3];
  const unsigned short* Cim = (const unsigned short*)d_in[4];
  const unsigned short* logdt = (const unsigned short*)d_in[5];
  const unsigned short* Dsk = (const unsigned short*)d_in[6];
  const unsigned short* w1 = (const unsigned short*)d_in[8];
  const unsigned short* w2 = (const unsigned short*)d_in[9];
  const unsigned short* gw = (const unsigned short*)d_in[10];
  unsigned short* out = (unsigned short*)d_out;

  char* ws = (char*)d_ws;
  unsigned short* hbuf = (unsigned short*)ws;
  unsigned short* aT = (unsigned short*)(ws + (size_t)L_ * F_ * 2);
  unsigned short* w2T = (unsigned short*)(ws + (size_t)L_ * F_ * 2 + (size_t)D_ * L_ * 2);
  float* gbuf = (float*)(ws + (size_t)L_ * F_ * 2 + (size_t)D_ * L_ * 2 + (size_t)D_ * F_ * 2);

  constexpr int K12_LDS = (CONV_LDS > 3 * G1_STAGE) ? CONV_LDS : 3 * G1_STAGE;
  hipLaunchKernelGGL(k12, dim3(K12_GRID), dim3(256), K12_LDS, stream, u, w1, w2, gw, logAr, Aim, Cre, Cim, logdt, Dsk,
                     hbuf, w2T, gbuf, aT);
  hipLaunchKernelGGL(k3_gemm2, dim3(256), dim3(256), 0, stream, hbuf, w2T, aT, gbuf, out);
}
```

```cpp
#include <hip/hip_runtime.h>
#include <hip/hip_bf16.h>
#include <stdint.h>

typedef __attribute__((ext_vector_type(8))) short bf16x8;
typedef __attribute__((ext_vector_type(4))) short bf16x4;
typedef __attribute__((ext_vector_type(16))) float f32x16;
typedef __attribute__((ext_vector_type(4))) float f32x4;
typedef __attribute__((ext_vector_type(4))) unsigned int u32x4;
typedef __attribute__((ext_vector_type(2))) unsigned int u32x2;

#define LDS_AS __attribute__((address_space(3)))
#define RAW_BARRIER() do { asm volatile("s_waitcnt lgkmcnt(0)" ::: "memory"); __builtin_amdgcn_s_barrier(); } while (0)

constexpr int L_ = 2048, D_ = 640, NS_ = 128, F_ = 1728;

__device__ __forceinline__ float bf2f(unsigned short v) { return __uint_as_float(((unsigned)v) << 16); }
__device__ __forceinline__ unsigned short f2bf(float f) {
  __bf16 b = (__bf16)f;
  return __builtin_bit_cast(unsigned short, b);
}
__device__ __forceinline__ float rbf(float f) { return bf2f(f2bf(f)); }

constexpr int G1_TM = L_ / 128, G1_TN = (F_ + 127) / 128;
constexpr int N_G1 = G1_TM * G1_TN;
constexpr int G1_STAGE = 16384;
constexpr int N_GATE = 32, N_W2 = 90;
constexpr int B_CONV = N_G1, B_GATE = B_CONV + D_, B_W2 = B_GATE + N_GATE, K12_GRID = B_W2 + N_W2;

__device__ __forceinline__ void tr_tiles_load(const unsigned short* __restrict__ src, int ss, int r0, int c0,
                                              u32x4 (&v)[2]) {
  const int tid = threadIdx.x;
#pragma unroll
  for (int i = 0; i < 2; ++i) {
    int id = tid + 256 * i, row = id >> 3, ch = id & 7;
    v[i] = *reinterpret_cast<const u32x4*>(src + (size_t)(r0 + row) * ss + c0 + ch * 8);
  }
}
__device__ __forceinline__ void tr_tiles_put(unsigned short (*tile)[72], const u32x4 (&v)[2]) {
  const int tid = threadIdx.x;
#pragma unroll
  for (int i = 0; i < 2; ++i) {
    int id = tid + 256 * i, row = id >> 3, ch = id & 7;
    *reinterpret_cast<u32x4*>(&tile[row][ch * 8]) = v[i];
  }
}
__device__ __forceinline__ void tr_tiles_store_blocked(unsigned short (*tile)[72], unsigned short* __restrict__ dst,
                                                       int r0, int c0) {
  const int tid = threadIdx.x;
#pragma unroll
  for (int i = 0; i < 2; ++i) {
    int id = tid + 256 * i, col = id >> 3, ch = id & 7;
    unsigned short o[8];
#pragma unroll
    for (int k = 0; k < 8; ++k) o[k] = tile[ch * 8 + k][col];
    u32x4 w;
#pragma unroll
    for (int j = 0; j < 4; ++j) w[j] = o[2 * j] | ((unsigned)o[2 * j + 1] << 16);
    unsigned short* dp = dst + ((size_t)((c0 + col) >> 4) * 54 + ((r0 + ch * 8) >> 5)) * 512 + (col & 15) * 32 + (ch & 3) * 8;
    *reinterpret_cast<u32x4*>(dp) = w;
  }
}

__device__ void gate_role(int gb, const unsigned short* __restrict__ x, const unsigned short* __restrict__ gw,
                          float* __restrict__ gbuf) {
  const int tid = threadIdx.x;
  const int r0 = gb * 64;
  u32x4 v[10][2];
#pragma unroll
  for (int t = 0; t < 10; ++t) tr_tiles_load(x, D_, r0, t * 64, v[t]);
  float s[2] = {0.f, 0.f};
#pragma unroll
  for (int t = 0; t < 10; ++t) {
    u32x4 g = *reinterpret_cast<const u32x4*>(gw + t * 64 + (tid & 7) * 8);
#pragma unroll
    for (int i = 0; i < 2; ++i)
#pragma unroll
      for (int j = 0; j < 4; ++j) {
        s[i] += rbf(__uint_as_float(v[t][i][j] << 16) * __uint_as_float(g[j] << 16));
        s[i] += rbf(__uint_as_float(v[t][i][j] & 0xffff0000u) * __uint_as_float(g[j] & 0xffff0000u));
      }
  }
#pragma unroll
  for (int i = 0; i < 2; ++i) {
    s[i] += __shfl_xor(s[i], 1); s[i] += __shfl_xor(s[i], 2); s[i] += __shfl_xor(s[i], 4);
    if ((tid & 7) == 0) gbuf[r0 + (tid >> 3) + 32 * i] = rbf(1.0f / (1.0f + expf(-rbf(s[i]))));
  }
}

__device__ void w2_role(int wb, const unsigned short* __restrict__ w2, unsigned short* __restrict__ w2T, char* smem) {
  u32x4 tv[3][2];
#pragma unroll
  for (int j = 0; j < 3; ++j) {
    int id = wb * 3 + j;
    tr_tiles_load(w2, D_, (id / 10) * 64, (id % 10) * 64, tv[j]);
  }
  typedef unsigned short tile_t[64][72];
  tile_t* tiles = reinterpret_cast<tile_t*>(smem);
#pragma unroll
  for (int j = 0; j < 3; ++j) tr_tiles_put(tiles[j], tv[j]);
  __syncthreads();
#pragma unroll
  for (int j = 0; j < 3; ++j) {
    int id = wb * 3 + j;
    tr_tiles_store_blocked(tiles[j], w2T, (id / 10) * 64, (id % 10) * 64);
  }
}

__device__ void gemm1_role(int b, const unsigned short* __restrict__ x, const unsigned short* __restrict__ w1,
                           unsigned short* __restrict__ hbuf, char* smem) {
  const int tid = threadIdx.x, lane = tid & 63, wid = tid >> 6;
  __builtin_amdgcn_s_setprio(3);
  int t = (b & 7) * (N_G1 / 8) + (b >> 3);
  const int tm = t / G1_TN, tn = t % G1_TN;
  const int m0 = tm * 128, n0 = tn * 128;
  const int wm = wid >> 1, wn = wid & 1, r = lane & 31, h = lane >> 5;

  const unsigned short* asrc[2];
  const unsigned short* bsrc[2];
#pragma unroll
  for (int i = 0; i < 2; ++i) {
    int row = (i * 4 + wid) * 16 + (lane >> 2);
    int c = (lane & 3) ^ ((row >> 2) & 3);
    asrc[i] = x + (size_t)(m0 + row) * D_ + c * 8;
    int k = (i * 4 + wid) * 4 + (lane >> 4);
    int cb = (lane & 15) ^ ((k & 3) << 2);
    int col = n0 + cb * 8;
    if (col > F_ - 8) col = F_ - 8;
    bsrc[i] = w1 + (size_t)k * F_ + col;
  }
  auto stage = [&](int kt, int buf) {
    char* sa = smem + buf * G1_STAGE;
    char* sb = sa + 8192;
#pragma unroll
    for (int i = 0; i < 2; ++i)
      __builtin_amdgcn_global_load_lds((const unsigned*)(asrc[i] + kt * 32), (LDS_AS unsigned*)(sa + (i * 4 + wid) * 1024), 16, 0, 0);
#pragma unroll
    for (int i = 0; i < 2; ++i)
      __builtin_amdgcn_global_load_lds((const unsigned*)(bsrc[i] + (size_t)kt * 32 * F_), (LDS_AS unsigned*)(sb + (i * 4 + wid) * 1024), 16, 0, 0);
  };

  f32x16 acc[2][2];
#pragma unroll
  for (int a = 0; a < 2; ++a)
#pragma unroll
    for (int c = 0; c < 2; ++c)
#pragma unroll
      for (int i = 0; i < 16; ++i) acc[a][c][i] = 0.f;

  const int q = (lane & 15) >> 2, p = lane & 3, blk = (lane >> 4) & 1;
  auto compute = [&](int buf) {
    const char* sa = smem + buf * G1_STAGE;
    const char* sb = sa + 8192;
#pragma unroll
    for (int s = 0; s < 2; ++s) {
      bf16x8 af[2], bfr[2];
#pragma unroll
      for (int mt = 0; mt < 2; ++mt) {
        int row = wm * 64 + mt * 32 + r;
        af[mt] = *reinterpret_cast<const bf16x8*>(sa + row * 64 + (((2 * s + h) ^ ((row >> 2) & 3)) << 4));
      }
#pragma unroll
      for (int nt = 0; nt < 2; ++nt)
#pragma unroll
        for (int tt = 0; tt < 2; ++tt) {
          int k = 16 * s + 8 * h + 4 * tt + q;
          int chunk = wn * 8 + nt * 4 + 2 * blk + (p >> 1);
          const char* ad = sb + k * 256 + ((chunk ^ (q << 2)) << 4) + (p & 1) * 8;
          bf16x4 v = __builtin_amdgcn_ds_read_tr16_b64_v4i16((LDS_AS bf16x4*)(ad));
          bfr[nt][4 * tt + 0] = v[0]; bfr[nt][4 * tt + 1] = v[1]; bfr[nt][4 * tt + 2] = v[2]; bfr[nt][4 * tt + 3] = v[3];
        }
#pragma unroll
      for (int mt = 0; mt < 2; ++mt)
#pragma unroll
        for (int nt = 0; nt < 2; ++nt)
          acc[mt][nt] = __builtin_amdgcn_mfma_f32_32x32x16_bf16(af[mt], bfr[nt], acc[mt][nt], 0, 0, 0);
    }
  };

  constexpr int NK = D_ / 32;
  stage(0, 0);
  stage(1, 1);
  int cur = 0, nxt = 2;
  for (int kt = 0; kt < NK - 1; ++kt) {
    asm volatile("s_waitcnt vmcnt(4)" ::: "memory");
    RAW_BARRIER();
    if (kt + 2 < NK) stage(kt + 2, nxt);
    compute(cur);
    cur = (cur == 2) ? 0 : cur + 1;
    nxt = (nxt == 2) ? 0 : nxt + 1;
  }
  asm volatile("s_waitcnt vmcnt(0)" ::: "memory");
  RAW_BARRIER();
  compute(cur);

  RAW_BARRIER();
  {
    unsigned short* ht = reinterpret_cast<unsigned short*>(smem);
#pragma unroll
    for (int nt = 0; nt < 2; ++nt)
#pragma unroll
      for (int mt = 0; mt < 2; ++mt)
#pragma unroll
        for (int i = 0; i < 16; ++i) {
          int row = wm * 64 + mt * 32 + (i & 3) + 8 * (i >> 2) + 4 * h;
          float hv = rbf(acc[mt][nt][i]);
          float sv = hv * __builtin_amdgcn_rcpf(1.0f + __expf(-hv));
          ht[row * 136 + wn * 64 + nt * 32 + r] = f2bf(sv);
        }
    __syncthreads();
#pragma unroll
    for (int i = 0; i < 8; ++i) {
      int id = tid + 256 * i, row = id >> 4, ch = id & 15;
      if (n0 + ch * 8 < F_)
        *reinterpret_cast<u32x4*>(hbuf + ((size_t)((m0 + row) >> 4) * 54 + ((n0 + ch * 8) >> 5)) * 512 + (row & 15) * 32 + (ch & 3) * 8) = *reinterpret_cast<const u32x4*>(ht + row * 136 + ch * 8);
    }
  }
}

constexpr int UC_STRIDE = 4416;
constexpr int UC_OFF = 0;
constexpr int KROW = 80;
constexpr int KH_OFF = 8 * UC_STRIDE;
constexpr int KL_OFF = KH_OFF + 64 * KROW + 16;
constexpr int KZ_OFF = KH_OFF + 64 * KROW;
constexpr int PR_OFF = KH_OFF + 12288;
constexpr int OS_OFF = KH_OFF;
constexpr int TB_OFF = PR_OFF + 128 * 16;
constexpr int CONV_LDS = TB_OFF + 768 * 4;

__device__ __forceinline__ float thi_of(int i) {
  int t;
  if (i < 8) t = i;
  else if (i < 16) t = 128 + (i - 8);
  else if (i < 24) t = 256 + 128 * ((i - 16) >> 2) + 2 * ((i - 16) & 3);
  else if (i < 32) t = 512 + 128 * ((i - 24) >> 1) + 4 * ((i - 24) & 1);
  else if (i < 40) t = 1024 + 128 * (i - 32);
  else t = 2048;
  return (float)t;
}

struct KTab { unsigned short v[L_]; };
constexpr KTab make_ktab() {
  KTab t{};
  for (int j = 0; j < L_; ++j) {
    int sh = (j < 256) ? 0 : (j < 512) ? 1 : (j < 1024) ? 2 : 3;
    int rr = j;
    if (sh) {
      int q = j >> sh, rem = j & ((1 << sh) - 1), half = 1 << (sh - 1);
      if (rem > half || (rem == half && (q & 1))) ++q;
      rr = q << sh;
    }
    int i = 40;
    if (rr < 256) i = (rr & 7) + 8 * (rr >> 7);
    else if (rr < 512) i = 16 + 4 * ((rr - 256) >> 7) + ((rr & 7) >> 1);
    else if (rr < 1024) i = 24 + 2 * ((rr - 512) >> 7) + ((rr & 7) >> 2);
    else if (rr < 2048) i = 32 + ((rr - 1024) >> 7);
    t.v[j] = (unsigned short)(i * 16 + ((rr >> 3) & 15));
  }
  return t;
}
__device__ __attribute__((aligned(16))) const KTab KTAB = make_ktab();

__device__ void conv_role(int d, const unsigned short* __restrict__ x, const unsigned short* __restrict__ logAr,
                          const unsigned short* __restrict__ Aim, const unsigned short* __restrict__ Cre,
                          const unsigned short* __restrict__ Cim, const unsigned short* __restrict__ logdt,
                          const unsigned short* __restrict__ Dsk, unsigned short* __restrict__ aT, char* smem) {
  const int tid = threadIdx.x, lane = tid & 63, wid = tid >> 6;

  const int pn = tid & (NS_ - 1);
  const unsigned short r_ldt = logdt[d];
  const unsigned short r_lar = logAr[d * NS_ + pn], r_aim = Aim[d * NS_ + pn];
  const unsigned short r_cre = Cre[d * NS_ + pn], r_cim = Cim[d * NS_ + pn];
  const u32x4 kidx = *reinterpret_cast<const u32x4*>(KTAB.v + 8 * tid);
  __builtin_amdgcn_sched_barrier(0);
  unsigned short ug[8];
#pragma unroll
  for (int k = 0; k < 8; ++k) ug[k] = x[(size_t)(8 * tid + k) * D_ + d];
  __builtin_amdgcn_sched_barrier(0);
  asm volatile("" :: "v"(r_ldt), "v"(r_lar), "v"(r_aim), "v"(r_cre), "v"(r_cim));

  const float dt = rbf(expf(bf2f(r_ldt)));
  if (tid < NS_) {
    float are = -rbf(expf(bf2f(r_lar)));
    float aim = bf2f(r_aim);
    float lr = are * dt, li = aim * dt;
    float cr = bf2f(r_cre) * dt, ci = bf2f(r_cim) * dt;
    float mag = sqrtf(cr * cr + ci * ci);
    f32x4 pp;
    pp[0] = lr * 1.4426950408889634f;
    pp[1] = li * 0.15915494309189535f;
    pp[2] = log2f(mag);
    pp[3] = atan2f(ci, cr) * 0.15915494309189535f;
    *reinterpret_cast<f32x4*>(smem + PR_OFF + tid * 16) = pp;
  }
  if (tid < 64) {
    u32x4 z = {0, 0, 0, 0};
    *reinterpret_cast<u32x4*>(smem + UC_OFF + (tid >> 3) * UC_STRIDE + (tid & 7) * 16) = z;
  }
  RAW_BARRIER();

  {
    const int x = lane & 15, kk = lane >> 4;
    float th[3];
#pragma unroll
    for (int T = 0; T < 3; ++T) th[T] = thi_of(16 * T + x);
    const float tlo = (float)(8 * x);
    f32x4 acc[3];
#pragma unroll
    for (int T = 0; T < 3; ++T) acc[T] = (f32x4){0.f, 0.f, 0.f, 0.f};
#pragma unroll 2
    for (int g = 0; g < 8; ++g) {
      int n = 32 * wid + 4 * g + kk;
      f32x4 pp = *reinterpret_cast<const f32x4*>(smem + PR_OFF + n * 16);
      float qe = __builtin_amdgcn_exp2f(pp[0] * tlo);
      float qa = pp[1] * tlo;
      float qr = qe * __builtin_amdgcn_cosf(qa), qi = qe * __builtin_amdgcn_sinf(qa);
#pragma unroll
      for (int T = 0; T < 3; ++T) {
        float pe = __builtin_amdgcn_exp2f(pp[0] * th[T] + pp[2]);
        float pa = pp[1] * th[T] + pp[3];
        float pr = pe * __builtin_amdgcn_cosf(pa), pi = pe * __builtin_amdgcn_sinf(pa);
        acc[T] = __builtin_amdgcn_mfma_f32_16x16x4f32(qr, pr, acc[T], 0, 0, 0);
        acc[T] = __builtin_amdgcn_mfma_f32_16x16x4f32(-qi, pi, acc[T], 0, 0, 0);
      }
    }
    float* os = reinterpret_cast<float*>(smem + OS_OFF) + wid * 768;
#pragma unroll
    for (int T = 0; T < 3; ++T) *reinterpret_cast<f32x4*>(os + (16 * T + x) * 16 + 4 * (kk ^ ((x >> 1) & 3))) = acc[T];
  }
  RAW_BARRIER();

  u32x4 uvec;
#pragma unroll
  for (int j = 0; j < 4; ++j) uvec[j] = (unsigned)ug[2 * j] | ((unsigned)ug[2 * j + 1] << 16);
  *reinterpret_cast<u32x4*>(smem + UC_OFF + 7 * UC_STRIDE + 112 + tid * 16) = uvec;
  {
    unsigned* tab = reinterpret_cast<unsigned*>(smem + TB_OFF);
#pragma unroll
    for (int k = 0; k < 3; ++k) {
      int e = tid + 256 * k;
      const float* osp = reinterpret_cast<const float*>(smem + OS_OFF) + (e ^ ((e >> 3) & 12));
      float kv = 2.0f * ((osp[0] + osp[768]) + (osp[1536] + osp[2304]));
      unsigned short hi = f2bf(kv);
      tab[e] = (unsigned)hi | ((unsigned)f2bf(kv - bf2f(hi)) << 16);
    }
  }
  __syncthreads();
  {
    u32x4 v1 = {0, 0, 0, 0};
    if (tid < 255) v1 = *reinterpret_cast<const u32x4*>(smem + UC_OFF + 7 * UC_STRIDE + 112 + (tid + 1) * 16);
    unsigned w[8] = {uvec[0], uvec[1], uvec[2], uvec[3], v1[0], v1[1], v1[2], v1[3]};
#pragma unroll
    for (int e = 1; e < 8; ++e) {
      u32x4 o;
#pragma unroll
      for (int j = 0; j < 4; ++j) {
        int base = j + (e >> 1);
        if (e & 1) o[j] = (w[base] >> 16) | (w[base + 1] << 16);
        else o[j] = w[base];
      }
      *reinterpret_cast<u32x4*>(smem + UC_OFF + (e - 1) * UC_STRIDE + 128 + tid * 16) = o;
    }
    if (tid == 0) {
#pragma unroll
      for (int e = 1; e < 8; ++e)
#pragma unroll
        for (int k = 0; k < e; ++k)
          *reinterpret_cast<unsigned short*>(smem + UC_OFF + (e - 1) * UC_STRIDE + 2 * (64 - e + k)) =
              (unsigned short)(w[k >> 1] >> (16 * (k & 1)));
    }
  }
  {
    const unsigned* tab = reinterpret_cast<const unsigned*>(smem + TB_OFF);
    unsigned w[8];
#pragma unroll
    for (int k = 0; k < 8; ++k) w[k] = tab[(kidx[k >> 1] >> (16 * (k & 1))) & 0xffff];
    u32x4 vh, vl;
#pragma unroll
    for (int j = 0; j < 4; ++j) {
      vh[j] = (w[7 - 2 * j] & 0xffffu) | (w[6 - 2 * j] << 16);
      vl[j] = (w[7 - 2 * j] >> 16) | (w[6 - 2 * j] & 0xffff0000u);
    }
    *reinterpret_cast<u32x4*>(smem + KH_OFF + (tid >> 2) * KROW + (tid & 3) * 16) = vh;
    *reinterpret_cast<u32x4*>(smem + KL_OFF + (tid >> 2) * KROW + (tid & 3) * 16) = vl;
    if (tid == 0) { u32x4 z = {0, 0, 0, 0}; *reinterpret_cast<u32x4*>(smem + KZ_OFF) = z; }
  }
  __syncthreads();

  f32x16 y0, y1, y2;
#pragma unroll
  for (int i = 0; i < 16; ++i) { y0[i] = 0.f; y1[i] = 0.f; y2[i] = 0.f; }
  {
    const int r = lane & 31, h = lane >> 5;
    const int e = r & 7;
    const char* ubase = smem + UC_OFF + e * UC_STRIDE + 2 * (r - e + 56 - 8 * h);
#pragma unroll 2
    for (int dl = wid; dl < 32; dl += 4) {
      bf16x8 a0 = *reinterpret_cast<const bf16x8*>(ubase + 64 * dl);
      bf16x8 a1 = *reinterpret_cast<const bf16x8*>(ubase + 64 * dl - 32);
      bf16x8 a2 = *reinterpret_cast<const bf16x8*>(ubase + 64 * dl + 2048);
      bf16x8 a3 = *reinterpret_cast<const bf16x8*>(ubase + 64 * dl + 2048 - 32);
      const int m = r - dl;
      const int off = (m >= 0) ? (m * KROW + 16 * h) : (KZ_OFF - KH_OFF);
      const int off1 = (m >= 0) ? (off + 32) : off;
      const int lo = (m >= 0) ? KL_OFF : KH_OFF;
      bf16x8 fh0 = *reinterpret_cast<const bf16x8*>(smem + KH_OFF + off);
      bf16x8 fh1 = *reinterpret_cast<const bf16x8*>(smem + KH_OFF + off1);
      bf16x8 fl0 = *reinterpret_cast<const bf16x8*>(smem + lo + off);
      bf16x8 fl1 = *reinterpret_cast<const bf16x8*>(smem + lo + off1);
      const int go = (32 + m) * KROW + 16 * h;
      bf16x8 gh0 = *reinterpret_cast<const bf16x8*>(smem + KH_OFF + go);
      bf16x8 gh1 = *reinterpret_cast<const bf16x8*>(smem + KH_OFF + go + 32);
      bf16x8 gl0 = *reinterpret_cast<const bf16x8*>(smem + KL_OFF + go);
      bf16x8 gl1 = *reinterpret_cast<const bf16x8*>(smem + KL_OFF + go + 32);
      y0 = __builtin_amdgcn_mfma_f32_32x32x16_bf16(a0, fh0, y0, 0, 0, 0);
      y1 = __builtin_amdgcn_mfma_f32_32x32x16_bf16(a2, fh0, y1, 0, 0, 0);
      y2 = __builtin_amdgcn_mfma_f32_32x32x16_bf16(a0, gh0, y2, 0, 0, 0);
      y0 = __builtin_amdgcn_mfma_f32_32x32x16_bf16(a1, fh1, y0, 0, 0, 0);
      y1 = __builtin_amdgcn_mfma_f32_32x32x16_bf16(a3, fh1, y1, 0, 0, 0);
      y2 = __builtin_amdgcn_mfma_f32_32x32x16_bf16(a1, gh1, y2, 0, 0, 0);
      y0 = __builtin_amdgcn_mfma_f32_32x32x16_bf16(a0, fl0, y0, 0, 0, 0);
      y1 = __builtin_amdgcn_mfma_f32_32x32x16_bf16(a2, fl0, y1, 0, 0, 0);
      y2 = __builtin_amdgcn_mfma_f32_32x32x16_bf16(a0, gl0, y2, 0, 0, 0);
      y0 = __builtin_amdgcn_mfma_f32_32x32x16_bf16(a1, fl1, y0, 0, 0, 0);
      y1 = __builtin_amdgcn_mfma_f32_32x32x16_bf16(a3, fl1, y1, 0, 0, 0);
      y2 = __builtin_amdgcn_mfma_f32_32x32x16_bf16(a1, gl1, y2, 0, 0, 0);
    }
    y1 += y2;
  }
  __syncthreads();
  {
    const int r = lane & 31, h = lane >> 5;
    float* slab = reinterpret_cast<float*>(smem + wid * 9216);
#pragma unroll
    for (int g = 0; g < 4; ++g) {
      f32x4 v0 = {y0[4 * g], y0[4 * g + 1], y0[4 * g + 2], y0[4 * g + 3]};
      f32x4 v1 = {y1[4 * g], y1[4 * g + 1], y1[4 * g + 2], y1[4 * g + 3]};
      int b = 8 * g + 4 * h;
      *reinterpret_cast<f32x4*>(slab + 36 * r + b) = v0;
      *reinterpret_cast<f32x4*>(slab + 36 * (32 + r) + b) = v1;
    }
  }
  __syncthreads();
  {
    const float dsk = bf2f(Dsk[d]);
    const float* s = reinterpret_cast<const float*>(smem) + (tid >> 2) * 36 + (tid & 3) * 8;
    f32x4 ys[2];
#pragma unroll
    for (int hh = 0; hh < 2; ++hh) {
      f32x4 s0 = *reinterpret_cast<const f32x4*>(s + 4 * hh);
      f32x4 s1 = *reinterpret_cast<const f32x4*>(s + 2304 + 4 * hh);
      f32x4 s2 = *reinterpret_cast<const f32x4*>(s + 4608 + 4 * hh);
      f32x4 s3 = *reinterpret_cast<const f32x4*>(s + 6912 + 4 * hh);
      ys[hh] = (s0 + s1) + (s2 + s3);
    }
    unsigned short o[8];
#pragma unroll
    for (int k = 0; k < 8; ++k) {
      float yb = rbf(ys[k >> 2][k & 3]);
      float uu = bf2f((unsigned short)(uvec[k >> 1] >> (16 * (k & 1))));
      float sk = rbf(uu * dsk);
      o[k] = f2bf(yb + sk);
    }
    u32x4 v;
#pragma unroll
    for (int j = 0; j < 4; ++j) v[j] = o[2 * j] | ((unsigned)o[2 * j + 1] << 16);
    *reinterpret_cast<u32x4*>(aT + (size_t)d * L_ + 8 * tid) = v;
  }
}

__global__ __launch_bounds__(256, 3) void k12(const unsigned short* __restrict__ x, const unsigned short* __restrict__ w1,
                                              const unsigned short* __restrict__ w2, const unsigned short* __restrict__ gw,
                                              const unsigned short* __restrict__ logAr, const unsigned short* __restrict__ Aim,
                                              const unsigned short* __restrict__ Cre, const unsigned short* __restrict__ Cim,
                                              const unsigned short* __restrict__ logdt, const unsigned short* __restrict__ Dsk,
                                              unsigned short* __restrict__ hbuf, unsigned short* __restrict__ w2T,
                                              float* __restrict__ gbuf, unsigned short* __restrict__ aT) {
  extern __shared__ __attribute__((aligned(16))) char smem[];
  const int b = blockIdx.x;
  constexpr int G0 = 0;
  if (b >= G0 && b < G0 + D_) conv_role(b - G0, x, logAr, Aim, Cre, Cim, logdt, Dsk, aT, smem);
  else if (b < B_GATE) gemm1_role(b < G0 ? b : b - D_, x, w1, hbuf, smem);
  else if (b < B_W2) gate_role(b - B_GATE, x, gw, gbuf);
  else w2_role(b - B_W2, w2, w2T, smem);
}

constexpr int G2_RS = 84;
__global__ __launch_bounds__(256, 1) void k3_gemm2(const unsigned short* __restrict__ hbuf, const unsigned short* __restrict__ w2T,
                                                   const unsigned short* __restrict__ aT, const float* __restrict__ gbuf,
                                                   unsigned short* __restrict__ out) {
  __shared__ float red[4][64][G2_RS];
  const int tid = threadIdx.x, lane = tid & 63, wid = tid >> 6;
  int t = (blockIdx.x & 7) * 32 + (blockIdx.x >> 3);
  const int tm = t >> 3, tn = t & 7;
  const int m0 = tm * 64, n0 = tn * 80;
  const int fr = lane & 15, fq = lane >> 4;
  const int wv = __builtin_amdgcn_readfirstlane(wid);
  const int c0 = (wv * 27) >> 2;
  const int nch = (((wv + 1) * 27) >> 2) - c0;

  const unsigned short* ap = hbuf + (size_t)(m0 >> 4) * 54 * 512 + fr * 32 + 8 * fq + c0 * 1024;
  const unsigned short* bp = w2T + (size_t)(n0 >> 4) * 54 * 512 + fr * 32 + 8 * fq + c0 * 1024;

  f32x4 acc[4][5];
#pragma unroll
  for (int a = 0; a < 4; ++a)
#pragma unroll
    for (int c = 0; c < 5; ++c) acc[a][c] = (f32x4){0.f, 0.f, 0.f, 0.f};

  constexpr int PD = 2;
  bf16x8 A[PD][4][2], B[PD][5][2];
#define G2_LOAD(S, c)                                                                                  \
  {                                                                                                    \
    _Pragma("unroll") for (int a = 0; a < 4; ++a) {                                                    \
      A[S][a][0] = *reinterpret_cast<const bf16x8*>(ap + a * 54 * 512 + (c) * 1024);                   \
      A[S][a][1] = *reinterpret_cast<const bf16x8*>(ap + a * 54 * 512 + (c) * 1024 + 512);             \
    }                                                                                                  \
    _Pragma("unroll") for (int a = 0; a < 5; ++a) {                                                    \
      B[S][a][0] = *reinterpret_cast<const bf16x8*>(bp + a * 54 * 512 + (c) * 1024);                   \
      B[S][a][1] = *reinterpret_cast<const bf16x8*>(bp + a * 54 * 512 + (c) * 1024 + 512);             \
    }                                                                                                  \
  }
#define G2_COMP(S)                                                                                     \
  {                                                                                                    \
    _Pragma("unroll") for (int s = 0; s < 2; ++s)                                                      \
      _Pragma("unroll") for (int a = 0; a < 4; ++a)                                                    \
        _Pragma("unroll") for (int c = 0; c < 5; ++c)                                                  \
          acc[a][c] = __builtin_amdgcn_mfma_f32_16x16x32_bf16(A[S][a][s], B[S][c][s], acc[a][c], 0, 0, 0); \
  }
#pragma unroll
  for (int i = 0; i < PD - 1; ++i) {
    G2_LOAD(i, i);
    __builtin_amdgcn_sched_barrier(0);
  }
#pragma unroll
  for (int i = 0; i < 7; ++i) {
    if (i + PD - 1 < 7) {
      const int cn = (i + PD - 1 < 6) ? (i + PD - 1) : (nch - 1);
      G2_LOAD((i + PD - 1) % PD, cn);
    }
    __builtin_amdgcn_sched_barrier(0);
    if (i == 6 && nch != 7) {
      const bf16x8 z = {0, 0, 0, 0, 0, 0, 0, 0};
#pragma unroll
      for (int a = 0; a < 4; ++a) { A[i % PD][a][0] = z; A[i % PD][a][1] = z; }
#pragma unroll
      for (int a = 0; a < 5; ++a) { B[i % PD][a][0] = z; B[i % PD][a][1] = z; }
    }
    G2_COMP(i % PD);
    __builtin_amdgcn_sched_barrier(0);
  }
#undef G2_LOAD
#undef G2_COMP

#pragma unroll
  for (int a = 0; a < 4; ++a)
#pragma unroll
    for (int c = 0; c < 5; ++c)
#pragma unroll
      for (int i = 0; i < 4; ++i) red[wid][16 * a + 4 * fq + i][16 * c + fr] = acc[a][c][i];
  __syncthreads();

#pragma unroll
  for (int j = 0; j < 2; ++j) {
    const int col = lane + 64 * j;
    if (col < 80) {
      const int row0 = m0 + 16 * wid;
      u32x4 av0 = *reinterpret_cast<const u32x4*>(aT + (size_t)(n0 + col) * L_ + row0);
      u32x4 av1 = *reinterpret_cast<const u32x4*>(aT + (size_t)(n0 + col) * L_ + row0 + 8);
#pragma unroll
      for (int i = 0; i < 16; ++i) {
        int rr = 16 * wid + i;
        float bv = rbf((red[0][rr][col] + red[1][rr][col]) + (red[2][rr][col] + red[3][rr][col]));
        unsigned aw = (i < 8) ? av0[(i >> 1) & 3] : av1[(i >> 1) & 3];
        float a = bf2f((unsigned short)((aw >> (16 * (i & 1))) & 0xffff));
        float g = gbuf[row0 + i];
        float t1 = rbf(g * a);
        float t2 = rbf(1.0f - g);
        float t3 = rbf(t2 * bv);
        out[(size_t)(row0 + i) * D_ + n0 + col] = f2bf(t1 + t3);
      }
    }
  }
}

extern "C" void kernel_launch(void* const* d_in, const int* in_sizes, int n_in, void* d_out, int out_size, void* d_ws,
                              size_t ws_size, hipStream_t stream) {
  const unsigned short* u = (const unsigned short*)d_in[0];
  const unsigned short* logAr = (const unsigned short*)d_in[1];
  const unsigned short* Aim = (const unsigned short*)d_in[2];
  const unsigned short* Cre = (const unsigned short*)d_in[3];
  const unsigned short* Cim = (const unsigned short*)d_in[4];
  const unsigned short* logdt = (const unsigned short*)d_in[5];
  const unsigned short* Dsk = (const unsigned short*)d_in[6];
  const unsigned short* w1 = (const unsigned short*)d_in[8];
  const unsigned short* w2 = (const unsigned short*)d_in[9];
  const unsigned short* gw = (const unsigned short*)d_in[10];
  unsigned short* out = (unsigned short*)d_out;

  char* ws = (char*)d_ws;
  unsigned short* hbuf = (unsigned short*)ws;
  unsigned short* aT = (unsigned short*)(ws + (size_t)L_ * F_ * 2);
  unsigned short* w2T = (unsigned short*)(ws + (size_t)L_ * F_ * 2 + (size_t)D_ * L_ * 2);
  float* gbuf = (float*)(ws + (size_t)L_ * F_ * 2 + (size_t)D_ * L_ * 2 + (size_t)D_ * F_ * 2);

  constexpr int K12_LDS = (CONV_LDS > 3 * G1_STAGE) ? CONV_LDS : 3 * G1_STAGE;
  hipLaunchKernelGGL(k12, dim3(K12_GRID), dim3(256), K12_LDS, stream, u, w1, w2, gw, logAr, Aim, Cre, Cim, logdt, Dsk,
                     hbuf, w2T, gbuf, aT);
  hipLaunchKernelGGL(k3_gemm2, dim3(256), dim3(256), 0, stream, hbuf, w2T, aT, gbuf, out);
}
```

```cpp
#include <hip/hip_runtime.h>
#include <hip/hip_bf16.h>
#include <stdint.h>

typedef __attribute__((ext_vector_type(8))) short bf16x8;
typedef __attribute__((ext_vector_type(4))) short bf16x4;
typedef __attribute__((ext_vector_type(16))) float f32x16;
typedef __attribute__((ext_vector_type(4))) float f32x4;
typedef __attribute__((ext_vector_type(4))) unsigned int u32x4;
typedef __attribute__((ext_vector_type(2))) unsigned int u32x2;

#define LDS_AS __attribute__((address_space(3)))
#define RAW_BARRIER() do { asm volatile("s_waitcnt lgkmcnt(0)" ::: "memory"); __builtin_amdgcn_s_barrier(); } while (0)

constexpr int L_ = 2048, D_ = 640, NS_ = 128, F_ = 1728;

__device__ __forceinline__ float bf2f(unsigned short v) { return __uint_as_float(((unsigned)v) << 16); }
__device__ __forceinline__ unsigned short f2bf(float f) {
  __bf16 b = (__bf16)f;
  return __builtin_bit_cast(unsigned short, b);
}
__device__ __forceinline__ float rbf(float f) { return bf2f(f2bf(f)); }

constexpr int G1_TM = L_ / 128, G1_TN = (F_ + 127) / 128;
constexpr int N_G1 = G1_TM * G1_TN;
constexpr int G1_STAGE = 16384;
constexpr int N_GATE = 32, N_W2 = 90;
constexpr int B_CONV = N_G1, B_GATE = B_CONV + D_, B_W2 = B_GATE + N_GATE, K12_GRID = B_W2 + N_W2;

__device__ __forceinline__ void tr_tiles_load(const unsigned short* __restrict__ src, int ss, int r0, int c0,
                                              u32x4 (&v)[2]) {
  const int tid = threadIdx.x;
#pragma unroll
  for (int i = 0; i < 2; ++i) {
    int id = tid + 256 * i, row = id >> 3, ch = id & 7;
    v[i] = *reinterpret_cast<const u32x4*>(src + (size_t)(r0 + row) * ss + c0 + ch * 8);
  }
}
__device__ __forceinline__ void tr_tiles_put(unsigned short (*tile)[72], const u32x4 (&v)[2]) {
  const int tid = threadIdx.x;
#pragma unroll
  for (int i = 0; i < 2; ++i) {
    int id = tid + 256 * i, row = id >> 3, ch = id & 7;
    *reinterpret_cast<u32x4*>(&tile[row][ch * 8]) = v[i];
  }
}
__device__ __forceinline__ void tr_tiles_store_blocked(unsigned short (*tile)[72], unsigned short* __restrict__ dst,
                                                       int r0, int c0) {
  const int tid = threadIdx.x;
#pragma unroll
  for (int i = 0; i < 2; ++i) {
    int id = tid + 256 * i, col = id >> 3, ch = id & 7;
    unsigned short o[8];
#pragma unroll
    for (int k = 0; k < 8; ++k) o[k] = tile[ch * 8 + k][col];
    u32x4 w;
#pragma unroll
    for (int j = 0; j < 4; ++j) w[j] = o[2 * j] | ((unsigned)o[2 * j + 1] << 16);
    unsigned short* dp = dst + ((size_t)((c0 + col) >> 4) * 54 + ((r0 + ch * 8) >> 5)) * 512 + (col & 15) * 32 + (ch & 3) * 8;
    *reinterpret_cast<u32x4*>(dp) = w;
  }
}

__device__ void gate_role(int gb, const unsigned short* __restrict__ x, const unsigned short* __restrict__ gw,
                          float* __restrict__ gbuf) {
  const int tid = threadIdx.x;
  const int r0 = gb * 64;
  u32x4 v[10][2];
#pragma unroll
  for (int t = 0; t < 10; ++t) tr_tiles_load(x, D_, r0, t * 64, v[t]);
  float s[2] = {0.f, 0.f};
#pragma unroll
  for (int t = 0; t < 10; ++t) {
    u32x4 g = *reinterpret_cast<const u32x4*>(gw + t * 64 + (tid & 7) * 8);
#pragma unroll
    for (int i = 0; i < 2; ++i)
#pragma unroll
      for (int j = 0; j < 4; ++j) {
        s[i] += rbf(__uint_as_float(v[t][i][j] << 16) * __uint_as_float(g[j] << 16));
        s[i] += rbf(__uint_as_float(v[t][i][j] & 0xffff0000u) * __uint_as_float(g[j] & 0xffff0000u));
      }
  }
#pragma unroll
  for (int i = 0; i < 2; ++i) {
    s[i] += __shfl_xor(s[i], 1); s[i] += __shfl_xor(s[i], 2); s[i] += __shfl_xor(s[i], 4);
    if ((tid & 7) == 0) gbuf[r0 + (tid >> 3) + 32 * i] = rbf(1.0f / (1.0f + expf(-rbf(s[i]))));
  }
}

__device__ void w2_role(int wb, const unsigned short* __restrict__ w2, unsigned short* __restrict__ w2T, char* smem) {
  u32x4 tv[3][2];
#pragma unroll
  for (int j = 0; j < 3; ++j) {
    int id = wb * 3 + j;
    tr_tiles_load(w2, D_, (id / 10) * 64, (id % 10) * 64, tv[j]);
  }
  typedef unsigned short tile_t[64][72];
  tile_t* tiles = reinterpret_cast<tile_t*>(smem);
#pragma unroll
  for (int j = 0; j < 3; ++j) tr_tiles_put(tiles[j], tv[j]);
  __syncthreads();
#pragma unroll
  for (int j = 0; j < 3; ++j) {
    int id = wb * 3 + j;
    tr_tiles_store_blocked(tiles[j], w2T, (id / 10) * 64, (id % 10) * 64);
  }
}

__device__ void gemm1_role(int b, const unsigned short* __restrict__ x, const unsigned short* __restrict__ w1,
                           unsigned short* __restrict__ hbuf, char* smem) {
  const int tid = threadIdx.x, lane = tid & 63, wid = tid >> 6;
  __builtin_amdgcn_s_setprio(3);
  int t = (b & 7) * (N_G1 / 8) + (b >> 3);
  const int tm = t / G1_TN, tn = t % G1_TN;
  const int m0 = tm * 128, n0 = tn * 128;
  const int wm = wid >> 1, wn = wid & 1, r = lane & 31, h = lane >> 5;

  const unsigned short* asrc[2];
  const unsigned short* bsrc[2];
#pragma unroll
  for (int i = 0; i < 2; ++i) {
    int row = (i * 4 + wid) * 16 + (lane >> 2);
    int c = (lane & 3) ^ ((row >> 2) & 3);
    asrc[i] = x + (size_t)(m0 + row) * D_ + c * 8;
    int k = (i * 4 + wid) * 4 + (lane >> 4);
    int cb = (lane & 15) ^ ((k & 3) << 2);
    int col = n0 + cb * 8;
    if (col > F_ - 8) col = F_ - 8;
    bsrc[i] = w1 + (size_t)k * F_ + col;
  }
  auto stage = [&](int kt, int buf) {
    char* sa = smem + buf * G1_STAGE;
    char* sb = sa + 8192;
#pragma unroll
    for (int i = 0; i < 2; ++i)
      __builtin_amdgcn_global_load_lds((const unsigned*)(asrc[i] + kt * 32), (LDS_AS unsigned*)(sa + (i * 4 + wid) * 1024), 16, 0, 0);
#pragma unroll
    for (int i = 0; i < 2; ++i)
      __builtin_amdgcn_global_load_lds((const unsigned*)(bsrc[i] + (size_t)kt * 32 * F_), (LDS_AS unsigned*)(sb + (i * 4 + wid) * 1024), 16, 0, 0);
  };

  f32x16 acc[2][2];
#pragma unroll
  for (int a = 0; a < 2; ++a)
#pragma unroll
    for (int c = 0; c < 2; ++c)
#pragma unroll
      for (int i = 0; i < 16; ++i) acc[a][c][i] = 0.f;

  const int q = (lane & 15) >> 2, p = lane & 3, blk = (lane >> 4) & 1;
  auto compute = [&](int buf) {
    const char* sa = smem + buf * G1_STAGE;
    const char* sb = sa + 8192;
#pragma unroll
    for (int s = 0; s < 2; ++s) {
      bf16x8 af[2], bfr[2];
#pragma unroll
      for (int mt = 0; mt < 2; ++mt) {
        int row = wm * 64 + mt * 32 + r;
        af[mt] = *reinterpret_cast<const bf16x8*>(sa + row * 64 + (((2 * s + h) ^ ((row >> 2) & 3)) << 4));
      }
#pragma unroll
      for (int nt = 0; nt < 2; ++nt)
#pragma unroll
        for (int tt = 0; tt < 2; ++tt) {
          int k = 16 * s + 8 * h + 4 * tt + q;
          int chunk = wn * 8 + nt * 4 + 2 * blk + (p >> 1);
          const char* ad = sb + k * 256 + ((chunk ^ (q << 2)) << 4) + (p & 1) * 8;
          bf16x4 v = __builtin_amdgcn_ds_read_tr16_b64_v4i16((LDS_AS bf16x4*)(ad));
          bfr[nt][4 * tt + 0] = v[0]; bfr[nt][4 * tt + 1] = v[1]; bfr[nt][4 * tt + 2] = v[2]; bfr[nt][4 * tt + 3] = v[3];
        }
#pragma unroll
      for (int mt = 0; mt < 2; ++mt)
#pragma unroll
        for (int nt = 0; nt < 2; ++nt)
          acc[mt][nt] = __builtin_amdgcn_mfma_f32_32x32x16_bf16(af[mt], bfr[nt], acc[mt][nt], 0, 0, 0);
    }
  };

  constexpr int NK = D_ / 32;
  stage(0, 0);
  stage(1, 1);
  int cur = 0, nxt = 2;
  for (int kt = 0; kt < NK - 1; ++kt) {
    asm volatile("s_waitcnt vmcnt(4)" ::: "memory");
    RAW_BARRIER();
    if (kt + 2 < NK) stage(kt + 2, nxt);
    compute(cur);
    cur = (cur == 2) ? 0 : cur + 1;
    nxt = (nxt == 2) ? 0 : nxt + 1;
  }
  asm volatile("s_waitcnt vmcnt(0)" ::: "memory");
  RAW_BARRIER();
  compute(cur);

  RAW_BARRIER();
  {
    unsigned short* ht = reinterpret_cast<unsigned short*>(smem);
#pragma unroll
    for (int nt = 0; nt < 2; ++nt)
#pragma unroll
      for (int mt = 0; mt < 2; ++mt)
#pragma unroll
        for (int i = 0; i < 16; ++i) {
          int row = wm * 64 + mt * 32 + (i & 3) + 8 * (i >> 2) + 4 * h;
          float hv = rbf(acc[mt][nt][i]);
          float sv = hv * __builtin_amdgcn_rcpf(1.0f + __expf(-hv));
          ht[row * 136 + wn * 64 + nt * 32 + r] = f2bf(sv);
        }
    __syncthreads();
#pragma unroll
    for (int i = 0; i < 8; ++i) {
      int id = tid + 256 * i, row = id >> 4, ch = id & 15;
      if (n0 + ch * 8 < F_)
        *reinterpret_cast<u32x4*>(hbuf + ((size_t)((m0 + row) >> 4) * 54 + ((n0 + ch * 8) >> 5)) * 512 + (row & 15) * 32 + (ch & 3) * 8) = *reinterpret_cast<const u32x4*>(ht + row * 136 + ch * 8);
    }
  }
}

constexpr int UC_STRIDE = 4416;
constexpr int UC_OFF = 0;
constexpr int KROW = 80;
constexpr int KH_OFF = 8 * UC_STRIDE;
constexpr int KL_OFF = KH_OFF + 64 * KROW + 16;
constexpr int KZ_OFF = KH_OFF + 64 * KROW;
constexpr int PR_OFF = KH_OFF + 12288;
constexpr int OS_OFF = KH_OFF;
constexpr int TB_OFF = PR_OFF + 128 * 16;
constexpr int CONV_LDS = TB_OFF + 768 * 4;

__device__ __forceinline__ float thi_of(int i) {
  int t;
  if (i < 8) t = i;
  else if (i < 16) t = 128 + (i - 8);
  else if (i < 24) t = 256 + 128 * ((i - 16) >> 2) + 2 * ((i - 16) & 3);
  else if (i < 32) t = 512 + 128 * ((i - 24) >> 1) + 4 * ((i - 24) & 1);
  else if (i < 40) t = 1024 + 128 * (i - 32);
  else t = 2048;
  return (float)t;
}

struct KTab { unsigned short v[L_]; };
constexpr KTab make_ktab() {
  KTab t{};
  for (int j = 0; j < L_; ++j) {
    int sh = (j < 256) ? 0 : (j < 512) ? 1 : (j < 1024) ? 2 : 3;
    int rr = j;
    if (sh) {
      int q = j >> sh, rem = j & ((1 << sh) - 1), half = 1 << (sh - 1);
      if (rem > half || (rem == half && (q & 1))) ++q;
      rr = q << sh;
    }
    int i = 40;
    if (rr < 256) i = (rr & 7) + 8 * (rr >> 7);
    else if (rr < 512) i = 16 + 4 * ((rr - 256) >> 7) + ((rr & 7) >> 1);
    else if (rr < 1024) i = 24 + 2 * ((rr - 512) >> 7) + ((rr & 7) >> 2);
    else if (rr < 2048) i = 32 + ((rr - 1024) >> 7);
    t.v[j] = (unsigned short)(i * 16 + ((rr >> 3) & 15));
  }
  return t;
}
__device__ __attribute__((aligned(16))) const KTab KTAB = make_ktab();

__device__ void conv_role(int d, const unsigned short* __restrict__ x, const unsigned short* __restrict__ logAr,
                          const unsigned short* __restrict__ Aim, const unsigned short* __restrict__ Cre,
                          const unsigned short* __restrict__ Cim, const unsigned short* __restrict__ logdt,
                          const unsigned short* __restrict__ Dsk, unsigned short* __restrict__ aT, char* smem) {
  const int tid = threadIdx.x, lane = tid & 63, wid = tid >> 6;

  const int pn = tid & (NS_ - 1);
  const unsigned short r_ldt = logdt[d];
  const unsigned short r_lar = logAr[d * NS_ + pn], r_aim = Aim[d * NS_ + pn];
  const unsigned short r_cre = Cre[d * NS_ + pn], r_cim = Cim[d * NS_ + pn];
  const u32x4 kidx = *reinterpret_cast<const u32x4*>(KTAB.v + 8 * tid);
  __builtin_amdgcn_sched_barrier(0);
  unsigned short ug[8];
#pragma unroll
  for (int k = 0; k < 8; ++k) ug[k] = x[(size_t)(512 * wid + 64 * k + lane) * D_ + d];
  __builtin_amdgcn_sched_barrier(0);
  asm volatile("" :: "v"(r_ldt), "v"(r_lar), "v"(r_aim), "v"(r_cre), "v"(r_cim));

  const float dt = rbf(expf(bf2f(r_ldt)));
  if (tid < NS_) {
    float are = -rbf(expf(bf2f(r_lar)));
    float aim = bf2f(r_aim);
    float lr = are * dt, li = aim * dt;
    float cr = bf2f(r_cre) * dt, ci = bf2f(r_cim) * dt;
    float mag = sqrtf(cr * cr + ci * ci);
    f32x4 pp;
    pp[0] = lr * 1.4426950408889634f;
    pp[1] = li * 0.15915494309189535f;
    pp[2] = log2f(mag);
    pp[3] = atan2f(ci, cr) * 0.15915494309189535f;
    *reinterpret_cast<f32x4*>(smem + PR_OFF + tid * 16) = pp;
  }
  if (tid < 64) {
    u32x4 z = {0, 0, 0, 0};
    *reinterpret_cast<u32x4*>(smem + UC_OFF + (tid >> 3) * UC_STRIDE + (tid & 7) * 16) = z;
  }
  RAW_BARRIER();

  {
    const int x = lane & 15, kk = lane >> 4;
    float th[3];
#pragma unroll
    for (int T = 0; T < 3; ++T) th[T] = thi_of(16 * T + x);
    const float tlo = (float)(8 * x);
    f32x4 acc[3];
#pragma unroll
    for (int T = 0; T < 3; ++T) acc[T] = (f32x4){0.f, 0.f, 0.f, 0.f};
#pragma unroll 2
    for (int g = 0; g < 8; ++g) {
      int n = 32 * wid + 4 * g + kk;
      f32x4 pp = *reinterpret_cast<const f32x4*>(smem + PR_OFF + n * 16);
      float qe = __builtin_amdgcn_exp2f(pp[0] * tlo);
      float qa = pp[1] * tlo;
      float qr = qe * __builtin_amdgcn_cosf(qa), qi = qe * __builtin_amdgcn_sinf(qa);
#pragma unroll
      for (int T = 0; T < 3; ++T) {
        float pe = __builtin_amdgcn_exp2f(pp[0] * th[T] + pp[2]);
        float pa = pp[1] * th[T] + pp[3];
        float pr = pe * __builtin_amdgcn_cosf(pa), pi = pe * __builtin_amdgcn_sinf(pa);
        acc[T] = __builtin_amdgcn_mfma_f32_16x16x4f32(qr, pr, acc[T], 0, 0, 0);
        acc[T] = __builtin_amdgcn_mfma_f32_16x16x4f32(-qi, pi, acc[T], 0, 0, 0);
      }
    }
    float* os = reinterpret_cast<float*>(smem + OS_OFF) + wid * 768;
#pragma unroll
    for (int T = 0; T < 3; ++T) *reinterpret_cast<f32x4*>(os + (16 * T + x) * 16 + 4 * (kk ^ ((x >> 1) & 3))) = acc[T];
  }
  RAW_BARRIER();

#pragma unroll
  for (int k = 0; k < 8; ++k)
    *reinterpret_cast<unsigned short*>(smem + UC_OFF + 7 * UC_STRIDE + 112 + 2 * (512 * wid + 64 * k + lane)) = ug[k];
  {
    unsigned* tab = reinterpret_cast<unsigned*>(smem + TB_OFF);
#pragma unroll
    for (int k = 0; k < 3; ++k) {
      int e = tid + 256 * k;
      const float* osp = reinterpret_cast<const float*>(smem + OS_OFF) + (e ^ ((e >> 3) & 12));
      float kv = 2.0f * ((osp[0] + osp[768]) + (osp[1536] + osp[2304]));
      unsigned short hi = f2bf(kv);
      tab[e] = (unsigned)hi | ((unsigned)f2bf(kv - bf2f(hi)) << 16);
    }
  }
  __syncthreads();
  u32x4 uvec;
  {
    u32x4 v1 = {0, 0, 0, 0};
    uvec = *reinterpret_cast<const u32x4*>(smem + UC_OFF + 7 * UC_STRIDE + 112 + tid * 16);
    if (tid < 255) v1 = *reinterpret_cast<const u32x4*>(smem + UC_OFF + 7 * UC_STRIDE + 112 + (tid + 1) * 16);
    unsigned w[8] = {uvec[0], uvec[1], uvec[2], uvec[3], v1[0], v1[1], v1[2], v1[3]};
#pragma unroll
    for (int e = 1; e < 8; ++e) {
      u32x4 o;
#pragma unroll
      for (int j = 0; j < 4; ++j) {
        int base = j + (e >> 1);
        if (e & 1) o[j] = (w[base] >> 16) | (w[base + 1] << 16);
        else o[j] = w[base];
      }
      *reinterpret_cast<u32x4*>(smem + UC_OFF + (e - 1) * UC_STRIDE + 128 + tid * 16) = o;
    }
    if (tid == 0) {
#pragma unroll
      for (int e = 1; e < 8; ++e)
#pragma unroll
        for (int k = 0; k < e; ++k)
          *reinterpret_cast<unsigned short*>(smem + UC_OFF + (e - 1) * UC_STRIDE + 2 * (64 - e + k)) =
              (unsigned short)(w[k >> 1] >> (16 * (k & 1)));
    }
  }
  {
    const unsigned* tab = reinterpret_cast<const unsigned*>(smem + TB_OFF);
    unsigned w[8];
#pragma unroll
    for (int k = 0; k < 8; ++k) w[k] = tab[(kidx[k >> 1] >> (16 * (k & 1))) & 0xffff];
    u32x4 vh, vl;
#pragma unroll
    for (int j = 0; j < 4; ++j) {
      vh[j] = (w[7 - 2 * j] & 0xffffu) | (w[6 - 2 * j] << 16);
      vl[j] = (w[7 - 2 * j] >> 16) | (w[6 - 2 * j] & 0xffff0000u);
    }
    *reinterpret_cast<u32x4*>(smem + KH_OFF + (tid >> 2) * KROW + (tid & 3) * 16) = vh;
    *reinterpret_cast<u32x4*>(smem + KL_OFF + (tid >> 2) * KROW + (tid & 3) * 16) = vl;
    if (tid == 0) { u32x4 z = {0, 0, 0, 0}; *reinterpret_cast<u32x4*>(smem + KZ_OFF) = z; }
  }
  __syncthreads();

  f32x16 y0, y1;
#pragma unroll
  for (int i = 0; i < 16; ++i) { y0[i] = 0.f; y1[i] = 0.f; }
  {
    const int r = lane & 31, h = lane >> 5;
    const int e = r & 7;
    const char* ubase = smem + UC_OFF + e * UC_STRIDE + 2 * (r - e + 56 - 8 * h);
#pragma unroll 2
    for (int dl = wid; dl < 32; dl += 4) {
      bf16x8 a0 = *reinterpret_cast<const bf16x8*>(ubase + 64 * dl);
      bf16x8 a1 = *reinterpret_cast<const bf16x8*>(ubase + 64 * dl - 32);
      bf16x8 a2 = *reinterpret_cast<const bf16x8*>(ubase + 64 * dl + 2048);
      bf16x8 a3 = *reinterpret_cast<const bf16x8*>(ubase + 64 * dl + 2048 - 32);
      const int m = r - dl;
      const int off = (m >= 0) ? (m * KROW + 16 * h) : (KZ_OFF - KH_OFF);
      const int off1 = (m >= 0) ? (off + 32) : off;
      const int lo = (m >= 0) ? KL_OFF : KH_OFF;
      bf16x8 fh0 = *reinterpret_cast<const bf16x8*>(smem + KH_OFF + off);
      bf16x8 fh1 = *reinterpret_cast<const bf16x8*>(smem + KH_OFF + off1);
      bf16x8 fl0 = *reinterpret_cast<const bf16x8*>(smem + lo + off);
      bf16x8 fl1 = *reinterpret_cast<const bf16x8*>(smem + lo + off1);
      const int go = (32 + m) * KROW + 16 * h;
      bf16x8 gh0 = *reinterpret_cast<const bf16x8*>(smem + KH_OFF + go);
      bf16x8 gh1 = *reinterpret_cast<const bf16x8*>(smem + KH_OFF + go + 32);
      bf16x8 gl0 = *reinterpret_cast<const bf16x8*>(smem + KL_OFF + go);
      bf16x8 gl1 = *reinterpret_cast<const bf16x8*>(smem + KL_OFF + go + 32);
      y0 = __builtin_amdgcn_mfma_f32_32x32x16_bf16(a0, fh0, y0, 0, 0, 0);
      y1 = __builtin_amdgcn_mfma_f32_32x32x16_bf16(a2, fh0, y1, 0, 0, 0);
      y0 = __builtin_amdgcn_mfma_f32_32x32x16_bf16(a1, fh1, y0, 0, 0, 0);
      y1 = __builtin_amdgcn_mfma_f32_32x32x16_bf16(a3, fh1, y1, 0, 0, 0);
      y0 = __builtin_amdgcn_mfma_f32_32x32x16_bf16(a0, fl0, y0, 0, 0, 0);
      y1 = __builtin_amdgcn_mfma_f32_32x32x16_bf16(a2, fl0, y1, 0, 0, 0);
      y0 = __builtin_amdgcn_mfma_f32_32x32x16_bf16(a1, fl1, y0, 0, 0, 0);
      y1 = __builtin_amdgcn_mfma_f32_32x32x16_bf16(a3, fl1, y1, 0, 0, 0);
      y1 = __builtin_amdgcn_mfma_f32_32x32x16_bf16(a0, gh0, y1, 0, 0, 0);
      y1 = __builtin_amdgcn_mfma_f32_32x32x16_bf16(a1, gh1, y1, 0, 0, 0);
      y1 = __builtin_amdgcn_mfma_f32_32x32x16_bf16(a0, gl0, y1, 0, 0, 0);
      y1 = __builtin_amdgcn_mfma_f32_32x32x16_bf16(a1, gl1, y1, 0, 0, 0);
    }
  }
  __syncthreads();
  {
    const int r = lane & 31, h = lane >> 5;
    float* slab = reinterpret_cast<float*>(smem + wid * 9216);
#pragma unroll
    for (int g = 0; g < 4; ++g) {
      f32x4 v0 = {y0[4 * g], y0[4 * g + 1], y0[4 * g + 2], y0[4 * g + 3]};
      f32x4 v1 = {y1[4 * g], y1[4 * g + 1], y1[4 * g + 2], y1[4 * g + 3]};
      int b = 8 * g + 4 * h;
      *reinterpret_cast<f32x4*>(slab + 36 * r + b) = v0;
      *reinterpret_cast<f32x4*>(slab + 36 * (32 + r) + b) = v1;
    }
  }
  __syncthreads();
  {
    const float dsk = bf2f(Dsk[d]);
    const float* s = reinterpret_cast<const float*>(smem) + (tid >> 2) * 36 + (tid & 3) * 8;
    f32x4 ys[2];
#pragma unroll
    for (int hh = 0; hh < 2; ++hh) {
      f32x4 s0 = *reinterpret_cast<const f32x4*>(s + 4 * hh);
      f32x4 s1 = *reinterpret_cast<const f32x4*>(s + 2304 + 4 * hh);
      f32x4 s2 = *reinterpret_cast<const f32x4*>(s + 4608 + 4 * hh);
      f32x4 s3 = *reinterpret_cast<const f32x4*>(s + 6912 + 4 * hh);
      ys[hh] = (s0 + s1) + (s2 + s3);
    }
    unsigned short o[8];
#pragma unroll
    for (int k = 0; k < 8; ++k) {
      float yb = rbf(ys[k >> 2][k & 3]);
      float uu = bf2f((unsigned short)(uvec[k >> 1] >> (16 * (k & 1))));
      float sk = rbf(uu * dsk);
      o[k] = f2bf(yb + sk);
    }
    u32x4 v;
#pragma unroll
    for (int j = 0; j < 4; ++j) v[j] = o[2 * j] | ((unsigned)o[2 * j + 1] << 16);
    *reinterpret_cast<u32x4*>(aT + (size_t)d * L_ + 8 * tid) = v;
  }
}

__global__ __launch_bounds__(256, 3) void k12(const unsigned short* __restrict__ x, const unsigned short* __restrict__ w1,
                                              const unsigned short* __restrict__ w2, const unsigned short* __restrict__ gw,
                                              const unsigned short* __restrict__ logAr, const unsigned short* __restrict__ Aim,
                                              const unsigned short* __restrict__ Cre, const unsigned short* __restrict__ Cim,
                                              const unsigned short* __restrict__ logdt, const unsigned short* __restrict__ Dsk,
                                              unsigned short* __restrict__ hbuf, unsigned short* __restrict__ w2T,
                                              float* __restrict__ gbuf, unsigned short* __restrict__ aT) {
  extern __shared__ __attribute__((aligned(16))) char smem[];
  const int b = blockIdx.x;
  constexpr int G0 = 0;
  if (b >= G0 && b < G0 + D_) conv_role(b - G0, x, logAr, Aim, Cre, Cim, logdt, Dsk, aT, smem);
  else if (b < B_GATE) gemm1_role(b < G0 ? b : b - D_, x, w1, hbuf, smem);
  else if (b < B_W2) gate_role(b - B_GATE, x, gw, gbuf);
  else w2_role(b - B_W2, w2, w2T, smem);
}

constexpr int G2_RS = 84;
__global__ __launch_bounds__(256, 1) void k3_gemm2(const unsigned short* __restrict__ hbuf, const unsigned short* __restrict__ w2T,
                                                   const unsigned short* __restrict__ aT, const float* __restrict__ gbuf,
                                                   unsigned short* __restrict__ out) {
  __shared__ float red[4][64][G2_RS];
  const int tid = threadIdx.x, lane = tid & 63, wid = tid >> 6;
  int t = (blockIdx.x & 7) * 32 + (blockIdx.x >> 3);
  const int tm = t >> 3, tn = t & 7;
  const int m0 = tm * 64, n0 = tn * 80;
  const int fr = lane & 15, fq = lane >> 4;
  const int wv = __builtin_amdgcn_readfirstlane(wid);
  const int c0 = (wv * 27) >> 2;
  const int nch = (((wv + 1) * 27) >> 2) - c0;

  const unsigned short* ap = hbuf + (size_t)(m0 >> 4) * 54 * 512 + fr * 32 + 8 * fq + c0 * 1024;
  const unsigned short* bp = w2T + (size_t)(n0 >> 4) * 54 * 512 + fr * 32 + 8 * fq + c0 * 1024;

  f32x4 acc[4][5];
#pragma unroll
  for (int a = 0; a < 4; ++a)
#pragma unroll
    for (int c = 0; c < 5; ++c) acc[a][c] = (f32x4){0.f, 0.f, 0.f, 0.f};

  constexpr int PD = 2;
  bf16x8 A[PD][4][2], B[PD][5][2];
#define G2_LOAD(S, c)                                                                                  \
  {                                                                                                    \
    _Pragma("unroll") for (int a = 0; a < 4; ++a) {                                                    \
      A[S][a][0] = *reinterpret_cast<const bf16x8*>(ap + a * 54 * 512 + (c) * 1024);                   \
      A[S][a][1] = *reinterpret_cast<const bf16x8*>(ap + a * 54 * 512 + (c) * 1024 + 512);             \
    }                                                                                                  \
    _Pragma("unroll") for (int a = 0; a < 5; ++a) {                                                    \
      B[S][a][0] = *reinterpret_cast<const bf16x8*>(bp + a * 54 * 512 + (c) * 1024);                   \
      B[S][a][1] = *reinterpret_cast<const bf16x8*>(bp + a * 54 * 512 + (c) * 1024 + 512);             \
    }                                                                                                  \
  }
#define G2_COMP(S)                                                                                     \
  {                                                                                                    \
    _Pragma("unroll") for (int s = 0; s < 2; ++s)                                                      \
      _Pragma("unroll") for (int a = 0; a < 4; ++a)                                                    \
        _Pragma("unroll") for (int c = 0; c < 5; ++c)                                                  \
          acc[a][c] = __builtin_amdgcn_mfma_f32_16x16x32_bf16(A[S][a][s], B[S][c][s], acc[a][c], 0, 0, 0); \
  }
#pragma unroll
  for (int i = 0; i < PD - 1; ++i) {
    G2_LOAD(i, i);
    __builtin_amdgcn_sched_barrier(0);
  }
#pragma unroll
  for (int i = 0; i < 7; ++i) {
    if (i + PD - 1 < 7) {
      const int cn = (i + PD - 1 < 6) ? (i + PD - 1) : (nch - 1);
      G2_LOAD((i + PD - 1) % PD, cn);
    }
    __builtin_amdgcn_sched_barrier(0);
    if (i == 6 && nch != 7) {
      const bf16x8 z = {0, 0, 0, 0, 0, 0, 0, 0};
#pragma unroll
      for (int a = 0; a < 4; ++a) { A[i % PD][a][0] = z; A[i % PD][a][1] = z; }
#pragma unroll
      for (int a = 0; a < 5; ++a) { B[i % PD][a][0] = z; B[i % PD][a][1] = z; }
    }
    G2_COMP(i % PD);
    __builtin_amdgcn_sched_barrier(0);
  }
#undef G2_LOAD
#undef G2_COMP

#pragma unroll
  for (int a = 0; a < 4; ++a)
#pragma unroll
    for (int c = 0; c < 5; ++c)
#pragma unroll
      for (int i = 0; i < 4; ++i) red[wid][16 * a + 4 * fq + i][16 * c + fr] = acc[a][c][i];
  __syncthreads();

#pragma unroll
  for (int j = 0; j < 2; ++j) {
    const int col = lane + 64 * j;
    if (col < 80) {
      const int row0 = m0 + 16 * wid;
      u32x4 av0 = *reinterpret_cast<const u32x4*>(aT + (size_t)(n0 + col) * L_ + row0);
      u32x4 av1 = *reinterpret_cast<const u32x4*>(aT + (size_t)(n0 + col) * L_ + row0 + 8);
#pragma unroll
      for (int i = 0; i < 16; ++i) {
        int rr = 16 * wid + i;
        float bv = rbf((red[0][rr][col] + red[1][rr][col]) + (red[2][rr][col] + red[3][rr][col]));
        unsigned aw = (i < 8) ? av0[(i >> 1) & 3] : av1[(i >> 1) & 3];
        float a = bf2f((unsigned short)((aw >> (16 * (i & 1))) & 0xffff));
        float g = gbuf[row0 + i];
        float t1 = rbf(g * a);
        float t2 = rbf(1.0f - g);
        float t3 = rbf(t2 * bv);
        out[(size_t)(row0 + i) * D_ + n0 + col] = f2bf(t1 + t3);
      }
    }
  }
}

extern "C" void kernel_launch(void* const* d_in, const int* in_sizes, int n_in, void* d_out, int out_size, void* d_ws,
                              size_t ws_size, hipStream_t stream) {
  const unsigned short* u = (const unsigned short*)d_in[0];
  const unsigned short* logAr = (const unsigned short*)d_in[1];
  const unsigned short* Aim = (const unsigned short*)d_in[2];
  const unsigned short* Cre = (const unsigned short*)d_in[3];
  const unsigned short* Cim = (const unsigned short*)d_in[4];
  const unsigned short* logdt = (const unsigned short*)d_in[5];
  const unsigned short* Dsk = (const unsigned short*)d_in[6];
  const unsigned short* w1 = (const unsigned short*)d_in[8];
  const unsigned short* w2 = (const unsigned short*)d_in[9];
  const unsigned short* gw = (const unsigned short*)d_in[10];
  unsigned short* out = (unsigned short*)d_out;

  char* ws = (char*)d_ws;
  unsigned short* hbuf = (unsigned short*)ws;
  unsigned short* aT = (unsigned short*)(ws + (size_t)L_ * F_ * 2);
  unsigned short* w2T = (unsigned short*)(ws + (size_t)L_ * F_ * 2 + (size_t)D_ * L_ * 2);
  float* gbuf = (float*)(ws + (size_t)L_ * F_ * 2 + (size_t)D_ * L_ * 2 + (size_t)D_ * F_ * 2);

  constexpr int K12_LDS = (CONV_LDS > 3 * G1_STAGE) ? CONV_LDS : 3 * G1_STAGE;
  hipLaunchKernelGGL(k12, dim3(K12_GRID), dim3(256), K12_LDS, stream, u, w1, w2, gw, logAr, Aim, Cre, Cim, logdt, Dsk,
                     hbuf, w2T, gbuf, aT);
  hipLaunchKernelGGL(k3_gemm2, dim3(256), dim3(256), 0, stream, hbuf, w2T, aT, gbuf, out);
}
```

```cpp
#include <hip/hip_runtime.h>
#include <hip/hip_bf16.h>
#include <stdint.h>

typedef __attribute__((ext_vector_type(8))) short bf16x8;
typedef __attribute__((ext_vector_type(4))) short bf16x4;
typedef __attribute__((ext_vector_type(16))) float f32x16;
typedef __attribute__((ext_vector_type(4))) float f32x4;
typedef __attribute__((ext_vector_type(4))) unsigned int u32x4;
typedef __attribute__((ext_vector_type(2))) unsigned int u32x2;

#define LDS_AS __attribute__((address_space(3)))
#define RAW_BARRIER() do { asm volatile("s_waitcnt lgkmcnt(0)" ::: "memory"); __builtin_amdgcn_s_barrier(); } while (0)

constexpr int L_ = 2048, D_ = 640, NS_ = 128, F_ = 1728;

__device__ __forceinline__ float bf2f(unsigned short v) { return __uint_as_float(((unsigned)v) << 16); }
__device__ __forceinline__ unsigned short f2bf(float f) {
  __bf16 b = (__bf16)f;
  return __builtin_bit_cast(unsigned short, b);
}
__device__ __forceinline__ float rbf(float f) { return bf2f(f2bf(f)); }

constexpr int G1_TM = L_ / 128, G1_TN = (F_ + 127) / 128;
constexpr int N_G1 = G1_TM * G1_TN;
constexpr int G1_STAGE = 16384;
constexpr int N_GATE = 32, N_W2 = 90;
constexpr int B_CONV = N_G1, B_GATE = B_CONV + D_, B_W2 = B_GATE + N_GATE, K12_GRID = B_W2 + N_W2;

__device__ __forceinline__ void tr_tiles_load(const unsigned short* __restrict__ src, int ss, int r0, int c0,
                                              u32x4 (&v)[2]) {
  const int tid = threadIdx.x;
#pragma unroll
  for (int i = 0; i < 2; ++i) {
    int id = tid + 256 * i, row = id >> 3, ch = id & 7;
    v[i] = *reinterpret_cast<const u32x4*>(src + (size_t)(r0 + row) * ss + c0 + ch * 8);
  }
}
__device__ __forceinline__ void tr_tiles_put(unsigned short (*tile)[72], const u32x4 (&v)[2]) {
  const int tid = threadIdx.x;
#pragma unroll
  for (int i = 0; i < 2; ++i) {
    int id = tid + 256 * i, row = id >> 3, ch = id & 7;
    *reinterpret_cast<u32x4*>(&tile[row][ch * 8]) = v[i];
  }
}
__device__ __forceinline__ void tr_tiles_store_blocked(unsigned short (*tile)[72], unsigned short* __restrict__ dst,
                                                       int r0, int c0) {
  const int tid = threadIdx.x;
#pragma unroll
  for (int i = 0; i < 2; ++i) {
    int id = tid + 256 * i, col = ((id >> 7) << 4) | (id & 15), ch = (id >> 4) & 7;
    unsigned short o[8];
#pragma unroll
    for (int k = 0; k < 8; ++k) o[k] = tile[ch * 8 + k][col];
    u32x4 w;
#pragma unroll
    for (int j = 0; j < 4; ++j) w[j] = o[2 * j] | ((unsigned)o[2 * j + 1] << 16);
    unsigned short* dp = dst + ((size_t)((c0 + col) >> 4) * 54 + ((r0 + ch * 8) >> 5)) * 512 + (id & 63) * 8;
    *reinterpret_cast<u32x4*>(dp) = w;
  }
}

__device__ void gate_role(int gb, const unsigned short* __restrict__ x, const unsigned short* __restrict__ gw,
                          float* __restrict__ gbuf) {
  const int tid = threadIdx.x;
  const int r0 = gb * 64;
  u32x4 v[10][2];
#pragma unroll
  for (int t = 0; t < 10; ++t) tr_tiles_load(x, D_, r0, t * 64, v[t]);
  float s[2] = {0.f, 0.f};
#pragma unroll
  for (int t = 0; t < 10; ++t) {
    u32x4 g = *reinterpret_cast<const u32x4*>(gw + t * 64 + (tid & 7) * 8);
#pragma unroll
    for (int i = 0; i < 2; ++i)
#pragma unroll
      for (int j = 0; j < 4; ++j) {
        s[i] += rbf(__uint_as_float(v[t][i][j] << 16) * __uint_as_float(g[j] << 16));
        s[i] += rbf(__uint_as_float(v[t][i][j] & 0xffff0000u) * __uint_as_float(g[j] & 0xffff0000u));
      }
  }
#pragma unroll
  for (int i = 0; i < 2; ++i) {
    s[i] += __shfl_xor(s[i], 1); s[i] += __shfl_xor(s[i], 2); s[i] += __shfl_xor(s[i], 4);
    if ((tid & 7) == 0) gbuf[r0 + (tid >> 3) + 32 * i] = rbf(1.0f / (1.0f + expf(-rbf(s[i]))));
  }
}

__device__ void w2_role(int wb, const unsigned short* __restrict__ w2, unsigned short* __restrict__ w2T, char* smem) {
  u32x4 tv[3][2];
#pragma unroll
  for (int j = 0; j < 3; ++j) {
    int id = wb * 3 + j;
    tr_tiles_load(w2, D_, (id / 10) * 64, (id % 10) * 64, tv[j]);
  }
  typedef unsigned short tile_t[64][72];
  tile_t* tiles = reinterpret_cast<tile_t*>(smem);
#pragma unroll
  for (int j = 0; j < 3; ++j) tr_tiles_put(tiles[j], tv[j]);
  __syncthreads();
#pragma unroll
  for (int j = 0; j < 3; ++j) {
    int id = wb * 3 + j;
    tr_tiles_store_blocked(tiles[j], w2T, (id / 10) * 64, (id % 10) * 64);
  }
}

__device__ void gemm1_role(int b, const unsigned short* __restrict__ x, const unsigned short* __restrict__ w1,
                           unsigned short* __restrict__ hbuf, char* smem) {
  const int tid = threadIdx.x, lane = tid & 63, wid = tid >> 6;
  __builtin_amdgcn_s_setprio(3);
  int t = (b & 7) * (N_G1 / 8) + (b >> 3);
  const int tm = t / G1_TN, tn = t % G1_TN;
  const int m0 = tm * 128, n0 = tn * 128;
  const int wm = wid >> 1, wn = wid & 1, r = lane & 31, h = lane >> 5;

  const unsigned short* asrc[2];
  const unsigned short* bsrc[2];
#pragma unroll
  for (int i = 0; i < 2; ++i) {
    int row = (i * 4 + wid) * 16 + (lane >> 2);
    int c = (lane & 3) ^ ((row >> 2) & 3);
    asrc[i] = x + (size_t)(m0 + row) * D_ + c * 8;
    int k = (i * 4 + wid) * 4 + (lane >> 4);
    int cb = (lane & 15) ^ ((k & 3) << 2);
    int col = n0 + cb * 8;
    if (col > F_ - 8) col = F_ - 8;
    bsrc[i] = w1 + (size_t)k * F_ + col;
  }
  auto stage = [&](int kt, int buf) {
    char* sa = smem + buf * G1_STAGE;
    char* sb = sa + 8192;
#pragma unroll
    for (int i = 0; i < 2; ++i)
      __builtin_amdgcn_global_load_lds((const unsigned*)(asrc[i] + kt * 32), (LDS_AS unsigned*)(sa + (i * 4 + wid) * 1024), 16, 0, 0);
#pragma unroll
    for (int i = 0; i < 2; ++i)
      __builtin_amdgcn_global_load_lds((const unsigned*)(bsrc[i] + (size_t)kt * 32 * F_), (LDS_AS unsigned*)(sb + (i * 4 + wid) * 1024), 16, 0, 0);
  };

  f32x16 acc[2][2];
#pragma unroll
  for (int a = 0; a < 2; ++a)
#pragma unroll
    for (int c = 0; c < 2; ++c)
#pragma unroll
      for (int i = 0; i < 16; ++i) acc[a][c][i] = 0.f;

  const int q = (lane & 15) >> 2, p = lane & 3, blk = (lane >> 4) & 1;
  auto compute = [&](int buf) {
    const char* sa = smem + buf * G1_STAGE;
    const char* sb = sa + 8192;
#pragma unroll
    for (int s = 0; s < 2; ++s) {
      bf16x8 af[2], bfr[2];
#pragma unroll
      for (int mt = 0; mt < 2; ++mt) {
        int row = wm * 64 + mt * 32 + r;
        af[mt] = *reinterpret_cast<const bf16x8*>(sa + row * 64 + (((2 * s + h) ^ ((row >> 2) & 3)) << 4));
      }
#pragma unroll
      for (int nt = 0; nt < 2; ++nt)
#pragma unroll
        for (int tt = 0; tt < 2; ++tt) {
          int k = 16 * s + 8 * h + 4 * tt + q;
          int chunk = wn * 8 + nt * 4 + 2 * blk + (p >> 1);
          const char* ad = sb + k * 256 + ((chunk ^ (q << 2)) << 4) + (p & 1) * 8;
          bf16x4 v = __builtin_amdgcn_ds_read_tr16_b64_v4i16((LDS_AS bf16x4*)(ad));
          bfr[nt][4 * tt + 0] = v[0]; bfr[nt][4 * tt + 1] = v[1]; bfr[nt][4 * tt + 2] = v[2]; bfr[nt][4 * tt + 3] = v[3];
        }
#pragma unroll
      for (int mt = 0; mt < 2; ++mt)
#pragma unroll
        for (int nt = 0; nt < 2; ++nt)
          acc[mt][nt] = __builtin_amdgcn_mfma_f32_32x32x16_bf16(af[mt], bfr[nt], acc[mt][nt], 0, 0, 0);
    }
  };

  constexpr int NK = D_ / 32;
  stage(0, 0);
  stage(1, 1);
  int cur = 0, nxt = 2;
  for (int kt = 0; kt < NK - 1; ++kt) {
    asm volatile("s_waitcnt vmcnt(4)" ::: "memory");
    RAW_BARRIER();
    if (kt + 2 < NK) stage(kt + 2, nxt);
    compute(cur);
    cur = (cur == 2) ? 0 : cur + 1;
    nxt = (nxt == 2) ? 0 : nxt + 1;
  }
  asm volatile("s_waitcnt vmcnt(0)" ::: "memory");
  RAW_BARRIER();
  compute(cur);

  RAW_BARRIER();
  {
    unsigned short* ht = reinterpret_cast<unsigned short*>(smem);
#pragma unroll
    for (int nt = 0; nt < 2; ++nt)
#pragma unroll
      for (int mt = 0; mt < 2; ++mt)
#pragma unroll
        for (int i = 0; i < 16; ++i) {
          int row = wm * 64 + mt * 32 + (i & 3) + 8 * (i >> 2) + 4 * h;
          float hv = rbf(acc[mt][nt][i]);
          float sv = hv * __builtin_amdgcn_rcpf(1.0f + __expf(-hv));
          ht[row * 136 + wn * 64 + nt * 32 + r] = f2bf(sv);
        }
    __syncthreads();
#pragma unroll
    for (int i = 0; i < 8; ++i) {
      int id = tid + 256 * i, row = ((id >> 8) << 4) | (id & 15), ch = (id >> 4) & 15;
      if (n0 + ch * 8 < F_)
        *reinterpret_cast<u32x4*>(hbuf + ((size_t)((m0 + row) >> 4) * 54 + ((n0 + ch * 8) >> 5)) * 512 + (id & 63) * 8) = *reinterpret_cast<const u32x4*>(ht + row * 136 + ch * 8);
    }
  }
}

constexpr int UC_STRIDE = 4416;
constexpr int UC_OFF = 0;
constexpr int KROW = 80;
constexpr int KH_OFF = 8 * UC_STRIDE;
constexpr int KL_OFF = KH_OFF + 64 * KROW + 16;
constexpr int KZ_OFF = KH_OFF + 64 * KROW;
constexpr int PR_OFF = KH_OFF + 12288;
constexpr int OS_OFF = KH_OFF;
constexpr int TB_OFF = PR_OFF + 128 * 16;
constexpr int CONV_LDS = TB_OFF + 768 * 4;

__device__ __forceinline__ float thi_of(int i) {
  int t;
  if (i < 8) t = i;
  else if (i < 16) t = 128 + (i - 8);
  else if (i < 24) t = 256 + 128 * ((i - 16) >> 2) + 2 * ((i - 16) & 3);
  else if (i < 32) t = 512 + 128 * ((i - 24) >> 1) + 4 * ((i - 24) & 1);
  else if (i < 40) t = 1024 + 128 * (i - 32);
  else t = 2048;
  return (float)t;
}

struct KTab { unsigned short v[L_]; };
constexpr KTab make_ktab() {
  KTab t{};
  for (int j = 0; j < L_; ++j) {
    int sh = (j < 256) ? 0 : (j < 512) ? 1 : (j < 1024) ? 2 : 3;
    int rr = j;
    if (sh) {
      int q = j >> sh, rem = j & ((1 << sh) - 1), half = 1 << (sh - 1);
      if (rem > half || (rem == half && (q & 1))) ++q;
      rr = q << sh;
    }
    int i = 40;
    if (rr < 256) i = (rr & 7) + 8 * (rr >> 7);
    else if (rr < 512) i = 16 + 4 * ((rr - 256) >> 7) + ((rr & 7) >> 1);
    else if (rr < 1024) i = 24 + 2 * ((rr - 512) >> 7) + ((rr & 7) >> 2);
    else if (rr < 2048) i = 32 + ((rr - 1024) >> 7);
    t.v[j] = (unsigned short)(i * 16 + ((rr >> 3) & 15));
  }
  return t;
}
__device__ __attribute__((aligned(16))) const KTab KTAB = make_ktab();

__device__ void conv_role(int d, const unsigned short* __restrict__ x, const unsigned short* __restrict__ logAr,
                          const unsigned short* __restrict__ Aim, const unsigned short* __restrict__ Cre,
                          const unsigned short* __restrict__ Cim, const unsigned short* __restrict__ logdt,
                          const unsigned short* __restrict__ Dsk, unsigned short* __restrict__ aT, char* smem) {
  const int tid = threadIdx.x, lane = tid & 63, wid = tid >> 6;

  const int pn = tid & (NS_ - 1);
  const unsigned short r_ldt = logdt[d];
  const unsigned short r_lar = logAr[d * NS_ + pn], r_aim = Aim[d * NS_ + pn];
  const unsigned short r_cre = Cre[d * NS_ + pn], r_cim = Cim[d * NS_ + pn];
  const u32x4 kidx = *reinterpret_cast<const u32x4*>(KTAB.v + 8 * tid);
  __builtin_amdgcn_sched_barrier(0);
  unsigned short ug[8];
#pragma unroll
  for (int k = 0; k < 8; ++k) ug[k] = x[(size_t)(8 * tid + k) * D_ + d];
  __builtin_amdgcn_sched_barrier(0);
  asm volatile("" :: "v"(r_ldt), "v"(r_lar), "v"(r_aim), "v"(r_cre), "v"(r_cim));

  const float dt = rbf(expf(bf2f(r_ldt)));
  if (tid < NS_) {
    float are = -rbf(expf(bf2f(r_lar)));
    float aim = bf2f(r_aim);
    float lr = are * dt, li = aim * dt;
    float cr = bf2f(r_cre) * dt, ci = bf2f(r_cim) * dt;
    float mag = sqrtf(cr * cr + ci * ci);
    f32x4 pp;
    pp[0] = lr * 1.4426950408889634f;
    pp[1] = li * 0.15915494309189535f;
    pp[2] = log2f(mag);
    pp[3] = atan2f(ci, cr) * 0.15915494309189535f;
    *reinterpret_cast<f32x4*>(smem + PR_OFF + tid * 16) = pp;
  }
  if (tid < 64) {
    u32x4 z = {0, 0, 0, 0};
    *reinterpret_cast<u32x4*>(smem + UC_OFF + (tid >> 3) * UC_STRIDE + (tid & 7) * 16) = z;
  }
  RAW_BARRIER();

  {
    const int x = lane & 15, kk = lane >> 4;
    float th[3];
#pragma unroll
    for (int T = 0; T < 3; ++T) th[T] = thi_of(16 * T + x);
    const float tlo = (float)(8 * x);
    f32x4 acc[3];
#pragma unroll
    for (int T = 0; T < 3; ++T) acc[T] = (f32x4){0.f, 0.f, 0.f, 0.f};
#pragma unroll 2
    for (int g = 0; g < 8; ++g) {
      int n = 32 * wid + 4 * g + kk;
      f32x4 pp = *reinterpret_cast<const f32x4*>(smem + PR_OFF + n * 16);
      float qe = __builtin_amdgcn_exp2f(pp[0] * tlo);
      float qa = pp[1] * tlo;
      float qr = qe * __builtin_amdgcn_cosf(qa), qi = qe * __builtin_amdgcn_sinf(qa);
#pragma unroll
      for (int T = 0; T < 3; ++T) {
        float pe = __builtin_amdgcn_exp2f(pp[0] * th[T] + pp[2]);
        float pa = pp[1] * th[T] + pp[3];
        float pr = pe * __builtin_amdgcn_cosf(pa), pi = pe * __builtin_amdgcn_sinf(pa);
        acc[T] = __builtin_amdgcn_mfma_f32_16x16x4f32(qr, pr, acc[T], 0, 0, 0);
        acc[T] = __builtin_amdgcn_mfma_f32_16x16x4f32(-qi, pi, acc[T], 0, 0, 0);
      }
    }
    float* os = reinterpret_cast<float*>(smem + OS_OFF) + wid * 768;
#pragma unroll
    for (int T = 0; T < 3; ++T) *reinterpret_cast<f32x4*>(os + (16 * T + x) * 16 + 4 * (kk ^ ((x >> 1) & 3))) = acc[T];
  }
  RAW_BARRIER();

  u32x4 uvec;
#pragma unroll
  for (int j = 0; j < 4; ++j) uvec[j] = (unsigned)ug[2 * j] | ((unsigned)ug[2 * j + 1] << 16);
  *reinterpret_cast<u32x4*>(smem + UC_OFF + 7 * UC_STRIDE + 112 + tid * 16) = uvec;
  {
    unsigned* tab = reinterpret_cast<unsigned*>(smem + TB_OFF);
#pragma unroll
    for (int k = 0; k < 3; ++k) {
      int e = tid + 256 * k;
      const float* osp = reinterpret_cast<const float*>(smem + OS_OFF) + (e ^ ((e >> 3) & 12));
      float kv = 2.0f * ((osp[0] + osp[768]) + (osp[1536] + osp[2304]));
      unsigned short hi = f2bf(kv);
      tab[e] = (unsigned)hi | ((unsigned)f2bf(kv - bf2f(hi)) << 16);
    }
  }
  __syncthreads();
  {
    u32x4 v1 = {0, 0, 0, 0};
    if (tid < 255) v1 = *reinterpret_cast<const u32x4*>(smem + UC_OFF + 7 * UC_STRIDE + 112 + (tid + 1) * 16);
    unsigned w[8] = {uvec[0], uvec[1], uvec[2], uvec[3], v1[0], v1[1], v1[2], v1[3]};
#pragma unroll
    for (int e = 1; e < 8; ++e) {
      u32x4 o;
#pragma unroll
      for (int j = 0; j < 4; ++j) {
        int base = j + (e >> 1);
        if (e & 1) o[j] = (w[base] >> 16) | (w[base + 1] << 16);
        else o[j] = w[base];
      }
      *reinterpret_cast<u32x4*>(smem + UC_OFF + (e - 1) * UC_STRIDE + 128 + tid * 16) = o;
    }
    if (tid == 0) {
#pragma unroll
      for (int e = 1; e < 8; ++e)
#pragma unroll
        for (int k = 0; k < e; ++k)
          *reinterpret_cast<unsigned short*>(smem + UC_OFF + (e - 1) * UC_STRIDE + 2 * (64 - e + k)) =
              (unsigned short)(w[k >> 1] >> (16 * (k & 1)));
    }
  }
  {
    const unsigned* tab = reinterpret_cast<const unsigned*>(smem + TB_OFF);
    unsigned w[8];
#pragma unroll
    for (int k = 0; k < 8; ++k) w[k] = tab[(kidx[k >> 1] >> (16 * (k & 1))) & 0xffff];
    u32x4 vh, vl;
#pragma unroll
    for (int j = 0; j < 4; ++j) {
      vh[j] = (w[7 - 2 * j] & 0xffffu) | (w[6 - 2 * j] << 16);
      vl[j] = (w[7 - 2 * j] >> 16) | (w[6 - 2 * j] & 0xffff0000u);
    }
    *reinterpret_cast<u32x4*>(smem + KH_OFF + (tid >> 2) * KROW + (tid & 3) * 16) = vh;
    *reinterpret_cast<u32x4*>(smem + KL_OFF + (tid >> 2) * KROW + (tid & 3) * 16) = vl;
    if (tid == 0) { u32x4 z = {0, 0, 0, 0}; *reinterpret_cast<u32x4*>(smem + KZ_OFF) = z; }
  }
  __syncthreads();

  f32x16 y0, y1;
#pragma unroll
  for (int i = 0; i < 16; ++i) { y0[i] = 0.f; y1[i] = 0.f; }
  {
    const int r = lane & 31, h = lane >> 5;
    const int e = r & 7;
    const char* ubase = smem + UC_OFF + e * UC_STRIDE + 2 * (r - e + 56 - 8 * h);
#pragma unroll 2
    for (int dl = wid; dl < 32; dl += 4) {
      bf16x8 a0 = *reinterpret_cast<const bf16x8*>(ubase + 64 * dl);
      bf16x8 a1 = *reinterpret_cast<const bf16x8*>(ubase + 64 * dl - 32);
      bf16x8 a2 = *reinterpret_cast<const bf16x8*>(ubase + 64 * dl + 2048);
      bf16x8 a3 = *reinterpret_cast<const bf16x8*>(ubase + 64 * dl + 2048 - 32);
      const int m = r - dl;
      const int off = (m >= 0) ? (m * KROW + 16 * h) : (KZ_OFF - KH_OFF);
      const int off1 = (m >= 0) ? (off + 32) : off;
      const int lo = (m >= 0) ? KL_OFF : KH_OFF;
      bf16x8 fh0 = *reinterpret_cast<const bf16x8*>(smem + KH_OFF + off);
      bf16x8 fh1 = *reinterpret_cast<const bf16x8*>(smem + KH_OFF + off1);
      bf16x8 fl0 = *reinterpret_cast<const bf16x8*>(smem + lo + off);
      bf16x8 fl1 = *reinterpret_cast<const bf16x8*>(smem + lo + off1);
      const int go = (32 + m) * KROW + 16 * h;
      bf16x8 gh0 = *reinterpret_cast<const bf16x8*>(smem + KH_OFF + go);
      bf16x8 gh1 = *reinterpret_cast<const bf16x8*>(smem + KH_OFF + go + 32);
      bf16x8 gl0 = *reinterpret_cast<const bf16x8*>(smem + KL_OFF + go);
      bf16x8 gl1 = *reinterpret_cast<const bf16x8*>(smem + KL_OFF + go + 32);
      y0 = __builtin_amdgcn_mfma_f32_32x32x16_bf16(a0, fh0, y0, 0, 0, 0);
      y1 = __builtin_amdgcn_mfma_f32_32x32x16_bf16(a2, fh0, y1, 0, 0, 0);
      y0 = __builtin_amdgcn_mfma_f32_32x32x16_bf16(a1, fh1, y0, 0, 0, 0);
      y1 = __builtin_amdgcn_mfma_f32_32x32x16_bf16(a3, fh1, y1, 0, 0, 0);
      y0 = __builtin_amdgcn_mfma_f32_32x32x16_bf16(a0, fl0, y0, 0, 0, 0);
      y1 = __builtin_amdgcn_mfma_f32_32x32x16_bf16(a2, fl0, y1, 0, 0, 0);
      y0 = __builtin_amdgcn_mfma_f32_32x32x16_bf16(a1, fl1, y0, 0, 0, 0);
      y1 = __builtin_amdgcn_mfma_f32_32x32x16_bf16(a3, fl1, y1, 0, 0, 0);
      y1 = __builtin_amdgcn_mfma_f32_32x32x16_bf16(a0, gh0, y1, 0, 0, 0);
      y1 = __builtin_amdgcn_mfma_f32_32x32x16_bf16(a1, gh1, y1, 0, 0, 0);
      y1 = __builtin_amdgcn_mfma_f32_32x32x16_bf16(a0, gl0, y1, 0, 0, 0);
      y1 = __builtin_amdgcn_mfma_f32_32x32x16_bf16(a1, gl1, y1, 0, 0, 0);
    }
  }
  __syncthreads();
  {
    const int r = lane & 31, h = lane >> 5;
    float* slab = reinterpret_cast<float*>(smem + wid * 9216);
#pragma unroll
    for (int g = 0; g < 4; ++g) {
      f32x4 v0 = {y0[4 * g], y0[4 * g + 1], y0[4 * g + 2], y0[4 * g + 3]};
      f32x4 v1 = {y1[4 * g], y1[4 * g + 1], y1[4 * g + 2], y1[4 * g + 3]};
      int b = 8 * g + 4 * h;
      *reinterpret_cast<f32x4*>(slab + 36 * r + b) = v0;
      *reinterpret_cast<f32x4*>(slab + 36 * (32 + r) + b) = v1;
    }
  }
  __syncthreads();
  {
    const float dsk = bf2f(Dsk[d]);
    const float* s = reinterpret_cast<const float*>(smem) + (tid >> 2) * 36 + (tid & 3) * 8;
    f32x4 ys[2];
#pragma unroll
    for (int hh = 0; hh < 2; ++hh) {
      f32x4 s0 = *reinterpret_cast<const f32x4*>(s + 4 * hh);
      f32x4 s1 = *reinterpret_cast<const f32x4*>(s + 2304 + 4 * hh);
      f32x4 s2 = *reinterpret_cast<const f32x4*>(s + 4608 + 4 * hh);
      f32x4 s3 = *reinterpret_cast<const f32x4*>(s + 6912 + 4 * hh);
      ys[hh] = (s0 + s1) + (s2 + s3);
    }
    unsigned short o[8];
#pragma unroll
    for (int k = 0; k < 8; ++k) {
      float yb = rbf(ys[k >> 2][k & 3]);
      float uu = bf2f((unsigned short)(uvec[k >> 1] >> (16 * (k & 1))));
      float sk = rbf(uu * dsk);
      o[k] = f2bf(yb + sk);
    }
    u32x4 v;
#pragma unroll
    for (int j = 0; j < 4; ++j) v[j] = o[2 * j] | ((unsigned)o[2 * j + 1] << 16);
    *reinterpret_cast<u32x4*>(aT + (size_t)d * L_ + 8 * tid) = v;
  }
}

__global__ __launch_bounds__(256, 3) void k12(const unsigned short* __restrict__ x, const unsigned short* __restrict__ w1,
                                              const unsigned short* __restrict__ w2, const unsigned short* __restrict__ gw,
                                              const unsigned short* __restrict__ logAr, const unsigned short* __restrict__ Aim,
                                              const unsigned short* __restrict__ Cre, const unsigned short* __restrict__ Cim,
                                              const unsigned short* __restrict__ logdt, const unsigned short* __restrict__ Dsk,
                                              unsigned short* __restrict__ hbuf, unsigned short* __restrict__ w2T,
                                              float* __restrict__ gbuf, unsigned short* __restrict__ aT) {
  extern __shared__ __attribute__((aligned(16))) char smem[];
  const int b = blockIdx.x;
  constexpr int G0 = 0;
  if (b >= G0 && b < G0 + D_) conv_role(b - G0, x, logAr, Aim, Cre, Cim, logdt, Dsk, aT, smem);
  else if (b < B_GATE) gemm1_role(b < G0 ? b : b - D_, x, w1, hbuf, smem);
  else if (b < B_W2) gate_role(b - B_GATE, x, gw, gbuf);
  else w2_role(b - B_W2, w2, w2T, smem);
}

constexpr int G2_RS = 84;
__global__ __launch_bounds__(256, 1) void k3_gemm2(const unsigned short* __restrict__ hbuf, const unsigned short* __restrict__ w2T,
                                                   const unsigned short* __restrict__ aT, const float* __restrict__ gbuf,
                                                   unsigned short* __restrict__ out) {
  __shared__ float red[4][64][G2_RS];
  const int tid = threadIdx.x, lane = tid & 63, wid = tid >> 6;
  int t = (blockIdx.x & 7) * 32 + (blockIdx.x >> 3);
  const int tm = t >> 3, tn = t & 7;
  const int m0 = tm * 64, n0 = tn * 80;
  const int fr = lane & 15, fq = lane >> 4;
  const int wv = __builtin_amdgcn_readfirstlane(wid);
  const int c0 = (wv * 27) >> 2;
  const int nch = (((wv + 1) * 27) >> 2) - c0;

  const unsigned short* ap = hbuf + (size_t)(m0 >> 4) * 54 * 512 + lane * 8 + c0 * 1024;
  const unsigned short* bp = w2T + (size_t)(n0 >> 4) * 54 * 512 + lane * 8 + c0 * 1024;

  f32x4 acc[4][5];
#pragma unroll
  for (int a = 0; a < 4; ++a)
#pragma unroll
    for (int c = 0; c < 5; ++c) acc[a][c] = (f32x4){0.f, 0.f, 0.f, 0.f};

  constexpr int PD = 2;
  bf16x8 A[PD][4][2], B[PD][5][2];
#define G2_LOAD(S, c)                                                                                  \
  {                                                                                                    \
    _Pragma("unroll") for (int a = 0; a < 4; ++a) {                                                    \
      A[S][a][0] = *reinterpret_cast<const bf16x8*>(ap + a * 54 * 512 + (c) * 1024);                   \
      A[S][a][1] = *reinterpret_cast<const bf16x8*>(ap + a * 54 * 512 + (c) * 1024 + 512);             \
    }                                                                                                  \
    _Pragma("unroll") for (int a = 0; a < 5; ++a) {                                                    \
      B[S][a][0] = *reinterpret_cast<const bf16x8*>(bp + a * 54 * 512 + (c) * 1024);                   \
      B[S][a][1] = *reinterpret_cast<const bf16x8*>(bp + a * 54 * 512 + (c) * 1024 + 512);             \
    }                                                                                                  \
  }
#define G2_COMP(S)                                                                                     \
  {                                                                                                    \
    _Pragma("unroll") for (int s = 0; s < 2; ++s)                                                      \
      _Pragma("unroll") for (int a = 0; a < 4; ++a)                                                    \
        _Pragma("unroll") for (int c = 0; c < 5; ++c)                                                  \
          acc[a][c] = __builtin_amdgcn_mfma_f32_16x16x32_bf16(A[S][a][s], B[S][c][s], acc[a][c], 0, 0, 0); \
  }
#pragma unroll
  for (int i = 0; i < PD - 1; ++i) {
    G2_LOAD(i, i);
    __builtin_amdgcn_sched_barrier(0);
  }
#pragma unroll
  for (int i = 0; i < 7; ++i) {
    if (i + PD - 1 < 7) {
      const int cn = (i + PD - 1 < 6) ? (i + PD - 1) : (nch - 1);
      G2_LOAD((i + PD - 1) % PD, cn);
    }
    __builtin_amdgcn_sched_barrier(0);
    if (i == 6 && nch != 7) {
      const bf16x8 z = {0, 0, 0, 0, 0, 0, 0, 0};
#pragma unroll
      for (int a = 0; a < 4; ++a) { A[i % PD][a][0] = z; A[i % PD][a][1] = z; }
#pragma unroll
      for (int a = 0; a < 5; ++a) { B[i % PD][a][0] = z; B[i % PD][a][1] = z; }
    }
    G2_COMP(i % PD);
    __builtin_amdgcn_sched_barrier(0);
  }
#undef G2_LOAD
#undef G2_COMP

#pragma unroll
  for (int a = 0; a < 4; ++a)
#pragma unroll
    for (int c = 0; c < 5; ++c)
#pragma unroll
      for (int i = 0; i < 4; ++i) red[wid][16 * a + 4 * fq + i][16 * c + fr] = acc[a][c][i];
  __syncthreads();

#pragma unroll
  for (int j = 0; j < 2; ++j) {
    const int col = lane + 64 * j;
    if (col < 80) {
      const int row0 = m0 + 16 * wid;
      u32x4 av0 = *reinterpret_cast<const u32x4*>(aT + (size_t)(n0 + col) * L_ + row0);
      u32x4 av1 = *reinterpret_cast<const u32x4*>(aT + (size_t)(n0 + col) * L_ + row0 + 8);
#pragma unroll
      for (int i = 0; i < 16; ++i) {
        int rr = 16 * wid + i;
        float bv = rbf((red[0][rr][col] + red[1][rr][col]) + (red[2][rr][col] + red[3][rr][col]));
        unsigned aw = (i < 8) ? av0[(i >> 1) & 3] : av1[(i >> 1) & 3];
        float a = bf2f((unsigned short)((aw >> (16 * (i & 1))) & 0xffff));
        float g = gbuf[row0 + i];
        float t1 = rbf(g * a);
        float t2 = rbf(1.0f - g);
        float t3 = rbf(t2 * bv);
        out[(size_t)(row0 + i) * D_ + n0 + col] = f2bf(t1 + t3);
      }
    }
  }
}

extern "C" void kernel_launch(void* const* d_in, const int* in_sizes, int n_in, void* d_out, int out_size, void* d_ws,
                              size_t ws_size, hipStream_t stream) {
  const unsigned short* u = (const unsigned short*)d_in[0];
  const unsigned short* logAr = (const unsigned short*)d_in[1];
  const unsigned short* Aim = (const unsigned short*)d_in[2];
  const unsigned short* Cre = (const unsigned short*)d_in[3];
  const unsigned short* Cim = (const unsigned short*)d_in[4];
  const unsigned short* logdt = (const unsigned short*)d_in[5];
  const unsigned short* Dsk = (const unsigned short*)d_in[6];
  const unsigned short* w1 = (const unsigned short*)d_in[8];
  const unsigned short* w2 = (const unsigned short*)d_in[9];
  const unsigned short* gw = (const unsigned short*)d_in[10];
  unsigned short* out = (unsigned short*)d_out;

  char* ws = (char*)d_ws;
  unsigned short* hbuf = (unsigned short*)ws;
  unsigned short* aT = (unsigned short*)(ws + (size_t)L_ * F_ * 2);
  unsigned short* w2T = (unsigned short*)(ws + (size_t)L_ * F_ * 2 + (size_t)D_ * L_ * 2);
  float* gbuf = (float*)(ws + (size_t)L_ * F_ * 2 + (size_t)D_ * L_ * 2 + (size_t)D_ * F_ * 2);

  constexpr int K12_LDS = (CONV_LDS > 3 * G1_STAGE) ? CONV_LDS : 3 * G1_STAGE;
  hipLaunchKernelGGL(k12, dim3(K12_GRID), dim3(256), K12_LDS, stream, u, w1, w2, gw, logAr, Aim, Cre, Cim, logdt, Dsk,
                     hbuf, w2T, gbuf, aT);
  hipLaunchKernelGGL(k3_gemm2, dim3(256), dim3(256), 0, stream, hbuf, w2T, aT, gbuf, out);
}
```

```cpp
#include <hip/hip_runtime.h>
#include <hip/hip_bf16.h>
#include <stdint.h>

typedef __attribute__((ext_vector_type(8))) short bf16x8;
typedef __attribute__((ext_vector_type(4))) short bf16x4;
typedef __attribute__((ext_vector_type(16))) float f32x16;
typedef __attribute__((ext_vector_type(4))) float f32x4;
typedef __attribute__((ext_vector_type(4))) unsigned int u32x4;
typedef __attribute__((ext_vector_type(2))) unsigned int u32x2;

#define LDS_AS __attribute__((address_space(3)))
#define RAW_BARRIER() do { asm volatile("s_waitcnt lgkmcnt(0)" ::: "memory"); __builtin_amdgcn_s_barrier(); } while (0)

constexpr int L_ = 2048, D_ = 640, NS_ = 128, F_ = 1728;

__device__ __forceinline__ float bf2f(unsigned short v) { return __uint_as_float(((unsigned)v) << 16); }
__device__ __forceinline__ unsigned short f2bf(float f) {
  __bf16 b = (__bf16)f;
  return __builtin_bit_cast(unsigned short, b);
}
__device__ __forceinline__ float rbf(float f) { return bf2f(f2bf(f)); }

constexpr int G1_TM = L_ / 128, G1_TN = (F_ + 127) / 128;
constexpr int N_G1 = G1_TM * G1_TN;
constexpr int G1_STAGE = 16384;
constexpr int N_GATE = 32, N_W2 = 90;
constexpr int B_CONV = N_G1, B_GATE = B_CONV + D_, B_W2 = B_GATE + N_GATE, K12_GRID = B_W2 + N_W2;

__device__ __forceinline__ void tr_tiles_load(const unsigned short* __restrict__ src, int ss, int r0, int c0,
                                              u32x4 (&v)[2]) {
  const int tid = threadIdx.x;
#pragma unroll
  for (int i = 0; i < 2; ++i) {
    int id = tid + 256 * i, row = id >> 3, ch = id & 7;
    v[i] = *reinterpret_cast<const u32x4*>(src + (size_t)(r0 + row) * ss + c0 + ch * 8);
  }
}
__device__ __forceinline__ void tr_tiles_put(unsigned short (*tile)[72], const u32x4 (&v)[2]) {
  const int tid = threadIdx.x;
#pragma unroll
  for (int i = 0; i < 2; ++i) {
    int id = tid + 256 * i, row = id >> 3, ch = id & 7;
    *reinterpret_cast<u32x4*>(&tile[row][ch * 8]) = v[i];
  }
}
__device__ __forceinline__ void tr_tiles_store_blocked(unsigned short (*tile)[72], unsigned short* __restrict__ dst,
                                                       int r0, int c0) {
  const int tid = threadIdx.x;
#pragma unroll
  for (int i = 0; i < 2; ++i) {
    int id = tid + 256 * i, col = ((id >> 7) << 4) | (id & 15), ch = (id >> 4) & 7;
    unsigned short o[8];
#pragma unroll
    for (int k = 0; k < 8; ++k) o[k] = tile[ch * 8 + k][col];
    u32x4 w;
#pragma unroll
    for (int j = 0; j < 4; ++j) w[j] = o[2 * j] | ((unsigned)o[2 * j + 1] << 16);
    unsigned short* dp = dst + ((size_t)((c0 + col) >> 4) * 54 + ((r0 + ch * 8) >> 5)) * 512 + (id & 63) * 8;
    *reinterpret_cast<u32x4*>(dp) = w;
  }
}

__device__ void gate_role(int gb, const unsigned short* __restrict__ x, const unsigned short* __restrict__ gw,
                          float* __restrict__ gbuf) {
  const int tid = threadIdx.x;
  const int r0 = gb * 64;
  u32x4 v[10][2];
#pragma unroll
  for (int t = 0; t < 10; ++t) tr_tiles_load(x, D_, r0, t * 64, v[t]);
  float s[2] = {0.f, 0.f};
#pragma unroll
  for (int t = 0; t < 10; ++t) {
    u32x4 g = *reinterpret_cast<const u32x4*>(gw + t * 64 + (tid & 7) * 8);
#pragma unroll
    for (int i = 0; i < 2; ++i)
#pragma unroll
      for (int j = 0; j < 4; ++j) {
        s[i] += rbf(__uint_as_float(v[t][i][j] << 16) * __uint_as_float(g[j] << 16));
        s[i] += rbf(__uint_as_float(v[t][i][j] & 0xffff0000u) * __uint_as_float(g[j] & 0xffff0000u));
      }
  }
#pragma unroll
  for (int i = 0; i < 2; ++i) {
    s[i] += __shfl_xor(s[i], 1); s[i] += __shfl_xor(s[i], 2); s[i] += __shfl_xor(s[i], 4);
    if ((tid & 7) == 0) gbuf[r0 + (tid >> 3) + 32 * i] = rbf(1.0f / (1.0f + expf(-rbf(s[i]))));
  }
}

__device__ void w2_role(int wb, const unsigned short* __restrict__ w2, unsigned short* __restrict__ w2T, char* smem) {
  u32x4 tv[3][2];
#pragma unroll
  for (int j = 0; j < 3; ++j) {
    int id = wb * 3 + j;
    tr_tiles_load(w2, D_, (id / 10) * 64, (id % 10) * 64, tv[j]);
  }
  typedef unsigned short tile_t[64][72];
  tile_t* tiles = reinterpret_cast<tile_t*>(smem);
#pragma unroll
  for (int j = 0; j < 3; ++j) tr_tiles_put(tiles[j], tv[j]);
  __syncthreads();
#pragma unroll
  for (int j = 0; j < 3; ++j) {
    int id = wb * 3 + j;
    tr_tiles_store_blocked(tiles[j], w2T, (id / 10) * 64, (id % 10) * 64);
  }
}

__device__ void gemm1_role(int b, const unsigned short* __restrict__ x, const unsigned short* __restrict__ w1,
                           unsigned short* __restrict__ hbuf, char* smem) {
  const int tid = threadIdx.x, lane = tid & 63, wid = tid >> 6;
  __builtin_amdgcn_s_setprio(3);
  int t = (b & 7) * (N_G1 / 8) + (b >> 3);
  const int tm = t / G1_TN, tn = t % G1_TN;
  const int m0 = tm * 128, n0 = tn * 128;
  const int wm = wid >> 1, wn = wid & 1, r = lane & 31, h = lane >> 5;

  const unsigned short* asrc[2];
  const unsigned short* bsrc[2];
#pragma unroll
  for (int i = 0; i < 2; ++i) {
    int row = (i * 4 + wid) * 16 + (lane >> 2);
    int c = (lane & 3) ^ ((row >> 2) & 3);
    asrc[i] = x + (size_t)(m0 + row) * D_ + c * 8;
    int k = (i * 4 + wid) * 4 + (lane >> 4);
    int cb = (lane & 15) ^ ((k & 3) << 2);
    int col = n0 + cb * 8;
    if (col > F_ - 8) col = F_ - 8;
    bsrc[i] = w1 + (size_t)k * F_ + col;
  }
  auto stage = [&](int kt, int buf) {
    char* sa = smem + buf * G1_STAGE;
    char* sb = sa + 8192;
#pragma unroll
    for (int i = 0; i < 2; ++i)
      __builtin_amdgcn_global_load_lds((const unsigned*)(asrc[i] + kt * 32), (LDS_AS unsigned*)(sa + (i * 4 + wid) * 1024), 16, 0, 0);
#pragma unroll
    for (int i = 0; i < 2; ++i)
      __builtin_amdgcn_global_load_lds((const unsigned*)(bsrc[i] + (size_t)kt * 32 * F_), (LDS_AS unsigned*)(sb + (i * 4 + wid) * 1024), 16, 0, 0);
  };

  f32x16 acc[2][2];
#pragma unroll
  for (int a = 0; a < 2; ++a)
#pragma unroll
    for (int c = 0; c < 2; ++c)
#pragma unroll
      for (int i = 0; i < 16; ++i) acc[a][c][i] = 0.f;

  const int q = (lane & 15) >> 2, p = lane & 3, blk = (lane >> 4) & 1;
  auto compute = [&](int buf) {
    const char* sa = smem + buf * G1_STAGE;
    const char* sb = sa + 8192;
#pragma unroll
    for (int s = 0; s < 2; ++s) {
      bf16x8 af[2], bfr[2];
#pragma unroll
      for (int mt = 0; mt < 2; ++mt) {
        int row = wm * 64 + mt * 32 + r;
        af[mt] = *reinterpret_cast<const bf16x8*>(sa + row * 64 + (((2 * s + h) ^ ((row >> 2) & 3)) << 4));
      }
#pragma unroll
      for (int nt = 0; nt < 2; ++nt)
#pragma unroll
        for (int tt = 0; tt < 2; ++tt) {
          int k = 16 * s + 8 * h + 4 * tt + q;
          int chunk = wn * 8 + nt * 4 + 2 * blk + (p >> 1);
          const char* ad = sb + k * 256 + ((chunk ^ (q << 2)) << 4) + (p & 1) * 8;
          bf16x4 v = __builtin_amdgcn_ds_read_tr16_b64_v4i16((LDS_AS bf16x4*)(ad));
          bfr[nt][4 * tt + 0] = v[0]; bfr[nt][4 * tt + 1] = v[1]; bfr[nt][4 * tt + 2] = v[2]; bfr[nt][4 * tt + 3] = v[3];
        }
#pragma unroll
      for (int mt = 0; mt < 2; ++mt)
#pragma unroll
        for (int nt = 0; nt < 2; ++nt)
          acc[mt][nt] = __builtin_amdgcn_mfma_f32_32x32x16_bf16(af[mt], bfr[nt], acc[mt][nt], 0, 0, 0);
    }
  };

  constexpr int NK = D_ / 32;
  stage(0, 0);
  stage(1, 1);
  int cur = 0, nxt = 2;
  for (int kt = 0; kt < NK - 1; ++kt) {
    asm volatile("s_waitcnt vmcnt(4)" ::: "memory");
    RAW_BARRIER();
    if (kt + 2 < NK) stage(kt + 2, nxt);
    compute(cur);
    cur = (cur == 2) ? 0 : cur + 1;
    nxt = (nxt == 2) ? 0 : nxt + 1;
  }
  asm volatile("s_waitcnt vmcnt(0)" ::: "memory");
  RAW_BARRIER();
  compute(cur);

  RAW_BARRIER();
  {
    unsigned short* ht = reinterpret_cast<unsigned short*>(smem);
#pragma unroll
    for (int nt = 0; nt < 2; ++nt)
#pragma unroll
      for (int mt = 0; mt < 2; ++mt)
#pragma unroll
        for (int i = 0; i < 16; ++i) {
          int row = wm * 64 + mt * 32 + (i & 3) + 8 * (i >> 2) + 4 * h;
          float hv = rbf(acc[mt][nt][i]);
          float sv = hv * __builtin_amdgcn_rcpf(1.0f + __expf(-hv));
          ht[row * 136 + wn * 64 + nt * 32 + r] = f2bf(sv);
        }
    __syncthreads();
#pragma unroll
    for (int i = 0; i < 8; ++i) {
      int id = tid + 256 * i, row = ((id >> 8) << 4) | (id & 15), ch = (id >> 4) & 15;
      if (n0 + ch * 8 < F_)
        *reinterpret_cast<u32x4*>(hbuf + ((size_t)((m0 + row) >> 4) * 54 + ((n0 + ch * 8) >> 5)) * 512 + (id & 63) * 8) = *reinterpret_cast<const u32x4*>(ht + row * 136 + ch * 8);
    }
  }
}

constexpr int UC_STRIDE = 4416;
constexpr int UC_OFF = 0;
constexpr int KROW = 80;
constexpr int KH_OFF = 8 * UC_STRIDE;
constexpr int KL_OFF = KH_OFF + 64 * KROW + 16;
constexpr int KZ_OFF = KH_OFF + 64 * KROW;
constexpr int PR_OFF = KH_OFF + 12288;
constexpr int OS_OFF = KH_OFF;
constexpr int TB_OFF = PR_OFF + 128 * 16;
constexpr int CONV_LDS = TB_OFF + 768 * 4;

__device__ __forceinline__ float thi_of(int i) {
  int t;
  if (i < 8) t = i;
  else if (i < 16) t = 128 + (i - 8);
  else if (i < 24) t = 256 + 128 * ((i - 16) >> 2) + 2 * ((i - 16) & 3);
  else if (i < 32) t = 512 + 128 * ((i - 24) >> 1) + 4 * ((i - 24) & 1);
  else if (i < 40) t = 1024 + 128 * (i - 32);
  else t = 2048;
  return (float)t;
}

struct KTab { unsigned short v[L_]; };
constexpr KTab make_ktab() {
  KTab t{};
  for (int j = 0; j < L_; ++j) {
    int sh = (j < 256) ? 0 : (j < 512) ? 1 : (j < 1024) ? 2 : 3;
    int rr = j;
    if (sh) {
      int q = j >> sh, rem = j & ((1 << sh) - 1), half = 1 << (sh - 1);
      if (rem > half || (rem == half && (q & 1))) ++q;
      rr = q << sh;
    }
    int i = 40;
    if (rr < 256) i = (rr & 7) + 8 * (rr >> 7);
    else if (rr < 512) i = 16 + 4 * ((rr - 256) >> 7) + ((rr & 7) >> 1);
    else if (rr < 1024) i = 24 + 2 * ((rr - 512) >> 7) + ((rr & 7) >> 2);
    else if (rr < 2048) i = 32 + ((rr - 1024) >> 7);
    t.v[j] = (unsigned short)(i * 16 + ((rr >> 3) & 15));
  }
  return t;
}
__device__ __attribute__((aligned(16))) const KTab KTAB = make_ktab();

__device__ void conv_role(int d, const unsigned short* __restrict__ x, const unsigned short* __restrict__ logAr,
                          const unsigned short* __restrict__ Aim, const unsigned short* __restrict__ Cre,
                          const unsigned short* __restrict__ Cim, const unsigned short* __restrict__ logdt,
                          const unsigned short* __restrict__ Dsk, unsigned short* __restrict__ aT, char* smem) {
  const int tid = threadIdx.x, lane = tid & 63, wid = tid >> 6;

  const int pn = tid & (NS_ - 1);
  const unsigned short r_ldt = logdt[d];
  const unsigned short r_lar = logAr[d * NS_ + pn], r_aim = Aim[d * NS_ + pn];
  const unsigned short r_cre = Cre[d * NS_ + pn], r_cim = Cim[d * NS_ + pn];
  const u32x4 kidx = *reinterpret_cast<const u32x4*>(KTAB.v + 8 * tid);
  __builtin_amdgcn_sched_barrier(0);
  unsigned short ug[8];
#pragma unroll
  for (int k = 0; k < 8; ++k) ug[k] = x[(size_t)(8 * tid + k) * D_ + d];
  __builtin_amdgcn_sched_barrier(0);
  asm volatile("" :: "v"(r_ldt), "v"(r_lar), "v"(r_aim), "v"(r_cre), "v"(r_cim));

  const float dt = rbf(expf(bf2f(r_ldt)));
  if (tid < NS_) {
    float are = -rbf(expf(bf2f(r_lar)));
    float aim = bf2f(r_aim);
    float lr = are * dt, li = aim * dt;
    float cr = bf2f(r_cre) * dt, ci = bf2f(r_cim) * dt;
    float mag = sqrtf(cr * cr + ci * ci);
    f32x4 pp;
    pp[0] = lr * 1.4426950408889634f;
    pp[1] = li * 0.15915494309189535f;
    pp[2] = log2f(mag);
    pp[3] = atan2f(ci, cr) * 0.15915494309189535f;
    *reinterpret_cast<f32x4*>(smem + PR_OFF + tid * 16) = pp;
  }
  if (tid < 64) {
    u32x4 z = {0, 0, 0, 0};
    *reinterpret_cast<u32x4*>(smem + UC_OFF + (tid >> 3) * UC_STRIDE + (tid & 7) * 16) = z;
  }
  RAW_BARRIER();

  {
    const int x = lane & 15, kk = lane >> 4;
    float th[3];
#pragma unroll
    for (int T = 0; T < 3; ++T) th[T] = thi_of(16 * T + x);
    const float tlo = (float)(8 * x);
    f32x4 acc[3];
#pragma unroll
    for (int T = 0; T < 3; ++T) acc[T] = (f32x4){0.f, 0.f, 0.f, 0.f};
#pragma unroll 2
    for (int g = 0; g < 8; ++g) {
      int n = 32 * wid + 4 * g + kk;
      f32x4 pp = *reinterpret_cast<const f32x4*>(smem + PR_OFF + n * 16);
      float qe = __builtin_amdgcn_exp2f(pp[0] * tlo);
      float qa = pp[1] * tlo;
      float qr = qe * __builtin_amdgcn_cosf(qa), qi = qe * __builtin_amdgcn_sinf(qa);
#pragma unroll
      for (int T = 0; T < 3; ++T) {
        float pe = __builtin_amdgcn_exp2f(pp[0] * th[T] + pp[2]);
        float pa = pp[1] * th[T] + pp[3];
        float pr = pe * __builtin_amdgcn_cosf(pa), pi = pe * __builtin_amdgcn_sinf(pa);
        acc[T] = __builtin_amdgcn_mfma_f32_16x16x4f32(qr, pr, acc[T], 0, 0, 0);
        acc[T] = __builtin_amdgcn_mfma_f32_16x16x4f32(-qi, pi, acc[T], 0, 0, 0);
      }
    }
    float* os = reinterpret_cast<float*>(smem + OS_OFF) + wid * 768;
#pragma unroll
    for (int T = 0; T < 3; ++T) *reinterpret_cast<f32x4*>(os + (16 * T + x) * 16 + 4 * (kk ^ ((x >> 1) & 3))) = acc[T];
  }
  RAW_BARRIER();

  u32x4 uvec;
#pragma unroll
  for (int j = 0; j < 4; ++j) uvec[j] = (unsigned)ug[2 * j] | ((unsigned)ug[2 * j + 1] << 16);
  *reinterpret_cast<u32x4*>(smem + UC_OFF + 7 * UC_STRIDE + 112 + tid * 16) = uvec;
  {
    unsigned* tab = reinterpret_cast<unsigned*>(smem + TB_OFF);
#pragma unroll
    for (int k = 0; k < 3; ++k) {
      int e = tid + 256 * k;
      const float* osp = reinterpret_cast<const float*>(smem + OS_OFF) + (e ^ ((e >> 3) & 12));
      float kv = 2.0f * ((osp[0] + osp[768]) + (osp[1536] + osp[2304]));
      unsigned short hi = f2bf(kv);
      tab[e] = (unsigned)hi | ((unsigned)f2bf(kv - bf2f(hi)) << 16);
    }
  }
  __syncthreads();
  {
    u32x4 v1 = {0, 0, 0, 0};
    if (tid < 255) v1 = *reinterpret_cast<const u32x4*>(smem + UC_OFF + 7 * UC_STRIDE + 112 + (tid + 1) * 16);
    unsigned w[8] = {uvec[0], uvec[1], uvec[2], uvec[3], v1[0], v1[1], v1[2], v1[3]};
#pragma unroll
    for (int e = 1; e < 8; ++e) {
      u32x4 o;
#pragma unroll
      for (int j = 0; j < 4; ++j) {
        int base = j + (e >> 1);
        if (e & 1) o[j] = (w[base] >> 16) | (w[base + 1] << 16);
        else o[j] = w[base];
      }
      *reinterpret_cast<u32x4*>(smem + UC_OFF + (e - 1) * UC_STRIDE + 128 + tid * 16) = o;
    }
    if (tid == 0) {
#pragma unroll
      for (int e = 1; e < 8; ++e)
#pragma unroll
        for (int k = 0; k < e; ++k)
          *reinterpret_cast<unsigned short*>(smem + UC_OFF + (e - 1) * UC_STRIDE + 2 * (64 - e + k)) =
              (unsigned short)(w[k >> 1] >> (16 * (k & 1)));
    }
  }
  {
    const unsigned* tab = reinterpret_cast<const unsigned*>(smem + TB_OFF);
    unsigned w[8];
#pragma unroll
    for (int k = 0; k < 8; ++k) w[k] = tab[(kidx[k >> 1] >> (16 * (k & 1))) & 0xffff];
    u32x4 vh, vl;
#pragma unroll
    for (int j = 0; j < 4; ++j) {
      vh[j] = (w[7 - 2 * j] & 0xffffu) | (w[6 - 2 * j] << 16);
      vl[j] = (w[7 - 2 * j] >> 16) | (w[6 - 2 * j] & 0xffff0000u);
    }
    *reinterpret_cast<u32x4*>(smem + KH_OFF + (tid >> 2) * KROW + (tid & 3) * 16) = vh;
    *reinterpret_cast<u32x4*>(smem + KL_OFF + (tid >> 2) * KROW + (tid & 3) * 16) = vl;
    if (tid == 0) { u32x4 z = {0, 0, 0, 0}; *reinterpret_cast<u32x4*>(smem + KZ_OFF) = z; }
  }
  __syncthreads();

  f32x16 y0, y1;
#pragma unroll
  for (int i = 0; i < 16; ++i) { y0[i] = 0.f; y1[i] = 0.f; }
  {
    const int r = lane & 31, h = lane >> 5;
    const int e = r & 7;
    const char* ubase = smem + UC_OFF + e * UC_STRIDE + 2 * (r - e + 56 - 8 * h);
#pragma unroll 2
    for (int dl = wid; dl < 32; dl += 4) {
      bf16x8 a0 = *reinterpret_cast<const bf16x8*>(ubase + 64 * dl);
      bf16x8 a1 = *reinterpret_cast<const bf16x8*>(ubase + 64 * dl - 32);
      bf16x8 a2 = *reinterpret_cast<const bf16x8*>(ubase + 64 * dl + 2048);
      bf16x8 a3 = *reinterpret_cast<const bf16x8*>(ubase + 64 * dl + 2048 - 32);
      const int m = r - dl;
      const int off = (m >= 0) ? (m * KROW + 16 * h) : (KZ_OFF - KH_OFF);
      const int off1 = (m >= 0) ? (off + 32) : off;
      const int lo = (m >= 0) ? KL_OFF : KH_OFF;
      bf16x8 fh0 = *reinterpret_cast<const bf16x8*>(smem + KH_OFF + off);
      bf16x8 fh1 = *reinterpret_cast<const bf16x8*>(smem + KH_OFF + off1);
      bf16x8 fl0 = *reinterpret_cast<const bf16x8*>(smem + lo + off);
      bf16x8 fl1 = *reinterpret_cast<const bf16x8*>(smem + lo + off1);
      const int go = (32 + m) * KROW + 16 * h;
      bf16x8 gh0 = *reinterpret_cast<const bf16x8*>(smem + KH_OFF + go);
      bf16x8 gh1 = *reinterpret_cast<const bf16x8*>(smem + KH_OFF + go + 32);
      bf16x8 gl0 = *reinterpret_cast<const bf16x8*>(smem + KL_OFF + go);
      bf16x8 gl1 = *reinterpret_cast<const bf16x8*>(smem + KL_OFF + go + 32);
      y0 = __builtin_amdgcn_mfma_f32_32x32x16_bf16(a0, fh0, y0, 0, 0, 0);
      y1 = __builtin_amdgcn_mfma_f32_32x32x16_bf16(a2, fh0, y1, 0, 0, 0);
      y0 = __builtin_amdgcn_mfma_f32_32x32x16_bf16(a1, fh1, y0, 0, 0, 0);
      y1 = __builtin_amdgcn_mfma_f32_32x32x16_bf16(a3, fh1, y1, 0, 0, 0);
      y0 = __builtin_amdgcn_mfma_f32_32x32x16_bf16(a0, fl0, y0, 0, 0, 0);
      y1 = __builtin_amdgcn_mfma_f32_32x32x16_bf16(a2, fl0, y1, 0, 0, 0);
      y0 = __builtin_amdgcn_mfma_f32_32x32x16_bf16(a1, fl1, y0, 0, 0, 0);
      y1 = __builtin_amdgcn_mfma_f32_32x32x16_bf16(a3, fl1, y1, 0, 0, 0);
      y1 = __builtin_amdgcn_mfma_f32_32x32x16_bf16(a0, gh0, y1, 0, 0, 0);
      y1 = __builtin_amdgcn_mfma_f32_32x32x16_bf16(a1, gh1, y1, 0, 0, 0);
      y1 = __builtin_amdgcn_mfma_f32_32x32x16_bf16(a0, gl0, y1, 0, 0, 0);
      y1 = __builtin_amdgcn_mfma_f32_32x32x16_bf16(a1, gl1, y1, 0, 0, 0);
    }
  }
  __syncthreads();
  {
    const int r = lane & 31, h = lane >> 5;
    float* slab = reinterpret_cast<float*>(smem + wid * 9216);
#pragma unroll
    for (int g = 0; g < 4; ++g) {
      f32x4 v0 = {y0[4 * g], y0[4 * g + 1], y0[4 * g + 2], y0[4 * g + 3]};
      f32x4 v1 = {y1[4 * g], y1[4 * g + 1], y1[4 * g + 2], y1[4 * g + 3]};
      int b = 8 * g + 4 * h;
      *reinterpret_cast<f32x4*>(slab + 36 * r + b) = v0;
      *reinterpret_cast<f32x4*>(slab + 36 * (32 + r) + b) = v1;
    }
  }
  __syncthreads();
  {
    const float dsk = bf2f(Dsk[d]);
    const float* s = reinterpret_cast<const float*>(smem) + (tid >> 2) * 36 + (tid & 3) * 8;
    f32x4 ys[2];
#pragma unroll
    for (int hh = 0; hh < 2; ++hh) {
      f32x4 s0 = *reinterpret_cast<const f32x4*>(s + 4 * hh);
      f32x4 s1 = *reinterpret_cast<const f32x4*>(s + 2304 + 4 * hh);
      f32x4 s2 = *reinterpret_cast<const f32x4*>(s + 4608 + 4 * hh);
      f32x4 s3 = *reinterpret_cast<const f32x4*>(s + 6912 + 4 * hh);
      ys[hh] = (s0 + s1) + (s2 + s3);
    }
    unsigned short o[8];
#pragma unroll
    for (int k = 0; k < 8; ++k) {
      float yb = rbf(ys[k >> 2][k & 3]);
      float uu = bf2f((unsigned short)(uvec[k >> 1] >> (16 * (k & 1))));
      float sk = rbf(uu * dsk);
      o[k] = f2bf(yb + sk);
    }
    u32x4 v;
#pragma unroll
    for (int j = 0; j < 4; ++j) v[j] = o[2 * j] | ((unsigned)o[2 * j + 1] << 16);
    *reinterpret_cast<u32x4*>(aT + ((size_t)(tid >> 3) * D_ + d) * 64 + 8 * (tid & 7)) = v;
  }
}

__global__ __launch_bounds__(256, 3) void k12(const unsigned short* __restrict__ x, const unsigned short* __restrict__ w1,
                                              const unsigned short* __restrict__ w2, const unsigned short* __restrict__ gw,
                                              const unsigned short* __restrict__ logAr, const unsigned short* __restrict__ Aim,
                                              const unsigned short* __restrict__ Cre, const unsigned short* __restrict__ Cim,
                                              const unsigned short* __restrict__ logdt, const unsigned short* __restrict__ Dsk,
                                              unsigned short* __restrict__ hbuf, unsigned short* __restrict__ w2T,
                                              float* __restrict__ gbuf, unsigned short* __restrict__ aT) {
  extern __shared__ __attribute__((aligned(16))) char smem[];
  const int b = blockIdx.x;
  constexpr int G0 = 0;
  if (b >= G0 && b < G0 + D_) conv_role(b - G0, x, logAr, Aim, Cre, Cim, logdt, Dsk, aT, smem);
  else if (b < B_GATE) gemm1_role(b < G0 ? b : b - D_, x, w1, hbuf, smem);
  else if (b < B_W2) gate_role(b - B_GATE, x, gw, gbuf);
  else w2_role(b - B_W2, w2, w2T, smem);
}

constexpr int G2_RS = 84;
__global__ __launch_bounds__(256, 1) void k3_gemm2(const unsigned short* __restrict__ hbuf, const unsigned short* __restrict__ w2T,
                                                   const unsigned short* __restrict__ aT, const float* __restrict__ gbuf,
                                                   unsigned short* __restrict__ out) {
  __shared__ __attribute__((aligned(16))) float red[4][64][G2_RS];
  __shared__ unsigned atile[80 * 33];
  const int tid = threadIdx.x, lane = tid & 63, wid = tid >> 6;
  int t = (blockIdx.x & 7) * 32 + (blockIdx.x >> 3);
  const int tm = t >> 3, tn = t & 7;
  const int m0 = tm * 64, n0 = tn * 80;
  const int fr = lane & 15, fq = lane >> 4;
  const int wv = __builtin_amdgcn_readfirstlane(wid);
  const int c0 = (wv * 27) >> 2;
  const int nch = (((wv + 1) * 27) >> 2) - c0;

  const unsigned short* ap = hbuf + (size_t)(m0 >> 4) * 54 * 512 + lane * 8 + c0 * 1024;
  const unsigned short* bp = w2T + (size_t)(n0 >> 4) * 54 * 512 + lane * 8 + c0 * 1024;

  f32x4 acc[4][5];
#pragma unroll
  for (int a = 0; a < 4; ++a)
#pragma unroll
    for (int c = 0; c < 5; ++c) acc[a][c] = (f32x4){0.f, 0.f, 0.f, 0.f};

  constexpr int PD = 2;
  bf16x8 A[PD][4][2], B[PD][5][2];
#define G2_LOAD(S, c)                                                                                  \
  {                                                                                                    \
    _Pragma("unroll") for (int a = 0; a < 4; ++a) {                                                    \
      A[S][a][0] = *reinterpret_cast<const bf16x8*>(ap + a * 54 * 512 + (c) * 1024);                   \
      A[S][a][1] = *reinterpret_cast<const bf16x8*>(ap + a * 54 * 512 + (c) * 1024 + 512);             \
    }                                                                                                  \
    _Pragma("unroll") for (int a = 0; a < 5; ++a) {                                                    \
      B[S][a][0] = *reinterpret_cast<const bf16x8*>(bp + a * 54 * 512 + (c) * 1024);                   \
      B[S][a][1] = *reinterpret_cast<const bf16x8*>(bp + a * 54 * 512 + (c) * 1024 + 512);             \
    }                                                                                                  \
  }
#define G2_COMP(S)                                                                                     \
  {                                                                                                    \
    _Pragma("unroll") for (int s = 0; s < 2; ++s)                                                      \
      _Pragma("unroll") for (int a = 0; a < 4; ++a)                                                    \
        _Pragma("unroll") for (int c = 0; c < 5; ++c)                                                  \
          acc[a][c] = __builtin_amdgcn_mfma_f32_16x16x32_bf16(A[S][a][s], B[S][c][s], acc[a][c], 0, 0, 0); \
  }
#pragma unroll
  for (int i = 0; i < PD - 1; ++i) {
    G2_LOAD(i, i);
    __builtin_amdgcn_sched_barrier(0);
  }
  u32x4 apre[3];
  float gpre[3];
#pragma unroll
  for (int j = 0; j < 3; ++j) {
    int idx = tid + 256 * j;
    if (idx > 639) idx = 639;
    apre[j] = *reinterpret_cast<const u32x4*>(aT + ((size_t)tm * D_ + n0) * 64 + idx * 8);
    gpre[j] = gbuf[m0 + idx / 10];
  }
  __builtin_amdgcn_sched_barrier(0);
#pragma unroll
  for (int i = 0; i < 7; ++i) {
    if (i + PD - 1 < 7) {
      const int cn = (i + PD - 1 < 6) ? (i + PD - 1) : (nch - 1);
      G2_LOAD((i + PD - 1) % PD, cn);
    }
    __builtin_amdgcn_sched_barrier(0);
    if (i == 6 && nch != 7) {
      const bf16x8 z = {0, 0, 0, 0, 0, 0, 0, 0};
#pragma unroll
      for (int a = 0; a < 4; ++a) { A[i % PD][a][0] = z; A[i % PD][a][1] = z; }
#pragma unroll
      for (int a = 0; a < 5; ++a) { B[i % PD][a][0] = z; B[i % PD][a][1] = z; }
    }
    G2_COMP(i % PD);
    __builtin_amdgcn_sched_barrier(0);
  }
#undef G2_LOAD
#undef G2_COMP

#pragma unroll
  for (int a = 0; a < 4; ++a)
#pragma unroll
    for (int c = 0; c < 5; ++c)
#pragma unroll
      for (int i = 0; i < 4; ++i) red[wid][16 * a + 4 * fq + i][16 * c + fr] = acc[a][c][i];
#pragma unroll
  for (int j = 0; j < 3; ++j) {
    const int idx = tid + 256 * j;
    if (idx < 640) {
#pragma unroll
      for (int q = 0; q < 4; ++q) atile[(idx >> 3) * 33 + (idx & 7) * 4 + q] = apre[j][q];
    }
  }
  __syncthreads();

#pragma unroll
  for (int j = 0; j < 3; ++j) {
    const int idx = tid + 256 * j;
    if (idx < 640) {
      const int row = idx / 10, cg = idx - row * 10;
      f32x4 s[2];
#pragma unroll
      for (int hh = 0; hh < 2; ++hh) {
        f32x4 s0 = *reinterpret_cast<const f32x4*>(&red[0][row][8 * cg + 4 * hh]);
        f32x4 s1 = *reinterpret_cast<const f32x4*>(&red[1][row][8 * cg + 4 * hh]);
        f32x4 s2 = *reinterpret_cast<const f32x4*>(&red[2][row][8 * cg + 4 * hh]);
        f32x4 s3 = *reinterpret_cast<const f32x4*>(&red[3][row][8 * cg + 4 * hh]);
        s[hh] = (s0 + s1) + (s2 + s3);
      }
      const float g = gpre[j];
      const float t2 = rbf(1.0f - g);
      const unsigned short* at = reinterpret_cast<const unsigned short*>(atile) + (8 * cg) * 66 + row;
      unsigned short o[8];
#pragma unroll
      for (int k = 0; k < 8; ++k) {
        float bv = rbf(s[k >> 2][k & 3]);
        float a = bf2f(at[k * 66]);
        float t1 = rbf(g * a);
        float t3 = rbf(t2 * bv);
        o[k] = f2bf(t1 + t3);
      }
      u32x4 v;
#pragma unroll
      for (int q = 0; q < 4; ++q) v[q] = o[2 * q] | ((unsigned)o[2 * q + 1] << 16);
      *reinterpret_cast<u32x4*>(out + (size_t)(m0 + row) * D_ + n0 + 8 * cg) = v;
    }
  }
}

extern "C" void kernel_launch(void* const* d_in, const int* in_sizes, int n_in, void* d_out, int out_size, void* d_ws,
                              size_t ws_size, hipStream_t stream) {
  const unsigned short* u = (const unsigned short*)d_in[0];
  const unsigned short* logAr = (const unsigned short*)d_in[1];
  const unsigned short* Aim = (const unsigned short*)d_in[2];
  const unsigned short* Cre = (const unsigned short*)d_in[3];
  const unsigned short* Cim = (const unsigned short*)d_in[4];
  const unsigned short* logdt = (const unsigned short*)d_in[5];
  const unsigned short* Dsk = (const unsigned short*)d_in[6];
  const unsigned short* w1 = (const unsigned short*)d_in[8];
  const unsigned short* w2 = (const unsigned short*)d_in[9];
  const unsigned short* gw = (const unsigned short*)d_in[10];
  unsigned short* out = (unsigned short*)d_out;

  char* ws = (char*)d_ws;
  unsigned short* hbuf = (unsigned short*)ws;
  unsigned short* aT = (unsigned short*)(ws + (size_t)L_ * F_ * 2);
  unsigned short* w2T = (unsigned short*)(ws + (size_t)L_ * F_ * 2 + (size_t)D_ * L_ * 2);
  float* gbuf = (float*)(ws + (size_t)L_ * F_ * 2 + (size_t)D_ * L_ * 2 + (size_t)D_ * F_ * 2);

  constexpr int K12_LDS = (CONV_LDS > 3 * G1_STAGE) ? CONV_LDS : 3 * G1_STAGE;
  hipLaunchKernelGGL(k12, dim3(K12_GRID), dim3(256), K12_LDS, stream, u, w1, w2, gw, logAr, Aim, Cre, Cim, logdt, Dsk,
                     hbuf, w2T, gbuf, aT);
  hipLaunchKernelGGL(k3_gemm2, dim3(256), dim3(256), 0, stream, hbuf, w2T, aT, gbuf, out);
}
```

```cpp
#include <hip/hip_runtime.h>
#include <hip/hip_bf16.h>
#include <stdint.h>

typedef __attribute__((ext_vector_type(8))) short bf16x8;
typedef __attribute__((ext_vector_type(4))) short bf16x4;
typedef __attribute__((ext_vector_type(16))) float f32x16;
typedef __attribute__((ext_vector_type(4))) float f32x4;
typedef __attribute__((ext_vector_type(4))) unsigned int u32x4;
typedef __attribute__((ext_vector_type(2))) unsigned int u32x2;

#define LDS_AS __attribute__((address_space(3)))
#define RAW_BARRIER() do { asm volatile("s_waitcnt lgkmcnt(0)" ::: "memory"); __builtin_amdgcn_s_barrier(); } while (0)

constexpr int L_ = 2048, D_ = 640, NS_ = 128, F_ = 1728;

__device__ __forceinline__ float bf2f(unsigned short v) { return __uint_as_float(((unsigned)v) << 16); }
__device__ __forceinline__ unsigned short f2bf(float f) {
  __bf16 b = (__bf16)f;
  return __builtin_bit_cast(unsigned short, b);
}
__device__ __forceinline__ float rbf(float f) { return bf2f(f2bf(f)); }

constexpr int G1_TM = L_ / 128, G1_TN = (F_ + 127) / 128;
constexpr int N_G1 = G1_TM * G1_TN;
constexpr int G1_STAGE = 16384;
constexpr int N_GATE = 32, N_W2 = 90;
constexpr int K12_GRID = 768;

__device__ __forceinline__ void tr_tiles_load(const unsigned short* __restrict__ src, int ss, int r0, int c0,
                                              u32x4 (&v)[2]) {
  const int tid = threadIdx.x;
#pragma unroll
  for (int i = 0; i < 2; ++i) {
    int id = tid + 256 * i, row = id >> 3, ch = id & 7;
    v[i] = *reinterpret_cast<const u32x4*>(src + (size_t)(r0 + row) * ss + c0 + ch * 8);
  }
}
__device__ __forceinline__ void tr_tiles_put(unsigned short (*tile)[72], const u32x4 (&v)[2]) {
  const int tid = threadIdx.x;
#pragma unroll
  for (int i = 0; i < 2; ++i) {
    int id = tid + 256 * i, row = id >> 3, ch = id & 7;
    *reinterpret_cast<u32x4*>(&tile[row][ch * 8]) = v[i];
  }
}
__device__ __forceinline__ void tr_tiles_store_blocked(unsigned short (*tile)[72], unsigned short* __restrict__ dst,
                                                       int r0, int c0) {
  const int tid = threadIdx.x;
#pragma unroll
  for (int i = 0; i < 2; ++i) {
    int id = tid + 256 * i, col = ((id >> 7) << 4) | (id & 15), ch = (id >> 4) & 7;
    unsigned short o[8];
#pragma unroll
    for (int k = 0; k < 8; ++k) o[k] = tile[ch * 8 + k][col];
    u32x4 w;
#pragma unroll
    for (int j = 0; j < 4; ++j) w[j] = o[2 * j] | ((unsigned)o[2 * j + 1] << 16);
    unsigned short* dp = dst + ((size_t)((c0 + col) >> 4) * 54 + ((r0 + ch * 8) >> 5)) * 512 + (id & 63) * 8;
    *reinterpret_cast<u32x4*>(dp) = w;
  }
}

__device__ __forceinline__ void gate_role(int gb, const unsigned short* __restrict__ x, const unsigned short* __restrict__ gw,
                          float* __restrict__ gbuf) {
  const int tid = threadIdx.x;
  const int r0 = gb * 64;
  u32x4 v[10][2];
#pragma unroll
  for (int t = 0; t < 10; ++t) tr_tiles_load(x, D_, r0, t * 64, v[t]);
  float s[2] = {0.f, 0.f};
#pragma unroll
  for (int t = 0; t < 10; ++t) {
    u32x4 g = *reinterpret_cast<const u32x4*>(gw + t * 64 + (tid & 7) * 8);
#pragma unroll
    for (int i = 0; i < 2; ++i)
#pragma unroll
      for (int j = 0; j < 4; ++j) {
        s[i] += rbf(__uint_as_float(v[t][i][j] << 16) * __uint_as_float(g[j] << 16));
        s[i] += rbf(__uint_as_float(v[t][i][j] & 0xffff0000u) * __uint_as_float(g[j] & 0xffff0000u));
      }
  }
#pragma unroll
  for (int i = 0; i < 2; ++i) {
    s[i] += __shfl_xor(s[i], 1); s[i] += __shfl_xor(s[i], 2); s[i] += __shfl_xor(s[i], 4);
    if ((tid & 7) == 0) gbuf[r0 + (tid >> 3) + 32 * i] = rbf(1.0f / (1.0f + expf(-rbf(s[i]))));
  }
}

__device__ __forceinline__ void w2_role(int wb, const unsigned short* __restrict__ w2, unsigned short* __restrict__ w2T, char* smem) {
  u32x4 tv[3][2];
#pragma unroll
  for (int j = 0; j < 3; ++j) {
    int id = wb * 3 + j;
    tr_tiles_load(w2, D_, (id / 10) * 64, (id % 10) * 64, tv[j]);
  }
  typedef unsigned short tile_t[64][72];
  tile_t* tiles = reinterpret_cast<tile_t*>(smem);
#pragma unroll
  for (int j = 0; j < 3; ++j) tr_tiles_put(tiles[j], tv[j]);
  __syncthreads();
#pragma unroll
  for (int j = 0; j < 3; ++j) {
    int id = wb * 3 + j;
    tr_tiles_store_blocked(tiles[j], w2T, (id / 10) * 64, (id % 10) * 64);
  }
}

__device__ __forceinline__ void gemm1_role(int b, const unsigned short* __restrict__ x, const unsigned short* __restrict__ w1,
                           unsigned short* __restrict__ hbuf, char* smem) {
  const int tid = threadIdx.x, lane = tid & 63, wid = tid >> 6;
  __builtin_amdgcn_s_setprio(3);
  int t = (b & 7) * (N_G1 / 8) + (b >> 3);
  const int tm = t / G1_TN, tn = t % G1_TN;
  const int m0 = tm * 128, n0 = tn * 128;
  const int wm = wid >> 1, wn = wid & 1, r = lane & 31, h = lane >> 5;

  const unsigned short* asrc[2];
  const unsigned short* bsrc[2];
#pragma unroll
  for (int i = 0; i < 2; ++i) {
    int row = (i * 4 + wid) * 16 + (lane >> 2);
    int c = (lane & 3) ^ ((row >> 2) & 3);
    asrc[i] = x + (size_t)(m0 + row) * D_ + c * 8;
    int k = (i * 4 + wid) * 4 + (lane >> 4);
    int cb = (lane & 15) ^ ((k & 3) << 2);
    int col = n0 + cb * 8;
    if (col > F_ - 8) col = F_ - 8;
    bsrc[i] = w1 + (size_t)k * F_ + col;
  }
  auto stage = [&](int kt, int buf) {
    char* sa = smem + buf * G1_STAGE;
    char* sb = sa + 8192;
#pragma unroll
    for (int i = 0; i < 2; ++i)
      __builtin_amdgcn_global_load_lds((const unsigned*)(asrc[i] + kt * 32), (LDS_AS unsigned*)(sa + (i * 4 + wid) * 1024), 16, 0, 0);
#pragma unroll
    for (int i = 0; i < 2; ++i)
      __builtin_amdgcn_global_load_lds((const unsigned*)(bsrc[i] + (size_t)kt * 32 * F_), (LDS_AS unsigned*)(sb + (i * 4 + wid) * 1024), 16, 0, 0);
  };

  f32x16 acc[2][2];
#pragma unroll
  for (int a = 0; a < 2; ++a)
#pragma unroll
    for (int c = 0; c < 2; ++c)
#pragma unroll
      for (int i = 0; i < 16; ++i) acc[a][c][i] = 0.f;

  const int q = (lane & 15) >> 2, p = lane & 3, blk = (lane >> 4) & 1;
  auto compute = [&](int buf) {
    const char* sa = smem + buf * G1_STAGE;
    const char* sb = sa + 8192;
#pragma unroll
    for (int s = 0; s < 2; ++s) {
      bf16x8 af[2], bfr[2];
#pragma unroll
      for (int mt = 0; mt < 2; ++mt) {
        int row = wm * 64 + mt * 32 + r;
        af[mt] = *reinterpret_cast<const bf16x8*>(sa + row * 64 + (((2 * s + h) ^ ((row >> 2) & 3)) << 4));
      }
#pragma unroll
      for (int nt = 0; nt < 2; ++nt)
#pragma unroll
        for (int tt = 0; tt < 2; ++tt) {
          int k = 16 * s + 8 * h + 4 * tt + q;
          int chunk = wn * 8 + nt * 4 + 2 * blk + (p >> 1);
          const char* ad = sb + k * 256 + ((chunk ^ (q << 2)) << 4) + (p & 1) * 8;
          bf16x4 v = __builtin_amdgcn_ds_read_tr16_b64_v4i16((LDS_AS bf16x4*)(ad));
          bfr[nt][4 * tt + 0] = v[0]; bfr[nt][4 * tt + 1] = v[1]; bfr[nt][4 * tt + 2] = v[2]; bfr[nt][4 * tt + 3] = v[3];
        }
#pragma unroll
      for (int mt = 0; mt < 2; ++mt)
#pragma unroll
        for (int nt = 0; nt < 2; ++nt)
          acc[mt][nt] = __builtin_amdgcn_mfma_f32_32x32x16_bf16(af[mt], bfr[nt], acc[mt][nt], 0, 0, 0);
    }
  };

  constexpr int NK = D_ / 32;
  stage(0, 0);
  stage(1, 1);
  int cur = 0, nxt = 2;
  for (int kt = 0; kt < NK - 1; ++kt) {
    asm volatile("s_waitcnt vmcnt(4)" ::: "memory");
    RAW_BARRIER();
    if (kt + 2 < NK) stage(kt + 2, nxt);
    compute(cur);
    cur = (cur == 2) ? 0 : cur + 1;
    nxt = (nxt == 2) ? 0 : nxt + 1;
  }
  asm volatile("s_waitcnt vmcnt(0)" ::: "memory");
  RAW_BARRIER();
  compute(cur);

  RAW_BARRIER();
  {
    unsigned short* ht = reinterpret_cast<unsigned short*>(smem);
#pragma unroll
    for (int nt = 0; nt < 2; ++nt)
#pragma unroll
      for (int mt = 0; mt < 2; ++mt)
#pragma unroll
        for (int i = 0; i < 16; ++i) {
          int row = wm * 64 + mt * 32 + (i & 3) + 8 * (i >> 2) + 4 * h;
          float hv = rbf(acc[mt][nt][i]);
          float sv = hv * __builtin_amdgcn_rcpf(1.0f + __expf(-hv));
          ht[row * 136 + wn * 64 + nt * 32 + r] = f2bf(sv);
        }
    __syncthreads();
#pragma unroll
    for (int i = 0; i < 8; ++i) {
      int id = tid + 256 * i, row = ((id >> 8) << 4) | (id & 15), ch = (id >> 4) & 15;
      if (n0 + ch * 8 < F_)
        *reinterpret_cast<u32x4*>(hbuf + ((size_t)((m0 + row) >> 4) * 54 + ((n0 + ch * 8) >> 5)) * 512 + (id & 63) * 8) = *reinterpret_cast<const u32x4*>(ht + row * 136 + ch * 8);
    }
  }
}

constexpr int UC_STRIDE = 4416;
constexpr int UC_OFF = 0;
constexpr int KROW = 80;
constexpr int KH_OFF = 36864;
constexpr int KZ_OFF = KH_OFF + 64 * KROW;
constexpr int KL_OFF = KZ_OFF + 16;
constexpr int TB_OFF = KL_OFF + 64 * KROW;
constexpr int EX_OFF = TB_OFF + 2 * 3072;
constexpr int CONV_LDS = EX_OFF + 128;
constexpr int OS_OFF = 0;
constexpr int PR_OFF = 24576;

__device__ __forceinline__ float thi_of(int i) {
  int t;
  if (i < 8) t = i;
  else if (i < 16) t = 128 + (i - 8);
  else if (i < 24) t = 256 + 128 * ((i - 16) >> 2) + 2 * ((i - 16) & 3);
  else if (i < 32) t = 512 + 128 * ((i - 24) >> 1) + 4 * ((i - 24) & 1);
  else if (i < 40) t = 1024 + 128 * (i - 32);
  else t = 2048;
  return (float)t;
}

struct KTab { unsigned short v[L_]; };
constexpr KTab make_ktab() {
  KTab t{};
  for (int j = 0; j < L_; ++j) {
    int sh = (j < 256) ? 0 : (j < 512) ? 1 : (j < 1024) ? 2 : 3;
    int rr = j;
    if (sh) {
      int q = j >> sh, rem = j & ((1 << sh) - 1), half = 1 << (sh - 1);
      if (rem > half || (rem == half && (q & 1))) ++q;
      rr = q << sh;
    }
    int i = 40;
    if (rr < 256) i = (rr & 7) + 8 * (rr >> 7);
    else if (rr < 512) i = 16 + 4 * ((rr - 256) >> 7) + ((rr & 7) >> 1);
    else if (rr < 1024) i = 24 + 2 * ((rr - 512) >> 7) + ((rr & 7) >> 2);
    else if (rr < 2048) i = 32 + ((rr - 1024) >> 7);
    t.v[j] = (unsigned short)(i * 16 + ((rr >> 3) & 15));
  }
  return t;
}
__device__ __attribute__((aligned(16))) const KTab KTAB = make_ktab();

__device__ __forceinline__ void conv_role(int d0, const unsigned short* __restrict__ x, const unsigned short* __restrict__ logAr,
                          const unsigned short* __restrict__ Aim, const unsigned short* __restrict__ Cre,
                          const unsigned short* __restrict__ Cim, const unsigned short* __restrict__ logdt,
                          const unsigned short* __restrict__ Dsk, unsigned short* __restrict__ aT, char* smem) {
  const int tid = threadIdx.x, lane = tid & 63, wid = tid >> 6;

  const unsigned short r_ldt = logdt[d0 + (tid >> 7)];
  const unsigned short r_lar = logAr[d0 * NS_ + tid], r_aim = Aim[d0 * NS_ + tid];
  const unsigned short r_cre = Cre[d0 * NS_ + tid], r_cim = Cim[d0 * NS_ + tid];
  const unsigned r_dsk = *reinterpret_cast<const unsigned*>(Dsk + d0);
  const u32x4 kidx = *reinterpret_cast<const u32x4*>(KTAB.v + 8 * tid);
  __builtin_amdgcn_sched_barrier(0);
  unsigned sq[16];
#pragma unroll
  for (int k = 0; k < 8; ++k) sq[8 + k] = *reinterpret_cast<const unsigned*>(x + (size_t)(8 * tid + k) * D_ + d0);
  __builtin_amdgcn_sched_barrier(0);
  asm volatile("" :: "v"(r_ldt), "v"(r_lar), "v"(r_aim), "v"(r_cre), "v"(r_cim), "v"(r_dsk));

  {
    const float dt = rbf(expf(bf2f(r_ldt)));
    float are = -rbf(expf(bf2f(r_lar)));
    float aim = bf2f(r_aim);
    float lr = are * dt, li = aim * dt;
    float cr = bf2f(r_cre) * dt, ci = bf2f(r_cim) * dt;
    float mag = sqrtf(cr * cr + ci * ci);
    f32x4 pp;
    pp[0] = lr * 1.4426950408889634f;
    pp[1] = li * 0.15915494309189535f;
    pp[2] = log2f(mag);
    pp[3] = atan2f(ci, cr) * 0.15915494309189535f;
    *reinterpret_cast<f32x4*>(smem + PR_OFF + tid * 16) = pp;
  }
  RAW_BARRIER();

  {
    const int x = lane & 15, kk = lane >> 4;
    float th[3];
#pragma unroll
    for (int T = 0; T < 3; ++T) th[T] = thi_of(16 * T + x);
    const float tlo = (float)(8 * x);
#pragma unroll 1
    for (int c = 0; c < 2; ++c) {
      f32x4 acc[3];
#pragma unroll
      for (int T = 0; T < 3; ++T) acc[T] = (f32x4){0.f, 0.f, 0.f, 0.f};
#pragma unroll 2
      for (int g = 0; g < 8; ++g) {
        int n = 128 * c + 32 * wid + 4 * g + kk;
        f32x4 pp = *reinterpret_cast<const f32x4*>(smem + PR_OFF + n * 16);
        float qe = __builtin_amdgcn_exp2f(pp[0] * tlo);
        float qa = pp[1] * tlo;
        float qr = qe * __builtin_amdgcn_cosf(qa), qi = qe * __builtin_amdgcn_sinf(qa);
#pragma unroll
        for (int T = 0; T < 3; ++T) {
          float pe = __builtin_amdgcn_exp2f(pp[0] * th[T] + pp[2]);
          float pa = pp[1] * th[T] + pp[3];
          float pr = pe * __builtin_amdgcn_cosf(pa), pi = pe * __builtin_amdgcn_sinf(pa);
          acc[T] = __builtin_amdgcn_mfma_f32_16x16x4f32(qr, pr, acc[T], 0, 0, 0);
          acc[T] = __builtin_amdgcn_mfma_f32_16x16x4f32(-qi, pi, acc[T], 0, 0, 0);
        }
      }
      float* os = reinterpret_cast<float*>(smem + OS_OFF) + (4 * c + wid) * 768;
#pragma unroll
      for (int T = 0; T < 3; ++T) *reinterpret_cast<f32x4*>(os + (16 * T + x) * 16 + 4 * (kk ^ ((x >> 1) & 3))) = acc[T];
    }
  }
  RAW_BARRIER();

  {
    unsigned* tab = reinterpret_cast<unsigned*>(smem + TB_OFF);
#pragma unroll
    for (int c = 0; c < 2; ++c)
#pragma unroll
      for (int k = 0; k < 3; ++k) {
        int e = tid + 256 * k;
        const float* osp = reinterpret_cast<const float*>(smem + OS_OFF) + 3072 * c + (e ^ ((e >> 3) & 12));
        float kv = 2.0f * ((osp[0] + osp[768]) + (osp[1536] + osp[2304]));
        unsigned short hi = f2bf(kv);
        tab[768 * c + e] = (unsigned)hi | ((unsigned)f2bf(kv - bf2f(hi)) << 16);
      }
  }
  if (lane == 63) {
    *reinterpret_cast<u32x4*>(smem + EX_OFF + wid * 32) = (u32x4){sq[8], sq[9], sq[10], sq[11]};
    *reinterpret_cast<u32x4*>(smem + EX_OFF + wid * 32 + 16) = (u32x4){sq[12], sq[13], sq[14], sq[15]};
  }
#pragma unroll
  for (int k = 1; k < 8; ++k) sq[k] = __shfl_up(sq[8 + k], 1);
  __syncthreads();
  if (lane == 0) {
    u32x4 p0 = {0, 0, 0, 0}, p1 = {0, 0, 0, 0};
    if (wid > 0) {
      p0 = *reinterpret_cast<const u32x4*>(smem + EX_OFF + (wid - 1) * 32);
      p1 = *reinterpret_cast<const u32x4*>(smem + EX_OFF + (wid - 1) * 32 + 16);
    }
    sq[1] = p0[1]; sq[2] = p0[2]; sq[3] = p0[3];
    sq[4] = p1[0]; sq[5] = p1[1]; sq[6] = p1[2]; sq[7] = p1[3];
  }

  auto expand_k = [&](int c) {
    const unsigned* tab = reinterpret_cast<const unsigned*>(smem + TB_OFF) + 768 * c;
    unsigned w[8];
#pragma unroll
    for (int k = 0; k < 8; ++k) w[k] = tab[(kidx[k >> 1] >> (16 * (k & 1))) & 0xffff];
    u32x4 vh, vl;
#pragma unroll
    for (int j = 0; j < 4; ++j) {
      vh[j] = (w[7 - 2 * j] & 0xffffu) | (w[6 - 2 * j] << 16);
      vl[j] = (w[7 - 2 * j] >> 16) | (w[6 - 2 * j] & 0xffff0000u);
    }
    *reinterpret_cast<u32x4*>(smem + KH_OFF + (tid >> 2) * KROW + (tid & 3) * 16) = vh;
    *reinterpret_cast<u32x4*>(smem + KL_OFF + (tid >> 2) * KROW + (tid & 3) * 16) = vl;
  };
  expand_k(0);
  if (tid == 0) { u32x4 z = {0, 0, 0, 0}; *reinterpret_cast<u32x4*>(smem + KZ_OFF) = z; }

#pragma unroll 1
  for (int c = 0; c < 2; ++c) {
    const unsigned sel = c ? 0x07060302u : 0x05040100u;
#pragma unroll
    for (int e = 1; e <= 8; ++e) {
      u32x4 o;
#pragma unroll
      for (int j = 0; j < 4; ++j) o[j] = __builtin_amdgcn_perm(sq[e + 2 * j + 1], sq[e + 2 * j], sel);
      *reinterpret_cast<u32x4*>(smem + UC_OFF + (e - 1) * UC_STRIDE + 112 + tid * 16) = o;
    }
    if (tid >= 192 && (tid & 7) < 7) {
      u32x4 z = {0, 0, 0, 0};
      *reinterpret_cast<u32x4*>(smem + UC_OFF + ((tid >> 3) & 7) * UC_STRIDE + (tid & 7) * 16) = z;
    }
    __syncthreads();

    f32x16 y0, y1;
#pragma unroll
    for (int i = 0; i < 16; ++i) { y0[i] = 0.f; y1[i] = 0.f; }
    {
      const int r = lane & 31, h = lane >> 5;
      const int e = r & 7;
      const char* ubase = smem + UC_OFF + e * UC_STRIDE + 2 * (r - e + 56 - 8 * h);
#pragma unroll 2
      for (int dl = wid; dl < 32; dl += 4) {
        bf16x8 a0 = *reinterpret_cast<const bf16x8*>(ubase + 64 * dl);
        bf16x8 a1 = *reinterpret_cast<const bf16x8*>(ubase + 64 * dl - 32);
        bf16x8 a2 = *reinterpret_cast<const bf16x8*>(ubase + 64 * dl + 2048);
        bf16x8 a3 = *reinterpret_cast<const bf16x8*>(ubase + 64 * dl + 2048 - 32);
        const int m = r - dl;
        const int off = (m >= 0) ? (m * KROW + 16 * h) : (KZ_OFF - KH_OFF);
        const int off1 = (m >= 0) ? (off + 32) : off;
        const int lo = (m >= 0) ? KL_OFF : KH_OFF;
        bf16x8 fh0 = *reinterpret_cast<const bf16x8*>(smem + KH_OFF + off);
        bf16x8 fh1 = *reinterpret_cast<const bf16x8*>(smem + KH_OFF + off1);
        bf16x8 fl0 = *reinterpret_cast<const bf16x8*>(smem + lo + off);
        bf16x8 fl1 = *reinterpret_cast<const bf16x8*>(smem + lo + off1);
        const int go = (32 + m) * KROW + 16 * h;
        bf16x8 gh0 = *reinterpret_cast<const bf16x8*>(smem + KH_OFF + go);
        bf16x8 gh1 = *reinterpret_cast<const bf16x8*>(smem + KH_OFF + go + 32);
        bf16x8 gl0 = *reinterpret_cast<const bf16x8*>(smem + KL_OFF + go);
        bf16x8 gl1 = *reinterpret_cast<const bf16x8*>(smem + KL_OFF + go + 32);
        y0 = __builtin_amdgcn_mfma_f32_32x32x16_bf16(a0, fh0, y0, 0, 0, 0);
        y1 = __builtin_amdgcn_mfma_f32_32x32x16_bf16(a2, fh0, y1, 0, 0, 0);
        y0 = __builtin_amdgcn_mfma_f32_32x32x16_bf16(a1, fh1, y0, 0, 0, 0);
        y1 = __builtin_amdgcn_mfma_f32_32x32x16_bf16(a3, fh1, y1, 0, 0, 0);
        y0 = __builtin_amdgcn_mfma_f32_32x32x16_bf16(a0, fl0, y0, 0, 0, 0);
        y1 = __builtin_amdgcn_mfma_f32_32x32x16_bf16(a2, fl0, y1, 0, 0, 0);
        y0 = __builtin_amdgcn_mfma_f32_32x32x16_bf16(a1, fl1, y0, 0, 0, 0);
        y1 = __builtin_amdgcn_mfma_f32_32x32x16_bf16(a3, fl1, y1, 0, 0, 0);
        y1 = __builtin_amdgcn_mfma_f32_32x32x16_bf16(a0, gh0, y1, 0, 0, 0);
        y1 = __builtin_amdgcn_mfma_f32_32x32x16_bf16(a1, gh1, y1, 0, 0, 0);
        y1 = __builtin_amdgcn_mfma_f32_32x32x16_bf16(a0, gl0, y1, 0, 0, 0);
        y1 = __builtin_amdgcn_mfma_f32_32x32x16_bf16(a1, gl1, y1, 0, 0, 0);
      }
    }
    __syncthreads();
    {
      const int r = lane & 31, h = lane >> 5;
      float* slab = reinterpret_cast<float*>(smem + wid * 9216);
#pragma unroll
      for (int g = 0; g < 4; ++g) {
        f32x4 v0 = {y0[4 * g], y0[4 * g + 1], y0[4 * g + 2], y0[4 * g + 3]};
        f32x4 v1 = {y1[4 * g], y1[4 * g + 1], y1[4 * g + 2], y1[4 * g + 3]};
        int b = 8 * g + 4 * h;
        *reinterpret_cast<f32x4*>(slab + 36 * r + b) = v0;
        *reinterpret_cast<f32x4*>(slab + 36 * (32 + r) + b) = v1;
      }
    }
    if (c == 0) expand_k(1);
    __syncthreads();
    {
      const float dsk = bf2f((unsigned short)(r_dsk >> (16 * c)));
      const float* s = reinterpret_cast<const float*>(smem) + (tid >> 2) * 36 + (tid & 3) * 8;
      f32x4 ys[2];
#pragma unroll
      for (int hh = 0; hh < 2; ++hh) {
        f32x4 s0 = *reinterpret_cast<const f32x4*>(s + 4 * hh);
        f32x4 s1 = *reinterpret_cast<const f32x4*>(s + 2304 + 4 * hh);
        f32x4 s2 = *reinterpret_cast<const f32x4*>(s + 4608 + 4 * hh);
        f32x4 s3 = *reinterpret_cast<const f32x4*>(s + 6912 + 4 * hh);
        ys[hh] = (s0 + s1) + (s2 + s3);
      }
      if (c == 0) __syncthreads();
      unsigned short o[8];
#pragma unroll
      for (int k = 0; k < 8; ++k) {
        float yb = rbf(ys[k >> 2][k & 3]);
        float uu = bf2f((unsigned short)(sq[8 + k] >> (16 * c)));
        float sk = rbf(uu * dsk);
        o[k] = f2bf(yb + sk);
      }
      u32x4 v;
#pragma unroll
      for (int j = 0; j < 4; ++j) v[j] = o[2 * j] | ((unsigned)o[2 * j + 1] << 16);
      *reinterpret_cast<u32x4*>(aT + ((size_t)(tid >> 3) * D_ + d0 + c) * 64 + 8 * (tid & 7)) = v;
    }
  }
}

__global__ __launch_bounds__(256, 3) void k12(const unsigned short* __restrict__ x, const unsigned short* __restrict__ w1,
                                              const unsigned short* __restrict__ w2, const unsigned short* __restrict__ gw,
                                              const unsigned short* __restrict__ logAr, const unsigned short* __restrict__ Aim,
                                              const unsigned short* __restrict__ Cre, const unsigned short* __restrict__ Cim,
                                              const unsigned short* __restrict__ logdt, const unsigned short* __restrict__ Dsk,
                                              unsigned short* __restrict__ hbuf, unsigned short* __restrict__ w2T,
                                              float* __restrict__ gbuf, unsigned short* __restrict__ aT) {
  extern __shared__ __attribute__((aligned(16))) char smem[];
  const int b = blockIdx.x;
  const int xcd = b & 7, cu = (b >> 3) & 31, slot = b >> 8;
  if (slot == 0 || (slot == 1 && cu < 8)) conv_role(2 * (b & 511), x, logAr, Aim, Cre, Cim, logdt, Dsk, aT, smem);
  else if (slot == 1 || (cu >= 8 && cu < 12)) gemm1_role((slot == 1 ? cu - 8 : cu + 16) * 8 + xcd, x, w1, hbuf, smem);
  else if (cu >= 12) {
    const int hb = (cu - 12) * 8 + xcd;
    if (hb < N_GATE) gate_role(hb, x, gw, gbuf);
    else if (hb < N_GATE + N_W2) w2_role(hb - N_GATE, w2, w2T, smem);
  }
}

constexpr int G2_RS = 84;
__global__ __launch_bounds__(256, 1) void k3_gemm2(const unsigned short* __restrict__ hbuf, const unsigned short* __restrict__ w2T,
                                                   const unsigned short* __restrict__ aT, const float* __restrict__ gbuf,
                                                   unsigned short* __restrict__ out) {
  __shared__ __attribute__((aligned(16))) float red[4][64][G2_RS];
  __shared__ unsigned atile[80 * 33];
  const int tid = threadIdx.x, lane = tid & 63, wid = tid >> 6;
  int t = (blockIdx.x & 7) * 32 + (blockIdx.x >> 3);
  const int tm = t >> 3, tn = t & 7;
  const int m0 = tm * 64, n0 = tn * 80;
  const int fr = lane & 15, fq = lane >> 4;
  const int wv = __builtin_amdgcn_readfirstlane(wid);
  const int c0 = (wv * 27) >> 2;
  const int nch = (((wv + 1) * 27) >> 2) - c0;

  const unsigned short* ap = hbuf + (size_t)(m0 >> 4) * 54 * 512 + lane * 8 + c0 * 1024;
  const unsigned short* bp = w2T + (size_t)(n0 >> 4) * 54 * 512 + lane * 8 + c0 * 1024;

  f32x4 acc[4][5];
#pragma unroll
  for (int a = 0; a < 4; ++a)
#pragma unroll
    for (int c = 0; c < 5; ++c) acc[a][c] = (f32x4){0.f, 0.f, 0.f, 0.f};

  constexpr int PD = 2;
  bf16x8 A[PD][4][2], B[PD][5][2];
#define G2_LOAD(S, c)                                                                                  \
  {                                                                                                    \
    _Pragma("unroll") for (int a = 0; a < 4; ++a) {                                                    \
      A[S][a][0] = *reinterpret_cast<const bf16x8*>(ap + a * 54 * 512 + (c) * 1024);                   \
      A[S][a][1] = *reinterpret_cast<const bf16x8*>(ap + a * 54 * 512 + (c) * 1024 + 512);             \
    }                                                                                                  \
    _Pragma("unroll") for (int a = 0; a < 5; ++a) {                                                    \
      B[S][a][0] = *reinterpret_cast<const bf16x8*>(bp + a * 54 * 512 + (c) * 1024);                   \
      B[S][a][1] = *reinterpret_cast<const bf16x8*>(bp + a * 54 * 512 + (c) * 1024 + 512);             \
    }                                                                                                  \
  }
#define G2_COMP(S)                                                                                     \
  {                                                                                                    \
    _Pragma("unroll") for (int s = 0; s < 2; ++s)                                                      \
      _Pragma("unroll") for (int a = 0; a < 4; ++a)                                                    \
        _Pragma("unroll") for (int c = 0; c < 5; ++c)                                                  \
          acc[a][c] = __builtin_amdgcn_mfma_f32_16x16x32_bf16(A[S][a][s], B[S][c][s], acc[a][c], 0, 0, 0); \
  }
#pragma unroll
  for (int i = 0; i < PD - 1; ++i) {
    G2_LOAD(i, i);
    __builtin_amdgcn_sched_barrier(0);
  }
  u32x4 apre[3];
  float gpre[3];
#pragma unroll
  for (int j = 0; j < 3; ++j) {
    int idx = tid + 256 * j;
    if (idx > 639) idx = 639;
    apre[j] = *reinterpret_cast<const u32x4*>(aT + ((size_t)tm * D_ + n0) * 64 + idx * 8);
    gpre[j] = gbuf[m0 + idx / 10];
  }
  __builtin_amdgcn_sched_barrier(0);
#pragma unroll
  for (int i = 0; i < 7; ++i) {
    if (i + PD - 1 < 7) {
      const int cn = (i + PD - 1 < 6) ? (i + PD - 1) : (nch - 1);
      G2_LOAD((i + PD - 1) % PD, cn);
    }
    __builtin_amdgcn_sched_barrier(0);
    if (i == 6 && nch != 7) {
      const bf16x8 z = {0, 0, 0, 0, 0, 0, 0, 0};
#pragma unroll
      for (int a = 0; a < 4; ++a) { A[i % PD][a][0] = z; A[i % PD][a][1] = z; }
#pragma unroll
      for (int a = 0; a < 5; ++a) { B[i % PD][a][0] = z; B[i % PD][a][1] = z; }
    }
    G2_COMP(i % PD);
    __builtin_amdgcn_sched_barrier(0);
  }
#undef G2_LOAD
#undef G2_COMP

#pragma unroll
  for (int a = 0; a < 4; ++a)
#pragma unroll
    for (int c = 0; c < 5; ++c)
#pragma unroll
      for (int i = 0; i < 4; ++i) red[wid][16 * a + 4 * fq + i][16 * c + fr] = acc[a][c][i];
#pragma unroll
  for (int j = 0; j < 3; ++j) {
    const int idx = tid + 256 * j;
    if (idx < 640) {
#pragma unroll
      for (int q = 0; q < 4; ++q) atile[(idx >> 3) * 33 + (idx & 7) * 4 + q] = apre[j][q];
    }
  }
  __syncthreads();

#pragma unroll
  for (int j = 0; j < 3; ++j) {
    const int idx = tid + 256 * j;
    if (idx < 640) {
      const int row = idx / 10, cg = idx - row * 10;
      f32x4 s[2];
#pragma unroll
      for (int hh = 0; hh < 2; ++hh) {
        f32x4 s0 = *reinterpret_cast<const f32x4*>(&red[0][row][8 * cg + 4 * hh]);
        f32x4 s1 = *reinterpret_cast<const f32x4*>(&red[1][row][8 * cg + 4 * hh]);
        f32x4 s2 = *reinterpret_cast<const f32x4*>(&red[2][row][8 * cg + 4 * hh]);
        f32x4 s3 = *reinterpret_cast<const f32x4*>(&red[3][row][8 * cg + 4 * hh]);
        s[hh] = (s0 + s1) + (s2 + s3);
      }
      const float g = gpre[j];
      const float t2 = rbf(1.0f - g);
      const unsigned short* at = reinterpret_cast<const unsigned short*>(atile) + (8 * cg) * 66 + row;
      unsigned short o[8];
#pragma unroll
      for (int k = 0; k < 8; ++k) {
        float bv = rbf(s[k >> 2][k & 3]);
        float a = bf2f(at[k * 66]);
        float t1 = rbf(g * a);
        float t3 = rbf(t2 * bv);
        o[k] = f2bf(t1 + t3);
      }
      u32x4 v;
#pragma unroll
      for (int q = 0; q < 4; ++q) v[q] = o[2 * q] | ((unsigned)o[2 * q + 1] << 16);
      *reinterpret_cast<u32x4*>(out + (size_t)(m0 + row) * D_ + n0 + 8 * cg) = v;
    }
  }
}

extern "C" void kernel_launch(void* const* d_in, const int* in_sizes, int n_in, void* d_out, int out_size, void* d_ws,
                              size_t ws_size, hipStream_t stream) {
  const unsigned short* u = (const unsigned short*)d_in[0];
  const unsigned short* logAr = (const unsigned short*)d_in[1];
  const unsigned short* Aim = (const unsigned short*)d_in[2];
  const unsigned short* Cre = (const unsigned short*)d_in[3];
  const unsigned short* Cim = (const unsigned short*)d_in[4];
  const unsigned short* logdt = (const unsigned short*)d_in[5];
  const unsigned short* Dsk = (const unsigned short*)d_in[6];
  const unsigned short* w1 = (const unsigned short*)d_in[8];
  const unsigned short* w2 = (const unsigned short*)d_in[9];
  const unsigned short* gw = (const unsigned short*)d_in[10];
  unsigned short* out = (unsigned short*)d_out;

  char* ws = (char*)d_ws;
  unsigned short* hbuf = (unsigned short*)ws;
  unsigned short* aT = (unsigned short*)(ws + (size_t)L_ * F_ * 2);
  unsigned short* w2T = (unsigned short*)(ws + (size_t)L_ * F_ * 2 + (size_t)D_ * L_ * 2);
  float* gbuf = (float*)(ws + (size_t)L_ * F_ * 2 + (size_t)D_ * L_ * 2 + (size_t)D_ * F_ * 2);

  constexpr int K12_LDS = (CONV_LDS > 3 * G1_STAGE) ? CONV_LDS : 3 * G1_STAGE;
  hipLaunchKernelGGL(k12, dim3(K12_GRID), dim3(256), K12_LDS, stream, u, w1, w2, gw, logAr, Aim, Cre, Cim, logdt, Dsk,
                     hbuf, w2T, gbuf, aT);
  hipLaunchKernelGGL(k3_gemm2, dim3(256), dim3(256), 0, stream, hbuf, w2T, aT, gbuf, out);
}
```

```cpp
#include <hip/hip_runtime.h>
#include <hip/hip_bf16.h>
#include <stdint.h>

typedef __attribute__((ext_vector_type(8))) short bf16x8;
typedef __attribute__((ext_vector_type(4))) short bf16x4;
typedef __attribute__((ext_vector_type(16))) float f32x16;
typedef __attribute__((ext_vector_type(4))) float f32x4;
typedef __attribute__((ext_vector_type(4))) unsigned int u32x4;
typedef __attribute__((ext_vector_type(2))) unsigned int u32x2;

#define LDS_AS __attribute__((address_space(3)))
#define RAW_BARRIER() do { asm volatile("s_waitcnt lgkmcnt(0)" ::: "memory"); __builtin_amdgcn_s_barrier(); } while (0)

#define GST(P, V) asm volatile("global_store_dwordx4 %0, %1, off sc0 sc1\n\ts_nop 1" :: "v"(P), "v"(V) : "memory")
constexpr int L_ = 2048, D_ = 640, NS_ = 128, F_ = 1728;

__device__ __forceinline__ float bf2f(unsigned short v) { return __uint_as_float(((unsigned)v) << 16); }
__device__ __forceinline__ unsigned short f2bf(float f) {
  __bf16 b = (__bf16)f;
  return __builtin_bit_cast(unsigned short, b);
}
__device__ __forceinline__ float rbf(float f) { return bf2f(f2bf(f)); }

constexpr int G1_TM = L_ / 128, G1_TN = (F_ + 127) / 128;
constexpr int N_G1 = G1_TM * G1_TN;
constexpr int G1_STAGE = 16384;
constexpr int N_GATE = 32, N_W2 = 90;
constexpr int B_CONV = N_G1, B_GATE = B_CONV + D_, B_W2 = B_GATE + N_GATE, K12_GRID = B_W2 + N_W2;

__device__ __forceinline__ void tr_tiles_load(const unsigned short* __restrict__ src, int ss, int r0, int c0,
                                              u32x4 (&v)[2]) {
  const int tid = threadIdx.x;
#pragma unroll
  for (int i = 0; i < 2; ++i) {
    int id = tid + 256 * i, row = id >> 3, ch = id & 7;
    v[i] = *reinterpret_cast<const u32x4*>(src + (size_t)(r0 + row) * ss + c0 + ch * 8);
  }
}
__device__ __forceinline__ void tr_tiles_put(unsigned short (*tile)[72], const u32x4 (&v)[2]) {
  const int tid = threadIdx.x;
#pragma unroll
  for (int i = 0; i < 2; ++i) {
    int id = tid + 256 * i, row = id >> 3, ch = id & 7;
    *reinterpret_cast<u32x4*>(&tile[row][ch * 8]) = v[i];
  }
}
__device__ __forceinline__ void tr_tiles_store_blocked(unsigned short (*tile)[72], unsigned short* __restrict__ dst,
                                                       int r0, int c0) {
  const int tid = threadIdx.x;
#pragma unroll
  for (int i = 0; i < 2; ++i) {
    int id = tid + 256 * i, col = ((id >> 7) << 4) | (id & 15), ch = (id >> 4) & 7;
    unsigned short o[8];
#pragma unroll
    for (int k = 0; k < 8; ++k) o[k] = tile[ch * 8 + k][col];
    u32x4 w;
#pragma unroll
    for (int j = 0; j < 4; ++j) w[j] = o[2 * j] | ((unsigned)o[2 * j + 1] << 16);
    unsigned short* dp = dst + ((size_t)((c0 + col) >> 4) * 54 + ((r0 + ch * 8) >> 5)) * 512 + (id & 63) * 8;
    GST(dp, w);
  }
}

__device__ void gate_role(int gb, const unsigned short* __restrict__ x, const unsigned short* __restrict__ gw,
                          float* __restrict__ gbuf) {
  const int tid = threadIdx.x;
  const int r0 = gb * 64;
  u32x4 v[10][2];
#pragma unroll
  for (int t = 0; t < 10; ++t) tr_tiles_load(x, D_, r0, t * 64, v[t]);
  float s[2] = {0.f, 0.f};
#pragma unroll
  for (int t = 0; t < 10; ++t) {
    u32x4 g = *reinterpret_cast<const u32x4*>(gw + t * 64 + (tid & 7) * 8);
#pragma unroll
    for (int i = 0; i < 2; ++i)
#pragma unroll
      for (int j = 0; j < 4; ++j) {
        s[i] += rbf(__uint_as_float(v[t][i][j] << 16) * __uint_as_float(g[j] << 16));
        s[i] += rbf(__uint_as_float(v[t][i][j] & 0xffff0000u) * __uint_as_float(g[j] & 0xffff0000u));
      }
  }
#pragma unroll
  for (int i = 0; i < 2; ++i) {
    s[i] += __shfl_xor(s[i], 1); s[i] += __shfl_xor(s[i], 2); s[i] += __shfl_xor(s[i], 4);
    if ((tid & 7) == 0) gbuf[r0 + (tid >> 3) + 32 * i] = rbf(1.0f / (1.0f + expf(-rbf(s[i]))));
  }
}

__device__ void w2_role(int wb, const unsigned short* __restrict__ w2, unsigned short* __restrict__ w2T, char* smem) {
  u32x4 tv[3][2];
#pragma unroll
  for (int j = 0; j < 3; ++j) {
    int id = wb * 3 + j;
    tr_tiles_load(w2, D_, (id / 10) * 64, (id % 10) * 64, tv[j]);
  }
  typedef unsigned short tile_t[64][72];
  tile_t* tiles = reinterpret_cast<tile_t*>(smem);
#pragma unroll
  for (int j = 0; j < 3; ++j) tr_tiles_put(tiles[j], tv[j]);
  __syncthreads();
#pragma unroll
  for (int j = 0; j < 3; ++j) {
    int id = wb * 3 + j;
    tr_tiles_store_blocked(tiles[j], w2T, (id / 10) * 64, (id % 10) * 64);
  }
}

__device__ void gemm1_role(int b, const unsigned short* __restrict__ x, const unsigned short* __restrict__ w1,
                           unsigned short* __restrict__ hbuf, char* smem) {
  const int tid = threadIdx.x, lane = tid & 63, wid = tid >> 6;
  __builtin_amdgcn_s_setprio(3);
  int t = (b & 7) * (N_G1 / 8) + (b >> 3);
  const int tm = t / G1_TN, tn = t % G1_TN;
  const int m0 = tm * 128, n0 = tn * 128;
  const int wm = wid >> 1, wn = wid & 1, r = lane & 31, h = lane >> 5;

  const unsigned short* asrc[2];
  const unsigned short* bsrc[2];
#pragma unroll
  for (int i = 0; i < 2; ++i) {
    int row = (i * 4 + wid) * 16 + (lane >> 2);
    int c = (lane & 3) ^ ((row >> 2) & 3);
    asrc[i] = x + (size_t)(m0 + row) * D_ + c * 8;
    int k = (i * 4 + wid) * 4 + (lane >> 4);
    int cb = (lane & 15) ^ ((k & 3) << 2);
    int col = n0 + cb * 8;
    if (col > F_ - 8) col = F_ - 8;
    bsrc[i] = w1 + (size_t)k * F_ + col;
  }
  auto stage = [&](int kt, int buf) {
    char* sa = smem + buf * G1_STAGE;
    char* sb = sa + 8192;
#pragma unroll
    for (int i = 0; i < 2; ++i)
      __builtin_amdgcn_global_load_lds((const unsigned*)(asrc[i] + kt * 32), (LDS_AS unsigned*)(sa + (i * 4 + wid) * 1024), 16, 0, 0);
#pragma unroll
    for (int i = 0; i < 2; ++i)
      __builtin_amdgcn_global_load_lds((const unsigned*)(bsrc[i] + (size_t)kt * 32 * F_), (LDS_AS unsigned*)(sb + (i * 4 + wid) * 1024), 16, 0, 0);
  };

  f32x16 acc[2][2];
#pragma unroll
  for (int a = 0; a < 2; ++a)
#pragma unroll
    for (int c = 0; c < 2; ++c)
#pragma unroll
      for (int i = 0; i < 16; ++i) acc[a][c][i] = 0.f;

  const int q = (lane & 15) >> 2, p = lane & 3, blk = (lane >> 4) & 1;
  auto compute = [&](int buf) {
    const char* sa = smem + buf * G1_STAGE;
    const char* sb = sa + 8192;
#pragma unroll
    for (int s = 0; s < 2; ++s) {
      bf16x8 af[2], bfr[2];
#pragma unroll
      for (int mt = 0; mt < 2; ++mt) {
        int row = wm * 64 + mt * 32 + r;
        af[mt] = *reinterpret_cast<const bf16x8*>(sa + row * 64 + (((2 * s + h) ^ ((row >> 2) & 3)) << 4));
      }
#pragma unroll
      for (int nt = 0; nt < 2; ++nt)
#pragma unroll
        for (int tt = 0; tt < 2; ++tt) {
          int k = 16 * s + 8 * h + 4 * tt + q;
          int chunk = wn * 8 + nt * 4 + 2 * blk + (p >> 1);
          const char* ad = sb + k * 256 + ((chunk ^ (q << 2)) << 4) + (p & 1) * 8;
          bf16x4 v = __builtin_amdgcn_ds_read_tr16_b64_v4i16((LDS_AS bf16x4*)(ad));
          bfr[nt][4 * tt + 0] = v[0]; bfr[nt][4 * tt + 1] = v[1]; bfr[nt][4 * tt + 2] = v[2]; bfr[nt][4 * tt + 3] = v[3];
        }
#pragma unroll
      for (int mt = 0; mt < 2; ++mt)
#pragma unroll
        for (int nt = 0; nt < 2; ++nt)
          acc[mt][nt] = __builtin_amdgcn_mfma_f32_32x32x16_bf16(af[mt], bfr[nt], acc[mt][nt], 0, 0, 0);
    }
  };

  constexpr int NK = D_ / 32;
  stage(0, 0);
  stage(1, 1);
  int cur = 0, nxt = 2;
  for (int kt = 0; kt < NK - 1; ++kt) {
    asm volatile("s_waitcnt vmcnt(4)" ::: "memory");
    RAW_BARRIER();
    if (kt + 2 < NK) stage(kt + 2, nxt);
    compute(cur);
    cur = (cur == 2) ? 0 : cur + 1;
    nxt = (nxt == 2) ? 0 : nxt + 1;
  }
  asm volatile("s_waitcnt vmcnt(0)" ::: "memory");
  RAW_BARRIER();
  compute(cur);

  RAW_BARRIER();
  {
    unsigned short* ht = reinterpret_cast<unsigned short*>(smem);
#pragma unroll
    for (int nt = 0; nt < 2; ++nt)
#pragma unroll
      for (int mt = 0; mt < 2; ++mt)
#pragma unroll
        for (int i = 0; i < 16; ++i) {
          int row = wm * 64 + mt * 32 + (i & 3) + 8 * (i >> 2) + 4 * h;
          float hv = rbf(acc[mt][nt][i]);
          float sv = hv * __builtin_amdgcn_rcpf(1.0f + __expf(-hv));
          ht[row * 136 + wn * 64 + nt * 32 + r] = f2bf(sv);
        }
    __syncthreads();
#pragma unroll
    for (int i = 0; i < 8; ++i) {
      int id = tid + 256 * i, row = ((id >> 8) << 4) | (id & 15), ch = (id >> 4) & 15;
      if (n0 + ch * 8 < F_)
        { u32x4 hv = *reinterpret_cast<const u32x4*>(ht + row * 136 + ch * 8); GST(hbuf + ((size_t)((m0 + row) >> 4) * 54 + ((n0 + ch * 8) >> 5)) * 512 + (id & 63) * 8, hv); }
    }
  }
}

constexpr int UC_STRIDE = 4416;
constexpr int UC_OFF = 0;
constexpr int KROW = 80;
constexpr int KH_OFF = 8 * UC_STRIDE;
constexpr int KL_OFF = KH_OFF + 64 * KROW + 16;
constexpr int KZ_OFF = KH_OFF + 64 * KROW;
constexpr int PR_OFF = KH_OFF + 12288;
constexpr int OS_OFF = KH_OFF;
constexpr int TB_OFF = PR_OFF + 128 * 16;
constexpr int CONV_LDS = TB_OFF + 768 * 4;

__device__ __forceinline__ float thi_of(int i) {
  int t;
  if (i < 8) t = i;
  else if (i < 16) t = 128 + (i - 8);
  else if (i < 24) t = 256 + 128 * ((i - 16) >> 2) + 2 * ((i - 16) & 3);
  else if (i < 32) t = 512 + 128 * ((i - 24) >> 1) + 4 * ((i - 24) & 1);
  else if (i < 40) t = 1024 + 128 * (i - 32);
  else t = 2048;
  return (float)t;
}

struct KTab { unsigned short v[L_]; };
constexpr KTab make_ktab() {
  KTab t{};
  for (int j = 0; j < L_; ++j) {
    int sh = (j < 256) ? 0 : (j < 512) ? 1 : (j < 1024) ? 2 : 3;
    int rr = j;
    if (sh) {
      int q = j >> sh, rem = j & ((1 << sh) - 1), half = 1 << (sh - 1);
      if (rem > half || (rem == half && (q & 1))) ++q;
      rr = q << sh;
    }
    int i = 40;
    if (rr < 256) i = (rr & 7) + 8 * (rr >> 7);
    else if (rr < 512) i = 16 + 4 * ((rr - 256) >> 7) + ((rr & 7) >> 1);
    else if (rr < 1024) i = 24 + 2 * ((rr - 512) >> 7) + ((rr & 7) >> 2);
    else if (rr < 2048) i = 32 + ((rr - 1024) >> 7);
    t.v[j] = (unsigned short)(i * 16 + ((rr >> 3) & 15));
  }
  return t;
}
__device__ __attribute__((aligned(16))) const KTab KTAB = make_ktab();

__device__ void conv_role(int d, const unsigned short* __restrict__ x, const unsigned short* __restrict__ logAr,
                          const unsigned short* __restrict__ Aim, const unsigned short* __restrict__ Cre,
                          const unsigned short* __restrict__ Cim, const unsigned short* __restrict__ logdt,
                          const unsigned short* __restrict__ Dsk, unsigned short* __restrict__ aT, char* smem) {
  const int tid = threadIdx.x, lane = tid & 63, wid = tid >> 6;

  const int pn = tid & (NS_ - 1);
  const unsigned short r_ldt = logdt[d];
  const unsigned short r_lar = logAr[d * NS_ + pn], r_aim = Aim[d * NS_ + pn];
  const unsigned short r_cre = Cre[d * NS_ + pn], r_cim = Cim[d * NS_ + pn];
  const u32x4 kidx = *reinterpret_cast<const u32x4*>(KTAB.v + 8 * tid);
  __builtin_amdgcn_sched_barrier(0);
  unsigned short ug[8];
#pragma unroll
  for (int k = 0; k < 8; ++k) ug[k] = x[(size_t)(8 * tid + k) * D_ + d];
  __builtin_amdgcn_sched_barrier(0);
  asm volatile("" :: "v"(r_ldt), "v"(r_lar), "v"(r_aim), "v"(r_cre), "v"(r_cim));

  const float dt = rbf(expf(bf2f(r_ldt)));
  if (tid < NS_) {
    float are = -rbf(expf(bf2f(r_lar)));
    float aim = bf2f(r_aim);
    float lr = are * dt, li = aim * dt;
    float cr = bf2f(r_cre) * dt, ci = bf2f(r_cim) * dt;
    float mag = sqrtf(cr * cr + ci * ci);
    f32x4 pp;
    pp[0] = lr * 1.4426950408889634f;
    pp[1] = li * 0.15915494309189535f;
    pp[2] = log2f(mag);
    pp[3] = atan2f(ci, cr) * 0.15915494309189535f;
    *reinterpret_cast<f32x4*>(smem + PR_OFF + tid * 16) = pp;
  }
  if (tid < 64) {
    u32x4 z = {0, 0, 0, 0};
    *reinterpret_cast<u32x4*>(smem + UC_OFF + (tid >> 3) * UC_STRIDE + (tid & 7) * 16) = z;
  }
  RAW_BARRIER();

  {
    const int x = lane & 15, kk = lane >> 4;
    float th[3];
#pragma unroll
    for (int T = 0; T < 3; ++T) th[T] = thi_of(16 * T + x);
    const float tlo = (float)(8 * x);
    f32x4 acc[3];
#pragma unroll
    for (int T = 0; T < 3; ++T) acc[T] = (f32x4){0.f, 0.f, 0.f, 0.f};
#pragma unroll 2
    for (int g = 0; g < 8; ++g) {
      int n = 32 * wid + 4 * g + kk;
      f32x4 pp = *reinterpret_cast<const f32x4*>(smem + PR_OFF + n * 16);
      float qe = __builtin_amdgcn_exp2f(pp[0] * tlo);
      float qa = pp[1] * tlo;
      float qr = qe * __builtin_amdgcn_cosf(qa), qi = qe * __builtin_amdgcn_sinf(qa);
#pragma unroll
      for (int T = 0; T < 3; ++T) {
        float pe = __builtin_amdgcn_exp2f(pp[0] * th[T] + pp[2]);
        float pa = pp[1] * th[T] + pp[3];
        float pr = pe * __builtin_amdgcn_cosf(pa), pi = pe * __builtin_amdgcn_sinf(pa);
        acc[T] = __builtin_amdgcn_mfma_f32_16x16x4f32(qr, pr, acc[T], 0, 0, 0);
        acc[T] = __builtin_amdgcn_mfma_f32_16x16x4f32(-qi, pi, acc[T], 0, 0, 0);
      }
    }
    float* os = reinterpret_cast<float*>(smem + OS_OFF) + wid * 768;
#pragma unroll
    for (int T = 0; T < 3; ++T) *reinterpret_cast<f32x4*>(os + (16 * T + x) * 16 + 4 * (kk ^ ((x >> 1) & 3))) = acc[T];
  }
  RAW_BARRIER();

  u32x4 uvec;
#pragma unroll
  for (int j = 0; j < 4; ++j) uvec[j] = (unsigned)ug[2 * j] | ((unsigned)ug[2 * j + 1] << 16);
  *reinterpret_cast<u32x4*>(smem + UC_OFF + 7 * UC_STRIDE + 112 + tid * 16) = uvec;
  {
    unsigned* tab = reinterpret_cast<unsigned*>(smem + TB_OFF);
#pragma unroll
    for (int k = 0; k < 3; ++k) {
      int e = tid + 256 * k;
      const float* osp = reinterpret_cast<const float*>(smem + OS_OFF) + (e ^ ((e >> 3) & 12));
      float kv = 2.0f * ((osp[0] + osp[768]) + (osp[1536] + osp[2304]));
      unsigned short hi = f2bf(kv);
      tab[e] = (unsigned)hi | ((unsigned)f2bf(kv - bf2f(hi)) << 16);
    }
  }
  __syncthreads();
  {
    u32x4 v1 = {0, 0, 0, 0};
    if (tid < 255) v1 = *reinterpret_cast<const u32x4*>(smem + UC_OFF + 7 * UC_STRIDE + 112 + (tid + 1) * 16);
    unsigned w[8] = {uvec[0], uvec[1], uvec[2], uvec[3], v1[0], v1[1], v1[2], v1[3]};
#pragma unroll
    for (int e = 1; e < 8; ++e) {
      u32x4 o;
#pragma unroll
      for (int j = 0; j < 4; ++j) {
        int base = j + (e >> 1);
        if (e & 1) o[j] = (w[base] >> 16) | (w[base + 1] << 16);
        else o[j] = w[base];
      }
      *reinterpret_cast<u32x4*>(smem + UC_OFF + (e - 1) * UC_STRIDE + 128 + tid * 16) = o;
    }
    if (tid == 0) {
#pragma unroll
      for (int e = 1; e < 8; ++e)
#pragma unroll
        for (int k = 0; k < e; ++k)
          *reinterpret_cast<unsigned short*>(smem + UC_OFF + (e - 1) * UC_STRIDE + 2 * (64 - e + k)) =
              (unsigned short)(w[k >> 1] >> (16 * (k & 1)));
    }
  }
  {
    const unsigned* tab = reinterpret_cast<const unsigned*>(smem + TB_OFF);
    unsigned w[8];
#pragma unroll
    for (int k = 0; k < 8; ++k) w[k] = tab[(kidx[k >> 1] >> (16 * (k & 1))) & 0xffff];
    u32x4 vh, vl;
#pragma unroll
    for (int j = 0; j < 4; ++j) {
      vh[j] = (w[7 - 2 * j] & 0xffffu) | (w[6 - 2 * j] << 16);
      vl[j] = (w[7 - 2 * j] >> 16) | (w[6 - 2 * j] & 0xffff0000u);
    }
    *reinterpret_cast<u32x4*>(smem + KH_OFF + (tid >> 2) * KROW + (tid & 3) * 16) = vh;
    *reinterpret_cast<u32x4*>(smem + KL_OFF + (tid >> 2) * KROW + (tid & 3) * 16) = vl;
    if (tid == 0) { u32x4 z = {0, 0, 0, 0}; *reinterpret_cast<u32x4*>(smem + KZ_OFF) = z; }
  }
  __syncthreads();

  f32x16 y0, y1;
#pragma unroll
  for (int i = 0; i < 16; ++i) { y0[i] = 0.f; y1[i] = 0.f; }
  {
    const int r = lane & 31, h = lane >> 5;
    const int e = r & 7;
    const char* ubase = smem + UC_OFF + e * UC_STRIDE + 2 * (r - e + 56 - 8 * h);
#pragma unroll 2
    for (int dl = wid; dl < 32; dl += 4) {
      bf16x8 a0 = *reinterpret_cast<const bf16x8*>(ubase + 64 * dl);
      bf16x8 a1 = *reinterpret_cast<const bf16x8*>(ubase + 64 * dl - 32);
      bf16x8 a2 = *reinterpret_cast<const bf16x8*>(ubase + 64 * dl + 2048);
      bf16x8 a3 = *reinterpret_cast<const bf16x8*>(ubase + 64 * dl + 2048 - 32);
      const int m = r - dl;
      const int off = (m >= 0) ? (m * KROW + 16 * h) : (KZ_OFF - KH_OFF);
      const int off1 = (m >= 0) ? (off + 32) : off;
      const int lo = (m >= 0) ? KL_OFF : KH_OFF;
      bf16x8 fh0 = *reinterpret_cast<const bf16x8*>(smem + KH_OFF + off);
      bf16x8 fh1 = *reinterpret_cast<const bf16x8*>(smem + KH_OFF + off1);
      bf16x8 fl0 = *reinterpret_cast<const bf16x8*>(smem + lo + off);
      bf16x8 fl1 = *reinterpret_cast<const bf16x8*>(smem + lo + off1);
      const int go = (32 + m) * KROW + 16 * h;
      bf16x8 gh0 = *reinterpret_cast<const bf16x8*>(smem + KH_OFF + go);
      bf16x8 gh1 = *reinterpret_cast<const bf16x8*>(smem + KH_OFF + go + 32);
      bf16x8 gl0 = *reinterpret_cast<const bf16x8*>(smem + KL_OFF + go);
      bf16x8 gl1 = *reinterpret_cast<const bf16x8*>(smem + KL_OFF + go + 32);
      y0 = __builtin_amdgcn_mfma_f32_32x32x16_bf16(a0, fh0, y0, 0, 0, 0);
      y1 = __builtin_amdgcn_mfma_f32_32x32x16_bf16(a2, fh0, y1, 0, 0, 0);
      y0 = __builtin_amdgcn_mfma_f32_32x32x16_bf16(a1, fh1, y0, 0, 0, 0);
      y1 = __builtin_amdgcn_mfma_f32_32x32x16_bf16(a3, fh1, y1, 0, 0, 0);
      y0 = __builtin_amdgcn_mfma_f32_32x32x16_bf16(a0, fl0, y0, 0, 0, 0);
      y1 = __builtin_amdgcn_mfma_f32_32x32x16_bf16(a2, fl0, y1, 0, 0, 0);
      y0 = __builtin_amdgcn_mfma_f32_32x32x16_bf16(a1, fl1, y0, 0, 0, 0);
      y1 = __builtin_amdgcn_mfma_f32_32x32x16_bf16(a3, fl1, y1, 0, 0, 0);
      y1 = __builtin_amdgcn_mfma_f32_32x32x16_bf16(a0, gh0, y1, 0, 0, 0);
      y1 = __builtin_amdgcn_mfma_f32_32x32x16_bf16(a1, gh1, y1, 0, 0, 0);
      y1 = __builtin_amdgcn_mfma_f32_32x32x16_bf16(a0, gl0, y1, 0, 0, 0);
      y1 = __builtin_amdgcn_mfma_f32_32x32x16_bf16(a1, gl1, y1, 0, 0, 0);
    }
  }
  __syncthreads();
  {
    const int r = lane & 31, h = lane >> 5;
    float* slab = reinterpret_cast<float*>(smem + wid * 9216);
#pragma unroll
    for (int g = 0; g < 4; ++g) {
      f32x4 v0 = {y0[4 * g], y0[4 * g + 1], y0[4 * g + 2], y0[4 * g + 3]};
      f32x4 v1 = {y1[4 * g], y1[4 * g + 1], y1[4 * g + 2], y1[4 * g + 3]};
      int b = 8 * g + 4 * h;
      *reinterpret_cast<f32x4*>(slab + 36 * r + b) = v0;
      *reinterpret_cast<f32x4*>(slab + 36 * (32 + r) + b) = v1;
    }
  }
  __syncthreads();
  {
    const float dsk = bf2f(Dsk[d]);
    const float* s = reinterpret_cast<const float*>(smem) + (tid >> 2) * 36 + (tid & 3) * 8;
    f32x4 ys[2];
#pragma unroll
    for (int hh = 0; hh < 2; ++hh) {
      f32x4 s0 = *reinterpret_cast<const f32x4*>(s + 4 * hh);
      f32x4 s1 = *reinterpret_cast<const f32x4*>(s + 2304 + 4 * hh);
      f32x4 s2 = *reinterpret_cast<const f32x4*>(s + 4608 + 4 * hh);
      f32x4 s3 = *reinterpret_cast<const f32x4*>(s + 6912 + 4 * hh);
      ys[hh] = (s0 + s1) + (s2 + s3);
    }
    unsigned short o[8];
#pragma unroll
    for (int k = 0; k < 8; ++k) {
      float yb = rbf(ys[k >> 2][k & 3]);
      float uu = bf2f((unsigned short)(uvec[k >> 1] >> (16 * (k & 1))));
      float sk = rbf(uu * dsk);
      o[k] = f2bf(yb + sk);
    }
    u32x4 v;
#pragma unroll
    for (int j = 0; j < 4; ++j) v[j] = o[2 * j] | ((unsigned)o[2 * j + 1] << 16);
    GST(aT + ((size_t)(tid >> 3) * D_ + d) * 64 + 8 * (tid & 7), v);
  }
}

__global__ __launch_bounds__(256, 3) void k12(const unsigned short* __restrict__ x, const unsigned short* __restrict__ w1,
                                              const unsigned short* __restrict__ w2, const unsigned short* __restrict__ gw,
                                              const unsigned short* __restrict__ logAr, const unsigned short* __restrict__ Aim,
                                              const unsigned short* __restrict__ Cre, const unsigned short* __restrict__ Cim,
                                              const unsigned short* __restrict__ logdt, const unsigned short* __restrict__ Dsk,
                                              unsigned short* __restrict__ hbuf, unsigned short* __restrict__ w2T,
                                              float* __restrict__ gbuf, unsigned short* __restrict__ aT) {
  extern __shared__ __attribute__((aligned(16))) char smem[];
  const int b = blockIdx.x;
  constexpr int G0 = 0;
  if (b >= G0 && b < G0 + D_) conv_role(b - G0, x, logAr, Aim, Cre, Cim, logdt, Dsk, aT, smem);
  else if (b < B_GATE) gemm1_role(b < G0 ? b : b - D_, x, w1, hbuf, smem);
  else if (b < B_W2) gate_role(b - B_GATE, x, gw, gbuf);
  else w2_role(b - B_W2, w2, w2T, smem);
}

constexpr int G2_RS = 84;
__global__ __launch_bounds__(256, 1) void k3_gemm2(const unsigned short* __restrict__ hbuf, const unsigned short* __restrict__ w2T,
                                                   const unsigned short* __restrict__ aT, const float* __restrict__ gbuf,
                                                   unsigned short* __restrict__ out) {
  __shared__ __attribute__((aligned(16))) float red[4][64][G2_RS];
  __shared__ unsigned atile[80 * 33];
  const int tid = threadIdx.x, lane = tid & 63, wid = tid >> 6;
  int t = (blockIdx.x & 7) * 32 + (blockIdx.x >> 3);
  const int tm = t >> 3, tn = t & 7;
  const int m0 = tm * 64, n0 = tn * 80;
  const int fr = lane & 15, fq = lane >> 4;
  const int wv = __builtin_amdgcn_readfirstlane(wid);
  const int c0 = (wv * 27) >> 2;
  const int nch = (((wv + 1) * 27) >> 2) - c0;

  const unsigned short* ap = hbuf + (size_t)(m0 >> 4) * 54 * 512 + lane * 8 + c0 * 1024;
  const unsigned short* bp = w2T + (size_t)(n0 >> 4) * 54 * 512 + lane * 8 + c0 * 1024;

  f32x4 acc[4][5];
#pragma unroll
  for (int a = 0; a < 4; ++a)
#pragma unroll
    for (int c = 0; c < 5; ++c) acc[a][c] = (f32x4){0.f, 0.f, 0.f, 0.f};

  constexpr int PD = 2;
  bf16x8 A[PD][4][2], B[PD][5][2];
#define G2_LOAD(S, c)                                                                                  \
  {                                                                                                    \
    _Pragma("unroll") for (int a = 0; a < 4; ++a) {                                                    \
      A[S][a][0] = *reinterpret_cast<const bf16x8*>(ap + a * 54 * 512 + (c) * 1024);                   \
      A[S][a][1] = *reinterpret_cast<const bf16x8*>(ap + a * 54 * 512 + (c) * 1024 + 512);             \
    }                                                                                                  \
    _Pragma("unroll") for (int a = 0; a < 5; ++a) {                                                    \
      B[S][a][0] = *reinterpret_cast<const bf16x8*>(bp + a * 54 * 512 + (c) * 1024);                   \
      B[S][a][1] = *reinterpret_cast<const bf16x8*>(bp + a * 54 * 512 + (c) * 1024 + 512);             \
    }                                                                                                  \
  }
#define G2_COMP(S)                                                                                     \
  {                                                                                                    \
    _Pragma("unroll") for (int s = 0; s < 2; ++s)                                                      \
      _Pragma("unroll") for (int a = 0; a < 4; ++a)                                                    \
        _Pragma("unroll") for (int c = 0; c < 5; ++c)                                                  \
          acc[a][c] = __builtin_amdgcn_mfma_f32_16x16x32_bf16(A[S][a][s], B[S][c][s], acc[a][c], 0, 0, 0); \
  }
#pragma unroll
  for (int i = 0; i < PD - 1; ++i) {
    G2_LOAD(i, i);
    __builtin_amdgcn_sched_barrier(0);
  }
  u32x4 apre[3];
  float gpre[3];
#pragma unroll
  for (int j = 0; j < 3; ++j) {
    int idx = tid + 256 * j;
    if (idx > 639) idx = 639;
    apre[j] = *reinterpret_cast<const u32x4*>(aT + ((size_t)tm * D_ + n0) * 64 + idx * 8);
    gpre[j] = gbuf[m0 + idx / 10];
  }
  __builtin_amdgcn_sched_barrier(0);
#pragma unroll
  for (int i = 0; i < 7; ++i) {
    if (i + PD - 1 < 7) {
      const int cn = (i + PD - 1 < 6) ? (i + PD - 1) : (nch - 1);
      G2_LOAD((i + PD - 1) % PD, cn);
    }
    __builtin_amdgcn_sched_barrier(0);
    if (i == 6 && nch != 7) {
      const bf16x8 z = {0, 0, 0, 0, 0, 0, 0, 0};
#pragma unroll
      for (int a = 0; a < 4; ++a) { A[i % PD][a][0] = z; A[i % PD][a][1] = z; }
#pragma unroll
      for (int a = 0; a < 5; ++a) { B[i % PD][a][0] = z; B[i % PD][a][1] = z; }
    }
    G2_COMP(i % PD);
    __builtin_amdgcn_sched_barrier(0);
  }
#undef G2_LOAD
#undef G2_COMP

#pragma unroll
  for (int a = 0; a < 4; ++a)
#pragma unroll
    for (int c = 0; c < 5; ++c)
#pragma unroll
      for (int i = 0; i < 4; ++i) red[wid][16 * a + 4 * fq + i][16 * c + fr] = acc[a][c][i];
#pragma unroll
  for (int j = 0; j < 3; ++j) {
    const int idx = tid + 256 * j;
    if (idx < 640) {
#pragma unroll
      for (int q = 0; q < 4; ++q) atile[(idx >> 3) * 33 + (idx & 7) * 4 + q] = apre[j][q];
    }
  }
  __syncthreads();

#pragma unroll
  for (int j = 0; j < 3; ++j) {
    const int idx = tid + 256 * j;
    if (idx < 640) {
      const int row = idx / 10, cg = idx - row * 10;
      f32x4 s[2];
#pragma unroll
      for (int hh = 0; hh < 2; ++hh) {
        f32x4 s0 = *reinterpret_cast<const f32x4*>(&red[0][row][8 * cg + 4 * hh]);
        f32x4 s1 = *reinterpret_cast<const f32x4*>(&red[1][row][8 * cg + 4 * hh]);
        f32x4 s2 = *reinterpret_cast<const f32x4*>(&red[2][row][8 * cg + 4 * hh]);
        f32x4 s3 = *reinterpret_cast<const f32x4*>(&red[3][row][8 * cg + 4 * hh]);
        s[hh] = (s0 + s1) + (s2 + s3);
      }
      const float g = gpre[j];
      const float t2 = rbf(1.0f - g);
      const unsigned short* at = reinterpret_cast<const unsigned short*>(atile) + (8 * cg) * 66 + row;
      unsigned short o[8];
#pragma unroll
      for (int k = 0; k < 8; ++k) {
        float bv = rbf(s[k >> 2][k & 3]);
        float a = bf2f(at[k * 66]);
        float t1 = rbf(g * a);
        float t3 = rbf(t2 * bv);
        o[k] = f2bf(t1 + t3);
      }
      u32x4 v;
#pragma unroll
      for (int q = 0; q < 4; ++q) v[q] = o[2 * q] | ((unsigned)o[2 * q + 1] << 16);
      *reinterpret_cast<u32x4*>(out + (size_t)(m0 + row) * D_ + n0 + 8 * cg) = v;
    }
  }
}

extern "C" void kernel_launch(void* const* d_in, const int* in_sizes, int n_in, void* d_out, int out_size, void* d_ws,
                              size_t ws_size, hipStream_t stream) {
  const unsigned short* u = (const unsigned short*)d_in[0];
  const unsigned short* logAr = (const unsigned short*)d_in[1];
  const unsigned short* Aim = (const unsigned short*)d_in[2];
  const unsigned short* Cre = (const unsigned short*)d_in[3];
  const unsigned short* Cim = (const unsigned short*)d_in[4];
  const unsigned short* logdt = (const unsigned short*)d_in[5];
  const unsigned short* Dsk = (const unsigned short*)d_in[6];
  const unsigned short* w1 = (const unsigned short*)d_in[8];
  const unsigned short* w2 = (const unsigned short*)d_in[9];
  const unsigned short* gw = (const unsigned short*)d_in[10];
  unsigned short* out = (unsigned short*)d_out;

  char* ws = (char*)d_ws;
  unsigned short* hbuf = (unsigned short*)ws;
  unsigned short* aT = (unsigned short*)(ws + (size_t)L_ * F_ * 2);
  unsigned short* w2T = (unsigned short*)(ws + (size_t)L_ * F_ * 2 + (size_t)D_ * L_ * 2);
  float* gbuf = (float*)(ws + (size_t)L_ * F_ * 2 + (size_t)D_ * L_ * 2 + (size_t)D_ * F_ * 2);

  constexpr int K12_LDS = (CONV_LDS > 3 * G1_STAGE) ? CONV_LDS : 3 * G1_STAGE;
  hipLaunchKernelGGL(k12, dim3(K12_GRID), dim3(256), K12_LDS, stream, u, w1, w2, gw, logAr, Aim, Cre, Cim, logdt, Dsk,
                     hbuf, w2T, gbuf, aT);
  hipLaunchKernelGGL(k3_gemm2, dim3(256), dim3(256), 0, stream, hbuf, w2T, aT, gbuf, out);
}
```

```cpp
#include <hip/hip_runtime.h>
#include <hip/hip_bf16.h>
#include <stdint.h>

typedef __attribute__((ext_vector_type(8))) short bf16x8;
typedef __attribute__((ext_vector_type(4))) short bf16x4;
typedef __attribute__((ext_vector_type(16))) float f32x16;
typedef __attribute__((ext_vector_type(4))) float f32x4;
typedef __attribute__((ext_vector_type(2))) float f32x2;
typedef __attribute__((ext_vector_type(2))) __bf16 bf16x2n;
typedef __attribute__((ext_vector_type(4))) unsigned int u32x4;
typedef __attribute__((ext_vector_type(2))) unsigned int u32x2;

#define LDS_AS __attribute__((address_space(3)))
#define RAW_BARRIER() do { asm volatile("s_waitcnt lgkmcnt(0)" ::: "memory"); __builtin_amdgcn_s_barrier(); } while (0)

#define GST(P, V) asm volatile("global_store_dwordx4 %0, %1, off sc0 sc1\n\ts_nop 1" :: "v"(P), "v"(V) : "memory")
constexpr int L_ = 2048, D_ = 640, NS_ = 128, F_ = 1728;

__device__ __forceinline__ float bf2f(unsigned short v) { return __uint_as_float(((unsigned)v) << 16); }
__device__ __forceinline__ unsigned short f2bf(float f) {
  __bf16 b = (__bf16)f;
  return __builtin_bit_cast(unsigned short, b);
}
__device__ __forceinline__ float rbf(float f) { return bf2f(f2bf(f)); }

constexpr int G1_TM = L_ / 128, G1_TN = (F_ + 127) / 128;
constexpr int N_G1 = G1_TM * G1_TN;
constexpr int G1_STAGE = 16384;
constexpr int N_GATE = 32, N_W2 = 90;
constexpr int B_CONV = N_G1, B_GATE = B_CONV + D_, B_W2 = B_GATE + N_GATE, K12_GRID = B_W2 + N_W2;

__device__ __forceinline__ void tr_tiles_load(const unsigned short* __restrict__ src, int ss, int r0, int c0,
                                              u32x4 (&v)[2]) {
  const int tid = threadIdx.x;
#pragma unroll
  for (int i = 0; i < 2; ++i) {
    int id = tid + 256 * i, row = id >> 3, ch = id & 7;
    v[i] = *reinterpret_cast<const u32x4*>(src + (size_t)(r0 + row) * ss + c0 + ch * 8);
  }
}
__device__ __forceinline__ void tr_tiles_put(unsigned short (*tile)[72], const u32x4 (&v)[2]) {
  const int tid = threadIdx.x;
#pragma unroll
  for (int i = 0; i < 2; ++i) {
    int id = tid + 256 * i, row = id >> 3, ch = id & 7;
    *reinterpret_cast<u32x4*>(&tile[row][ch * 8]) = v[i];
  }
}
__device__ __forceinline__ void tr_tiles_store_blocked(unsigned short (*tile)[72], unsigned short* __restrict__ dst,
                                                       int r0, int c0) {
  const int tid = threadIdx.x;
#pragma unroll
  for (int i = 0; i < 2; ++i) {
    int id = tid + 256 * i, col = ((id >> 7) << 4) | (id & 15), ch = (id >> 4) & 7;
    unsigned short o[8];
#pragma unroll
    for (int k = 0; k < 8; ++k) o[k] = tile[ch * 8 + k][col];
    u32x4 w;
#pragma unroll
    for (int j = 0; j < 4; ++j) w[j] = o[2 * j] | ((unsigned)o[2 * j + 1] << 16);
    unsigned short* dp = dst + ((size_t)((c0 + col) >> 4) * 54 + ((r0 + ch * 8) >> 5)) * 512 + (id & 63) * 8;
    GST(dp, w);
  }
}

__device__ void gate_role(int gb, const unsigned short* __restrict__ x, const unsigned short* __restrict__ gw,
                          float* __restrict__ gbuf) {
  const int tid = threadIdx.x;
  const int r0 = gb * 64;
  u32x4 v[10][2];
#pragma unroll
  for (int t = 0; t < 10; ++t) tr_tiles_load(x, D_, r0, t * 64, v[t]);
  float s[2] = {0.f, 0.f};
#pragma unroll
  for (int t = 0; t < 10; ++t) {
    u32x4 g = *reinterpret_cast<const u32x4*>(gw + t * 64 + (tid & 7) * 8);
#pragma unroll
    for (int i = 0; i < 2; ++i)
#pragma unroll
      for (int j = 0; j < 4; ++j) {
        s[i] += rbf(__uint_as_float(v[t][i][j] << 16) * __uint_as_float(g[j] << 16));
        s[i] += rbf(__uint_as_float(v[t][i][j] & 0xffff0000u) * __uint_as_float(g[j] & 0xffff0000u));
      }
  }
#pragma unroll
  for (int i = 0; i < 2; ++i) {
    s[i] += __shfl_xor(s[i], 1); s[i] += __shfl_xor(s[i], 2); s[i] += __shfl_xor(s[i], 4);
    if ((tid & 7) == 0) gbuf[r0 + (tid >> 3) + 32 * i] = rbf(1.0f / (1.0f + expf(-rbf(s[i]))));
  }
}

__device__ void w2_role(int wb, const unsigned short* __restrict__ w2, unsigned short* __restrict__ w2T, char* smem) {
  u32x4 tv[3][2];
#pragma unroll
  for (int j = 0; j < 3; ++j) {
    int id = wb * 3 + j;
    tr_tiles_load(w2, D_, (id / 10) * 64, (id % 10) * 64, tv[j]);
  }
  typedef unsigned short tile_t[64][72];
  tile_t* tiles = reinterpret_cast<tile_t*>(smem);
#pragma unroll
  for (int j = 0; j < 3; ++j) tr_tiles_put(tiles[j], tv[j]);
  __syncthreads();
#pragma unroll
  for (int j = 0; j < 3; ++j) {
    int id = wb * 3 + j;
    tr_tiles_store_blocked(tiles[j], w2T, (id / 10) * 64, (id % 10) * 64);
  }
}

__device__ void gemm1_role(int b, const unsigned short* __restrict__ x, const unsigned short* __restrict__ w1,
                           unsigned short* __restrict__ hbuf, char* smem) {
  const int tid = threadIdx.x, lane = tid & 63, wid = tid >> 6;
  __builtin_amdgcn_s_setprio(3);
  int t = (b & 7) * (N_G1 / 8) + (b >> 3);
  const int tm = t / G1_TN, tn = t % G1_TN;
  const int m0 = tm * 128, n0 = tn * 128;
  const int wm = wid >> 1, wn = wid & 1, r = lane & 31, h = lane >> 5;

  const unsigned short* asrc[2];
  const unsigned short* bsrc[2];
#pragma unroll
  for (int i = 0; i < 2; ++i) {
    int row = (i * 4 + wid) * 16 + (lane >> 2);
    int c = (lane & 3) ^ ((row >> 2) & 3);
    asrc[i] = x + (size_t)(m0 + row) * D_ + c * 8;
    int k = (i * 4 + wid) * 4 + (lane >> 4);
    int cb = (lane & 15) ^ ((k & 3) << 2);
    int col = n0 + cb * 8;
    if (col > F_ - 8) col = F_ - 8;
    bsrc[i] = w1 + (size_t)k * F_ + col;
  }
  auto stage = [&](int kt, int buf) {
    char* sa = smem + buf * G1_STAGE;
    char* sb = sa + 8192;
#pragma unroll
    for (int i = 0; i < 2; ++i)
      __builtin_amdgcn_global_load_lds((const unsigned*)(asrc[i] + kt * 32), (LDS_AS unsigned*)(sa + (i * 4 + wid) * 1024), 16, 0, 0);
#pragma unroll
    for (int i = 0; i < 2; ++i)
      __builtin_amdgcn_global_load_lds((const unsigned*)(bsrc[i] + (size_t)kt * 32 * F_), (LDS_AS unsigned*)(sb + (i * 4 + wid) * 1024), 16, 0, 0);
  };

  f32x16 acc[2][2];
#pragma unroll
  for (int a = 0; a < 2; ++a)
#pragma unroll
    for (int c = 0; c < 2; ++c)
#pragma unroll
      for (int i = 0; i < 16; ++i) acc[a][c][i] = 0.f;

  const int q = (lane & 15) >> 2, p = lane & 3, blk = (lane >> 4) & 1;
  auto compute = [&](int buf) {
    const char* sa = smem + buf * G1_STAGE;
    const char* sb = sa + 8192;
#pragma unroll
    for (int s = 0; s < 2; ++s) {
      bf16x8 af[2], bfr[2];
#pragma unroll
      for (int mt = 0; mt < 2; ++mt) {
        int row = wm * 64 + mt * 32 + r;
        af[mt] = *reinterpret_cast<const bf16x8*>(sa + row * 64 + (((2 * s + h) ^ ((row >> 2) & 3)) << 4));
      }
#pragma unroll
      for (int nt = 0; nt < 2; ++nt)
#pragma unroll
        for (int tt = 0; tt < 2; ++tt) {
          int k = 16 * s + 8 * h + 4 * tt + q;
          int chunk = wn * 8 + nt * 4 + 2 * blk + (p >> 1);
          const char* ad = sb + k * 256 + ((chunk ^ (q << 2)) << 4) + (p & 1) * 8;
          bf16x4 v = __builtin_amdgcn_ds_read_tr16_b64_v4i16((LDS_AS bf16x4*)(ad));
          bfr[nt][4 * tt + 0] = v[0]; bfr[nt][4 * tt + 1] = v[1]; bfr[nt][4 * tt + 2] = v[2]; bfr[nt][4 * tt + 3] = v[3];
        }
#pragma unroll
      for (int mt = 0; mt < 2; ++mt)
#pragma unroll
        for (int nt = 0; nt < 2; ++nt)
          acc[mt][nt] = __builtin_amdgcn_mfma_f32_32x32x16_bf16(bfr[nt], af[mt], acc[mt][nt], 0, 0, 0);
    }
  };

  constexpr int NK = D_ / 32;
  stage(0, 0);
  stage(1, 1);
  int cur = 0, nxt = 2;
  for (int kt = 0; kt < NK - 1; ++kt) {
    asm volatile("s_waitcnt vmcnt(4)" ::: "memory");
    RAW_BARRIER();
    if (kt + 2 < NK) stage(kt + 2, nxt);
    compute(cur);
    cur = (cur == 2) ? 0 : cur + 1;
    nxt = (nxt == 2) ? 0 : nxt + 1;
  }
  asm volatile("" ::: "memory");
  __builtin_amdgcn_s_waitcnt(0x0F70);
  RAW_BARRIER();
  compute(cur);

  if (n0 + wn * 64 < F_) {
    unsigned short* hp = hbuf + ((size_t)((m0 + wm * 64 + r) >> 4) * 54 + ((n0 + wn * 64) >> 5)) * 512 + (h * 16 + (r & 15)) * 8;
#pragma unroll
    for (int mt = 0; mt < 2; ++mt)
#pragma unroll
      for (int nt = 0; nt < 2; ++nt) {
        unsigned pk[8];
#pragma unroll
        for (int j = 0; j < 8; ++j) {
          f32x2 a = {acc[mt][nt][2 * j], acc[mt][nt][2 * j + 1]};
          unsigned hb = __builtin_bit_cast(unsigned, __builtin_convertvector(a, bf16x2n));
          f32x2 hv = {__uint_as_float(hb << 16), __uint_as_float(hb & 0xffff0000u)};
          f32x2 t = hv * (f32x2){-1.4426950408889634f, -1.4426950408889634f};
          f32x2 e = {__builtin_amdgcn_exp2f(t[0]), __builtin_amdgcn_exp2f(t[1])};
          e = e + (f32x2){1.0f, 1.0f};
          f32x2 rc = {__builtin_amdgcn_rcpf(e[0]), __builtin_amdgcn_rcpf(e[1])};
          f32x2 sv = hv * rc;
          pk[j] = __builtin_bit_cast(unsigned, __builtin_convertvector(sv, bf16x2n));
        }
#pragma unroll
        for (int gp = 0; gp < 2; ++gp) {
          u32x2 s0 = __builtin_amdgcn_permlane32_swap(pk[4 * gp + 0], pk[4 * gp + 2], false, false);
          u32x2 s1 = __builtin_amdgcn_permlane32_swap(pk[4 * gp + 1], pk[4 * gp + 3], false, false);
          u32x4 v = {s0[0], s1[0], s0[1], s1[1]};
          GST(hp + (mt * 2 * 54 + nt) * 512 + gp * 256, v);
        }
      }
  }
}

constexpr int UC_STRIDE = 4416;
constexpr int UC_OFF = 0;
constexpr int KROW = 80;
constexpr int KH_OFF = 8 * UC_STRIDE;
constexpr int KL_OFF = KH_OFF + 64 * KROW + 16;
constexpr int KZ_OFF = KH_OFF + 64 * KROW;
constexpr int PR_OFF = KH_OFF + 12288;
constexpr int OS_OFF = KH_OFF;
constexpr int TB_OFF = PR_OFF + 128 * 16;
constexpr int CONV_LDS = TB_OFF + 768 * 4;

__device__ __forceinline__ float thi_of(int i) {
  int t;
  if (i < 8) t = i;
  else if (i < 16) t = 128 + (i - 8);
  else if (i < 24) t = 256 + 128 * ((i - 16) >> 2) + 2 * ((i - 16) & 3);
  else if (i < 32) t = 512 + 128 * ((i - 24) >> 1) + 4 * ((i - 24) & 1);
  else if (i < 40) t = 1024 + 128 * (i - 32);
  else t = 2048;
  return (float)t;
}

struct KTab { unsigned short v[L_]; };
constexpr KTab make_ktab() {
  KTab t{};
  for (int j = 0; j < L_; ++j) {
    int sh = (j < 256) ? 0 : (j < 512) ? 1 : (j < 1024) ? 2 : 3;
    int rr = j;
    if (sh) {
      int q = j >> sh, rem = j & ((1 << sh) - 1), half = 1 << (sh - 1);
      if (rem > half || (rem == half && (q & 1))) ++q;
      rr = q << sh;
    }
    int i = 40;
    if (rr < 256) i = (rr & 7) + 8 * (rr >> 7);
    else if (rr < 512) i = 16 + 4 * ((rr - 256) >> 7) + ((rr & 7) >> 1);
    else if (rr < 1024) i = 24 + 2 * ((rr - 512) >> 7) + ((rr & 7) >> 2);
    else if (rr < 2048) i = 32 + ((rr - 1024) >> 7);
    t.v[j] = (unsigned short)(i * 16 + ((rr >> 3) & 15));
  }
  return t;
}
__device__ __attribute__((aligned(16))) const KTab KTAB = make_ktab();

__device__ void conv_role(int d, const unsigned short* __restrict__ x, const unsigned short* __restrict__ logAr,
                          const unsigned short* __restrict__ Aim, const unsigned short* __restrict__ Cre,
                          const unsigned short* __restrict__ Cim, const unsigned short* __restrict__ logdt,
                          const unsigned short* __restrict__ Dsk, unsigned short* __restrict__ aT, char* smem) {
  const int tid = threadIdx.x, lane = tid & 63, wid = tid >> 6;

  const int pn = tid & (NS_ - 1);
  const unsigned short r_ldt = logdt[d];
  const unsigned short r_lar = logAr[d * NS_ + pn], r_aim = Aim[d * NS_ + pn];
  const unsigned short r_cre = Cre[d * NS_ + pn], r_cim = Cim[d * NS_ + pn];
  const u32x4 kidx = *reinterpret_cast<const u32x4*>(KTAB.v + 8 * tid);
  __builtin_amdgcn_sched_barrier(0);
  unsigned short ug[8];
#pragma unroll
  for (int k = 0; k < 8; ++k) ug[k] = x[(size_t)(8 * tid + k) * D_ + d];
  __builtin_amdgcn_sched_barrier(0);
  asm volatile("" :: "v"(r_ldt), "v"(r_lar), "v"(r_aim), "v"(r_cre), "v"(r_cim));

  const float dt = rbf(expf(bf2f(r_ldt)));
  if (tid < NS_) {
    float are = -rbf(expf(bf2f(r_lar)));
    float aim = bf2f(r_aim);
    float lr = are * dt, li = aim * dt;
    float cr = bf2f(r_cre) * dt, ci = bf2f(r_cim) * dt;
    float mag = sqrtf(cr * cr + ci * ci);
    f32x4 pp;
    pp[0] = lr * 1.4426950408889634f;
    pp[1] = li * 0.15915494309189535f;
    pp[2] = log2f(mag);
    pp[3] = atan2f(ci, cr) * 0.15915494309189535f;
    *reinterpret_cast<f32x4*>(smem + PR_OFF + tid * 16) = pp;
  }
  if (tid < 64) {
    u32x4 z = {0, 0, 0, 0};
    *reinterpret_cast<u32x4*>(smem + UC_OFF + (tid >> 3) * UC_STRIDE + (tid & 7) * 16) = z;
  }
  RAW_BARRIER();

  {
    const int x = lane & 15, kk = lane >> 4;
    float th[3];
#pragma unroll
    for (int T = 0; T < 3; ++T) th[T] = thi_of(16 * T + x);
    const float tlo = (float)(8 * x);
    f32x4 acc[3];
#pragma unroll
    for (int T = 0; T < 3; ++T) acc[T] = (f32x4){0.f, 0.f, 0.f, 0.f};
#pragma unroll 2
    for (int g = 0; g < 8; ++g) {
      int n = 32 * wid + 4 * g + kk;
      f32x4 pp = *reinterpret_cast<const f32x4*>(smem + PR_OFF + n * 16);
      float qe = __builtin_amdgcn_exp2f(pp[0] * tlo);
      float qa = pp[1] * tlo;
      float qr = qe * __builtin_amdgcn_cosf(qa), qi = qe * __builtin_amdgcn_sinf(qa);
#pragma unroll
      for (int T = 0; T < 3; ++T) {
        float pe = __builtin_amdgcn_exp2f(pp[0] * th[T] + pp[2]);
        float pa = pp[1] * th[T] + pp[3];
        float pr = pe * __builtin_amdgcn_cosf(pa), pi = pe * __builtin_amdgcn_sinf(pa);
        acc[T] = __builtin_amdgcn_mfma_f32_16x16x4f32(qr, pr, acc[T], 0, 0, 0);
        acc[T] = __builtin_amdgcn_mfma_f32_16x16x4f32(-qi, pi, acc[T], 0, 0, 0);
      }
    }
    float* os = reinterpret_cast<float*>(smem + OS_OFF) + wid * 768;
#pragma unroll
    for (int T = 0; T < 3; ++T) *reinterpret_cast<f32x4*>(os + (16 * T + x) * 16 + 4 * (kk ^ ((x >> 1) & 3))) = acc[T];
  }
  RAW_BARRIER();

  u32x4 uvec;
#pragma unroll
  for (int j = 0; j < 4; ++j) uvec[j] = (unsigned)ug[2 * j] | ((unsigned)ug[2 * j + 1] << 16);
  *reinterpret_cast<u32x4*>(smem + UC_OFF + 7 * UC_STRIDE + 112 + tid * 16) = uvec;
  {
    unsigned* tab = reinterpret_cast<unsigned*>(smem + TB_OFF);
#pragma unroll
    for (int k = 0; k < 3; ++k) {
      int e = tid + 256 * k;
      const float* osp = reinterpret_cast<const float*>(smem + OS_OFF) + (e ^ ((e >> 3) & 12));
      float kv = 2.0f * ((osp[0] + osp[768]) + (osp[1536] + osp[2304]));
      unsigned short hi = f2bf(kv);
      tab[e] = (unsigned)hi | ((unsigned)f2bf(kv - bf2f(hi)) << 16);
    }
  }
  __syncthreads();
  {
    u32x4 v1 = {0, 0, 0, 0};
    if (tid < 255) v1 = *reinterpret_cast<const u32x4*>(smem + UC_OFF + 7 * UC_STRIDE + 112 + (tid + 1) * 16);
    unsigned w[8] = {uvec[0], uvec[1], uvec[2], uvec[3], v1[0], v1[1], v1[2], v1[3]};
#pragma unroll
    for (int e = 1; e < 8; ++e) {
      u32x4 o;
#pragma unroll
      for (int j = 0; j < 4; ++j) {
        int base = j + (e >> 1);
        if (e & 1) o[j] = (w[base] >> 16) | (w[base + 1] << 16);
        else o[j] = w[base];
      }
      *reinterpret_cast<u32x4*>(smem + UC_OFF + (e - 1) * UC_STRIDE + 128 + tid * 16) = o;
    }
    if (tid == 0) {
#pragma unroll
      for (int e = 1; e < 8; ++e)
#pragma unroll
        for (int k = 0; k < e; ++k)
          *reinterpret_cast<unsigned short*>(smem + UC_OFF + (e - 1) * UC_STRIDE + 2 * (64 - e + k)) =
              (unsigned short)(w[k >> 1] >> (16 * (k & 1)));
    }
  }
  {
    const unsigned* tab = reinterpret_cast<const unsigned*>(smem + TB_OFF);
    unsigned w[8];
#pragma unroll
    for (int k = 0; k < 8; ++k) w[k] = tab[(kidx[k >> 1] >> (16 * (k & 1))) & 0xffff];
    u32x4 vh, vl;
#pragma unroll
    for (int j = 0; j < 4; ++j) {
      vh[j] = (w[7 - 2 * j] & 0xffffu) | (w[6 - 2 * j] << 16);
      vl[j] = (w[7 - 2 * j] >> 16) | (w[6 - 2 * j] & 0xffff0000u);
    }
    *reinterpret_cast<u32x4*>(smem + KH_OFF + (tid >> 2) * KROW + (tid & 3) * 16) = vh;
    *reinterpret_cast<u32x4*>(smem + KL_OFF + (tid >> 2) * KROW + (tid & 3) * 16) = vl;
    if (tid == 0) { u32x4 z = {0, 0, 0, 0}; *reinterpret_cast<u32x4*>(smem + KZ_OFF) = z; }
  }
  __syncthreads();

  f32x16 y0, y1;
#pragma unroll
  for (int i = 0; i < 16; ++i) { y0[i] = 0.f; y1[i] = 0.f; }
  {
    const int r = lane & 31, h = lane >> 5;
    const int e = r & 7;
    const char* ubase = smem + UC_OFF + e * UC_STRIDE + 2 * (r - e + 56 - 8 * h);
#pragma unroll 2
    for (int dl = wid; dl < 32; dl += 4) {
      bf16x8 a0 = *reinterpret_cast<const bf16x8*>(ubase + 64 * dl);
      bf16x8 a1 = *reinterpret_cast<const bf16x8*>(ubase + 64 * dl - 32);
      bf16x8 a2 = *reinterpret_cast<const bf16x8*>(ubase + 64 * dl + 2048);
      bf16x8 a3 = *reinterpret_cast<const bf16x8*>(ubase + 64 * dl + 2048 - 32);
      const int m = r - dl;
      const int off = (m >= 0) ? (m * KROW + 16 * h) : (KZ_OFF - KH_OFF);
      const int off1 = (m >= 0) ? (off + 32) : off;
      const int lo = (m >= 0) ? KL_OFF : KH_OFF;
      bf16x8 fh0 = *reinterpret_cast<const bf16x8*>(smem + KH_OFF + off);
      bf16x8 fh1 = *reinterpret_cast<const bf16x8*>(smem + KH_OFF + off1);
      bf16x8 fl0 = *reinterpret_cast<const bf16x8*>(smem + lo + off);
      bf16x8 fl1 = *reinterpret_cast<const bf16x8*>(smem + lo + off1);
      const int go = (32 + m) * KROW + 16 * h;
      bf16x8 gh0 = *reinterpret_cast<const bf16x8*>(smem + KH_OFF + go);
      bf16x8 gh1 = *reinterpret_cast<const bf16x8*>(smem + KH_OFF + go + 32);
      bf16x8 gl0 = *reinterpret_cast<const bf16x8*>(smem + KL_OFF + go);
      bf16x8 gl1 = *reinterpret_cast<const bf16x8*>(smem + KL_OFF + go + 32);
      y0 = __builtin_amdgcn_mfma_f32_32x32x16_bf16(a0, fh0, y0, 0, 0, 0);
      y1 = __builtin_amdgcn_mfma_f32_32x32x16_bf16(a2, fh0, y1, 0, 0, 0);
      y0 = __builtin_amdgcn_mfma_f32_32x32x16_bf16(a1, fh1, y0, 0, 0, 0);
      y1 = __builtin_amdgcn_mfma_f32_32x32x16_bf16(a3, fh1, y1, 0, 0, 0);
      y0 = __builtin_amdgcn_mfma_f32_32x32x16_bf16(a0, fl0, y0, 0, 0, 0);
      y1 = __builtin_amdgcn_mfma_f32_32x32x16_bf16(a2, fl0, y1, 0, 0, 0);
      y0 = __builtin_amdgcn_mfma_f32_32x32x16_bf16(a1, fl1, y0, 0, 0, 0);
      y1 = __builtin_amdgcn_mfma_f32_32x32x16_bf16(a3, fl1, y1, 0, 0, 0);
      y1 = __builtin_amdgcn_mfma_f32_32x32x16_bf16(a0, gh0, y1, 0, 0, 0);
      y1 = __builtin_amdgcn_mfma_f32_32x32x16_bf16(a1, gh1, y1, 0, 0, 0);
      y1 = __builtin_amdgcn_mfma_f32_32x32x16_bf16(a0, gl0, y1, 0, 0, 0);
      y1 = __builtin_amdgcn_mfma_f32_32x32x16_bf16(a1, gl1, y1, 0, 0, 0);
    }
  }
  __syncthreads();
  {
    const int r = lane & 31, h = lane >> 5;
    float* slab = reinterpret_cast<float*>(smem + wid * 9216);
#pragma unroll
    for (int g = 0; g < 4; ++g) {
      f32x4 v0 = {y0[4 * g], y0[4 * g + 1], y0[4 * g + 2], y0[4 * g + 3]};
      f32x4 v1 = {y1[4 * g], y1[4 * g + 1], y1[4 * g + 2], y1[4 * g + 3]};
      int b = 8 * g + 4 * h;
      *reinterpret_cast<f32x4*>(slab + 36 * r + b) = v0;
      *reinterpret_cast<f32x4*>(slab + 36 * (32 + r) + b) = v1;
    }
  }
  __syncthreads();
  {
    const float dsk = bf2f(Dsk[d]);
    const float* s = reinterpret_cast<const float*>(smem) + (tid >> 2) * 36 + (tid & 3) * 8;
    f32x4 ys[2];
#pragma unroll
    for (int hh = 0; hh < 2; ++hh) {
      f32x4 s0 = *reinterpret_cast<const f32x4*>(s + 4 * hh);
      f32x4 s1 = *reinterpret_cast<const f32x4*>(s + 2304 + 4 * hh);
      f32x4 s2 = *reinterpret_cast<const f32x4*>(s + 4608 + 4 * hh);
      f32x4 s3 = *reinterpret_cast<const f32x4*>(s + 6912 + 4 * hh);
      ys[hh] = (s0 + s1) + (s2 + s3);
    }
    unsigned short o[8];
#pragma unroll
    for (int k = 0; k < 8; ++k) {
      float yb = rbf(ys[k >> 2][k & 3]);
      float uu = bf2f((unsigned short)(uvec[k >> 1] >> (16 * (k & 1))));
      float sk = rbf(uu * dsk);
      o[k] = f2bf(yb + sk);
    }
    u32x4 v;
#pragma unroll
    for (int j = 0; j < 4; ++j) v[j] = o[2 * j] | ((unsigned)o[2 * j + 1] << 16);
    GST(aT + ((size_t)(tid >> 3) * D_ + d) * 64 + 8 * (tid & 7), v);
  }
}

__global__ __launch_bounds__(256, 3) void k12(const unsigned short* __restrict__ x, const unsigned short* __restrict__ w1,
                                              const unsigned short* __restrict__ w2, const unsigned short* __restrict__ gw,
                                              const unsigned short* __restrict__ logAr, const unsigned short* __restrict__ Aim,
                                              const unsigned short* __restrict__ Cre, const unsigned short* __restrict__ Cim,
                                              const unsigned short* __restrict__ logdt, const unsigned short* __restrict__ Dsk,
                                              unsigned short* __restrict__ hbuf, unsigned short* __restrict__ w2T,
                                              float* __restrict__ gbuf, unsigned short* __restrict__ aT) {
  extern __shared__ __attribute__((aligned(16))) char smem[];
  const int b = blockIdx.x;
  constexpr int G0 = 0;
  if (b >= G0 && b < G0 + D_) conv_role(b - G0, x, logAr, Aim, Cre, Cim, logdt, Dsk, aT, smem);
  else if (b < B_GATE) gemm1_role(b < G0 ? b : b - D_, x, w1, hbuf, smem);
  else if (b < B_W2) gate_role(b - B_GATE, x, gw, gbuf);
  else w2_role(b - B_W2, w2, w2T, smem);
}

constexpr int G2_RS = 84;
__global__ __launch_bounds__(256, 1) void k3_gemm2(const unsigned short* __restrict__ hbuf, const unsigned short* __restrict__ w2T,
                                                   const unsigned short* __restrict__ aT, const float* __restrict__ gbuf,
                                                   unsigned short* __restrict__ out) {
  __shared__ __attribute__((aligned(16))) float red[4][64][G2_RS];
  __shared__ unsigned atile[80 * 33];
  const int tid = threadIdx.x, lane = tid & 63, wid = tid >> 6;
  int t = (blockIdx.x & 7) * 32 + (blockIdx.x >> 3);
  const int tm = t >> 3, tn = t & 7;
  const int m0 = tm * 64, n0 = tn * 80;
  const int fr = lane & 15, fq = lane >> 4;
  const int wv = __builtin_amdgcn_readfirstlane(wid);
  const int c0 = (wv * 27) >> 2;
  const int nch = (((wv + 1) * 27) >> 2) - c0;

  const unsigned short* ap = hbuf + (size_t)(m0 >> 4) * 54 * 512 + lane * 8 + c0 * 1024;
  const unsigned short* bp = w2T + (size_t)(n0 >> 4) * 54 * 512 + lane * 8 + c0 * 1024;

  f32x4 acc[4][5];
#pragma unroll
  for (int a = 0; a < 4; ++a)
#pragma unroll
    for (int c = 0; c < 5; ++c) acc[a][c] = (f32x4){0.f, 0.f, 0.f, 0.f};

  constexpr int PD = 2;
  bf16x8 A[PD][4][2], B[PD][5][2];
#define G2_LOAD(S, c)                                                                                  \
  {                                                                                                    \
    _Pragma("unroll") for (int a = 0; a < 4; ++a) {                                                    \
      A[S][a][0] = *reinterpret_cast<const bf16x8*>(ap + a * 54 * 512 + (c) * 1024);                   \
      A[S][a][1] = *reinterpret_cast<const bf16x8*>(ap + a * 54 * 512 + (c) * 1024 + 512);             \
    }                                                                                                  \
    _Pragma("unroll") for (int a = 0; a < 5; ++a) {                                                    \
      B[S][a][0] = *reinterpret_cast<const bf16x8*>(bp + a * 54 * 512 + (c) * 1024);                   \
      B[S][a][1] = *reinterpret_cast<const bf16x8*>(bp + a * 54 * 512 + (c) * 1024 + 512);             \
    }                                                                                                  \
  }
#define G2_COMP(S)                                                                                     \
  {                                                                                                    \
    _Pragma("unroll") for (int s = 0; s < 2; ++s)                                                      \
      _Pragma("unroll") for (int a = 0; a < 4; ++a)                                                    \
        _Pragma("unroll") for (int c = 0; c < 5; ++c)                                                  \
          acc[a][c] = __builtin_amdgcn_mfma_f32_16x16x32_bf16(A[S][a][s], B[S][c][s], acc[a][c], 0, 0, 0); \
  }
#pragma unroll
  for (int i = 0; i < PD - 1; ++i) {
    G2_LOAD(i, i);
    __builtin_amdgcn_sched_barrier(0);
  }
  u32x4 apre[3];
  float gpre[3];
#pragma unroll
  for (int j = 0; j < 3; ++j) {
    int idx = tid + 256 * j;
    if (idx > 639) idx = 639;
    apre[j] = *reinterpret_cast<const u32x4*>(aT + ((size_t)tm * D_ + n0) * 64 + idx * 8);
    gpre[j] = gbuf[m0 + idx / 10];
  }
  __builtin_amdgcn_sched_barrier(0);
#pragma unroll
  for (int i = 0; i < 7; ++i) {
    if (i + PD - 1 < 7) {
      const int cn = (i + PD - 1 < 6) ? (i + PD - 1) : (nch - 1);
      G2_LOAD((i + PD - 1) % PD, cn);
    }
    __builtin_amdgcn_sched_barrier(0);
    if (i == 6 && nch != 7) {
      const bf16x8 z = {0, 0, 0, 0, 0, 0, 0, 0};
#pragma unroll
      for (int a = 0; a < 4; ++a) { A[i % PD][a][0] = z; A[i % PD][a][1] = z; }
#pragma unroll
      for (int a = 0; a < 5; ++a) { B[i % PD][a][0] = z; B[i % PD][a][1] = z; }
    }
    G2_COMP(i % PD);
    __builtin_amdgcn_sched_barrier(0);
  }
#undef G2_LOAD
#undef G2_COMP

#pragma unroll
  for (int a = 0; a < 4; ++a)
#pragma unroll
    for (int c = 0; c < 5; ++c)
#pragma unroll
      for (int i = 0; i < 4; ++i) red[wid][16 * a + 4 * fq + i][16 * c + fr] = acc[a][c][i];
#pragma unroll
  for (int j = 0; j < 3; ++j) {
    const int idx = tid + 256 * j;
    if (idx < 640) {
#pragma unroll
      for (int q = 0; q < 4; ++q) atile[(idx >> 3) * 33 + (idx & 7) * 4 + q] = apre[j][q];
    }
  }
  __syncthreads();

#pragma unroll
  for (int j = 0; j < 3; ++j) {
    const int idx = tid + 256 * j;
    if (idx < 640) {
      const int row = idx / 10, cg = idx - row * 10;
      f32x4 s[2];
#pragma unroll
      for (int hh = 0; hh < 2; ++hh) {
        f32x4 s0 = *reinterpret_cast<const f32x4*>(&red[0][row][8 * cg + 4 * hh]);
        f32x4 s1 = *reinterpret_cast<const f32x4*>(&red[1][row][8 * cg + 4 * hh]);
        f32x4 s2 = *reinterpret_cast<const f32x4*>(&red[2][row][8 * cg + 4 * hh]);
        f32x4 s3 = *reinterpret_cast<const f32x4*>(&red[3][row][8 * cg + 4 * hh]);
        s[hh] = (s0 + s1) + (s2 + s3);
      }
      const float g = gpre[j];
      const float t2 = rbf(1.0f - g);
      const unsigned short* at = reinterpret_cast<const unsigned short*>(atile) + (8 * cg) * 66 + row;
      unsigned short o[8];
#pragma unroll
      for (int k = 0; k < 8; ++k) {
        float bv = rbf(s[k >> 2][k & 3]);
        float a = bf2f(at[k * 66]);
        float t1 = rbf(g * a);
        float t3 = rbf(t2 * bv);
        o[k] = f2bf(t1 + t3);
      }
      u32x4 v;
#pragma unroll
      for (int q = 0; q < 4; ++q) v[q] = o[2 * q] | ((unsigned)o[2 * q + 1] << 16);
      *reinterpret_cast<u32x4*>(out + (size_t)(m0 + row) * D_ + n0 + 8 * cg) = v;
    }
  }
}

extern "C" void kernel_launch(void* const* d_in, const int* in_sizes, int n_in, void* d_out, int out_size, void* d_ws,
                              size_t ws_size, hipStream_t stream) {
  const unsigned short* u = (const unsigned short*)d_in[0];
  const unsigned short* logAr = (const unsigned short*)d_in[1];
  const unsigned short* Aim = (const unsigned short*)d_in[2];
  const unsigned short* Cre = (const unsigned short*)d_in[3];
  const unsigned short* Cim = (const unsigned short*)d_in[4];
  const unsigned short* logdt = (const unsigned short*)d_in[5];
  const unsigned short* Dsk = (const unsigned short*)d_in[6];
  const unsigned short* w1 = (const unsigned short*)d_in[8];
  const unsigned short* w2 = (const unsigned short*)d_in[9];
  const unsigned short* gw = (const unsigned short*)d_in[10];
  unsigned short* out = (unsigned short*)d_out;

  char* ws = (char*)d_ws;
  unsigned short* hbuf = (unsigned short*)ws;
  unsigned short* aT = (unsigned short*)(ws + (size_t)L_ * F_ * 2);
  unsigned short* w2T = (unsigned short*)(ws + (size_t)L_ * F_ * 2 + (size_t)D_ * L_ * 2);
  float* gbuf = (float*)(ws + (size_t)L_ * F_ * 2 + (size_t)D_ * L_ * 2 + (size_t)D_ * F_ * 2);

  constexpr int K12_LDS = (CONV_LDS > 3 * G1_STAGE) ? CONV_LDS : 3 * G1_STAGE;
  hipLaunchKernelGGL(k12, dim3(K12_GRID), dim3(256), K12_LDS, stream, u, w1, w2, gw, logAr, Aim, Cre, Cim, logdt, Dsk,
                     hbuf, w2T, gbuf, aT);
  hipLaunchKernelGGL(k3_gemm2, dim3(256), dim3(256), 0, stream, hbuf, w2T, aT, gbuf, out);
}
```

```cpp
#include <hip/hip_runtime.h>
#include <hip/hip_bf16.h>
#include <stdint.h>

typedef __attribute__((ext_vector_type(8))) short bf16x8;
typedef __attribute__((ext_vector_type(4))) short bf16x4;
typedef __attribute__((ext_vector_type(16))) float f32x16;
typedef __attribute__((ext_vector_type(4))) float f32x4;
typedef __attribute__((ext_vector_type(2))) float f32x2;
typedef __attribute__((ext_vector_type(2))) __bf16 bf16x2n;
typedef __attribute__((ext_vector_type(4))) unsigned int u32x4;
typedef __attribute__((ext_vector_type(2))) unsigned int u32x2;

#define LDS_AS __attribute__((address_space(3)))
#define RAW_BARRIER() do { asm volatile("s_waitcnt lgkmcnt(0)" ::: "memory"); __builtin_amdgcn_s_barrier(); } while (0)

#define GST(P, V) asm volatile("global_store_dwordx4 %0, %1, off sc0 sc1\n\ts_nop 1" :: "v"(P), "v"(V) : "memory")
constexpr int L_ = 2048, D_ = 640, NS_ = 128, F_ = 1728;

__device__ __forceinline__ float bf2f(unsigned short v) { return __uint_as_float(((unsigned)v) << 16); }
__device__ __forceinline__ unsigned short f2bf(float f) {
  __bf16 b = (__bf16)f;
  return __builtin_bit_cast(unsigned short, b);
}
__device__ __forceinline__ float rbf(float f) { return bf2f(f2bf(f)); }

constexpr int G1_TM = L_ / 128, G1_TN = (F_ + 127) / 128;
constexpr int N_G1 = G1_TM * G1_TN;
constexpr int G1_STAGE = 16384;
constexpr int N_GATE = 32, N_W2 = 90;
constexpr int B_CONV = N_G1, B_GATE = B_CONV + D_, B_W2 = B_GATE + N_GATE, K12_GRID = B_W2 + N_W2;

__device__ __forceinline__ void tr_tiles_load(const unsigned short* __restrict__ src, int ss, int r0, int c0,
                                              u32x4 (&v)[2]) {
  const int tid = threadIdx.x;
#pragma unroll
  for (int i = 0; i < 2; ++i) {
    int id = tid + 256 * i, row = id >> 3, ch = id & 7;
    v[i] = *reinterpret_cast<const u32x4*>(src + (size_t)(r0 + row) * ss + c0 + ch * 8);
  }
}
__device__ __forceinline__ void tr_tiles_put(unsigned short (*tile)[72], const u32x4 (&v)[2]) {
  const int tid = threadIdx.x;
#pragma unroll
  for (int i = 0; i < 2; ++i) {
    int id = tid + 256 * i, row = id >> 3, ch = id & 7;
    *reinterpret_cast<u32x4*>(&tile[row][ch * 8]) = v[i];
  }
}
__device__ __forceinline__ void tr_tiles_store_blocked(unsigned short (*tile)[72], unsigned short* __restrict__ dst,
                                                       int r0, int c0) {
  const int tid = threadIdx.x;
#pragma unroll
  for (int i = 0; i < 2; ++i) {
    int id = tid + 256 * i, col = ((id >> 7) << 4) | (id & 15), ch = (id >> 4) & 7;
    unsigned short o[8];
#pragma unroll
    for (int k = 0; k < 8; ++k) o[k] = tile[ch * 8 + k][col];
    u32x4 w;
#pragma unroll
    for (int j = 0; j < 4; ++j) w[j] = o[2 * j] | ((unsigned)o[2 * j + 1] << 16);
    unsigned short* dp = dst + ((size_t)((c0 + col) >> 4) * 54 + ((r0 + ch * 8) >> 5)) * 512 + (id & 63) * 8;
    GST(dp, w);
  }
}

__device__ void gate_role(int gb, const unsigned short* __restrict__ x, const unsigned short* __restrict__ gw,
                          float* __restrict__ gbuf) {
  const int tid = threadIdx.x;
  const int r0 = gb * 64;
  u32x4 v[10][2];
#pragma unroll
  for (int t = 0; t < 10; ++t) tr_tiles_load(x, D_, r0, t * 64, v[t]);
  float s[2] = {0.f, 0.f};
#pragma unroll
  for (int t = 0; t < 10; ++t) {
    u32x4 g = *reinterpret_cast<const u32x4*>(gw + t * 64 + (tid & 7) * 8);
#pragma unroll
    for (int i = 0; i < 2; ++i)
#pragma unroll
      for (int j = 0; j < 4; ++j) {
        s[i] += rbf(__uint_as_float(v[t][i][j] << 16) * __uint_as_float(g[j] << 16));
        s[i] += rbf(__uint_as_float(v[t][i][j] & 0xffff0000u) * __uint_as_float(g[j] & 0xffff0000u));
      }
  }
#pragma unroll
  for (int i = 0; i < 2; ++i) {
    s[i] += __shfl_xor(s[i], 1); s[i] += __shfl_xor(s[i], 2); s[i] += __shfl_xor(s[i], 4);
    if ((tid & 7) == 0) gbuf[r0 + (tid >> 3) + 32 * i] = rbf(1.0f / (1.0f + expf(-rbf(s[i]))));
  }
}

__device__ void w2_role(int wb, const unsigned short* __restrict__ w2, unsigned short* __restrict__ w2T, char* smem) {
  u32x4 tv[3][2];
#pragma unroll
  for (int j = 0; j < 3; ++j) {
    int id = wb * 3 + j;
    tr_tiles_load(w2, D_, (id / 10) * 64, (id % 10) * 64, tv[j]);
  }
  typedef unsigned short tile_t[64][72];
  tile_t* tiles = reinterpret_cast<tile_t*>(smem);
#pragma unroll
  for (int j = 0; j < 3; ++j) tr_tiles_put(tiles[j], tv[j]);
  __syncthreads();
#pragma unroll
  for (int j = 0; j < 3; ++j) {
    int id = wb * 3 + j;
    tr_tiles_store_blocked(tiles[j], w2T, (id / 10) * 64, (id % 10) * 64);
  }
}

__device__ void gemm1_role(int b, const unsigned short* __restrict__ x, const unsigned short* __restrict__ w1,
                           unsigned short* __restrict__ hbuf, char* smem) {
  const int tid = threadIdx.x, lane = tid & 63, wid = tid >> 6;
  __builtin_amdgcn_s_setprio(3);
  int t = (b & 7) * (N_G1 / 8) + (b >> 3);
  const int tm = t / G1_TN, tn = t % G1_TN;
  const int m0 = tm * 128, n0 = tn * 128;
  const int wm = wid >> 1, wn = wid & 1, r = lane & 31, h = lane >> 5;

  const unsigned short* asrc[2];
  const unsigned short* bsrc[2];
#pragma unroll
  for (int i = 0; i < 2; ++i) {
    int row = (i * 4 + wid) * 16 + (lane >> 2);
    int c = (lane & 3) ^ ((row >> 2) & 3);
    asrc[i] = x + (size_t)(m0 + row) * D_ + c * 8;
    int k = (i * 4 + wid) * 4 + (lane >> 4);
    int cb = (lane & 15) ^ ((k & 3) << 2);
    int col = n0 + cb * 8;
    if (col > F_ - 8) col = F_ - 8;
    bsrc[i] = w1 + (size_t)k * F_ + col;
  }
  auto stage = [&](int kt, int buf) {
    char* sa = smem + buf * G1_STAGE;
    char* sb = sa + 8192;
#pragma unroll
    for (int i = 0; i < 2; ++i)
      __builtin_amdgcn_global_load_lds((const unsigned*)(asrc[i] + kt * 32), (LDS_AS unsigned*)(sa + (i * 4 + wid) * 1024), 16, 0, 0);
#pragma unroll
    for (int i = 0; i < 2; ++i)
      __builtin_amdgcn_global_load_lds((const unsigned*)(bsrc[i] + (size_t)kt * 32 * F_), (LDS_AS unsigned*)(sb + (i * 4 + wid) * 1024), 16, 0, 0);
  };

  f32x16 acc[2][2];
#pragma unroll
  for (int a = 0; a < 2; ++a)
#pragma unroll
    for (int c = 0; c < 2; ++c)
#pragma unroll
      for (int i = 0; i < 16; ++i) acc[a][c][i] = 0.f;

  const int q = (lane & 15) >> 2, p = lane & 3, blk = (lane >> 4) & 1;
  auto compute = [&](int buf) {
    const char* sa = smem + buf * G1_STAGE;
    const char* sb = sa + 8192;
#pragma unroll
    for (int s = 0; s < 2; ++s) {
      bf16x8 af[2], bfr[2];
#pragma unroll
      for (int mt = 0; mt < 2; ++mt) {
        int row = wm * 64 + mt * 32 + r;
        af[mt] = *reinterpret_cast<const bf16x8*>(sa + row * 64 + (((2 * s + h) ^ ((row >> 2) & 3)) << 4));
      }
#pragma unroll
      for (int nt = 0; nt < 2; ++nt)
#pragma unroll
        for (int tt = 0; tt < 2; ++tt) {
          int k = 16 * s + 8 * h + 4 * tt + q;
          int chunk = wn * 8 + nt * 4 + 2 * blk + (p >> 1);
          const char* ad = sb + k * 256 + ((chunk ^ (q << 2)) << 4) + (p & 1) * 8;
          bf16x4 v = __builtin_amdgcn_ds_read_tr16_b64_v4i16((LDS_AS bf16x4*)(ad));
          bfr[nt][4 * tt + 0] = v[0]; bfr[nt][4 * tt + 1] = v[1]; bfr[nt][4 * tt + 2] = v[2]; bfr[nt][4 * tt + 3] = v[3];
        }
#pragma unroll
      for (int mt = 0; mt < 2; ++mt)
#pragma unroll
        for (int nt = 0; nt < 2; ++nt)
          acc[mt][nt] = __builtin_amdgcn_mfma_f32_32x32x16_bf16(bfr[nt], af[mt], acc[mt][nt], 0, 0, 0);
    }
  };

  constexpr int NK = D_ / 32;
  stage(0, 0);
  stage(1, 1);
  int cur = 0, nxt = 2;
  for (int kt = 0; kt < NK - 1; ++kt) {
    asm volatile("s_waitcnt vmcnt(4)" ::: "memory");
    RAW_BARRIER();
    if (kt + 2 < NK) stage(kt + 2, nxt);
    compute(cur);
    cur = (cur == 2) ? 0 : cur + 1;
    nxt = (nxt == 2) ? 0 : nxt + 1;
  }
  asm volatile("" ::: "memory");
  __builtin_amdgcn_s_waitcnt(0x0F70);
  RAW_BARRIER();
  compute(cur);

  if (n0 + wn * 64 < F_) {
    unsigned short* hp = hbuf + ((size_t)((m0 + wm * 64 + r) >> 4) * 54 + ((n0 + wn * 64) >> 5)) * 512 + (h * 16 + (r & 15)) * 8;
#pragma unroll
    for (int mt = 0; mt < 2; ++mt)
#pragma unroll
      for (int nt = 0; nt < 2; ++nt) {
        unsigned pk[8];
#pragma unroll
        for (int j = 0; j < 8; ++j) {
          f32x2 a = {acc[mt][nt][2 * j], acc[mt][nt][2 * j + 1]};
          unsigned hb = __builtin_bit_cast(unsigned, __builtin_convertvector(a, bf16x2n));
          f32x2 hv = {__uint_as_float(hb << 16), __uint_as_float(hb & 0xffff0000u)};
          f32x2 t = hv * (f32x2){-1.4426950408889634f, -1.4426950408889634f};
          f32x2 e = {__builtin_amdgcn_exp2f(t[0]), __builtin_amdgcn_exp2f(t[1])};
          e = e + (f32x2){1.0f, 1.0f};
          f32x2 rc = {__builtin_amdgcn_rcpf(e[0]), __builtin_amdgcn_rcpf(e[1])};
          f32x2 sv = hv * rc;
          pk[j] = __builtin_bit_cast(unsigned, __builtin_convertvector(sv, bf16x2n));
        }
#pragma unroll
        for (int gp = 0; gp < 2; ++gp) {
          u32x2 s0 = __builtin_amdgcn_permlane32_swap(pk[4 * gp + 0], pk[4 * gp + 2], false, false);
          u32x2 s1 = __builtin_amdgcn_permlane32_swap(pk[4 * gp + 1], pk[4 * gp + 3], false, false);
          u32x4 v = {s0[0], s1[0], s0[1], s1[1]};
          GST(hp + (mt * 2 * 54 + nt) * 512 + gp * 256, v);
        }
      }
  }
}

constexpr int UC_STRIDE = 4416;
constexpr int UC_OFF = 0;
constexpr int KROW = 80;
constexpr int KH_OFF = 8 * UC_STRIDE;
constexpr int KL_OFF = KH_OFF + 64 * KROW + 16;
constexpr int KZ_OFF = KH_OFF + 64 * KROW;
constexpr int PR_OFF = KH_OFF + 12288;
constexpr int OS_OFF = KH_OFF;
constexpr int TB_OFF = PR_OFF + 128 * 16;
constexpr int CONV_LDS = TB_OFF + 768 * 4;

__device__ __forceinline__ float thi_of(int i) {
  int t;
  if (i < 8) t = i;
  else if (i < 16) t = 128 + (i - 8);
  else if (i < 24) t = 256 + 128 * ((i - 16) >> 2) + 2 * ((i - 16) & 3);
  else if (i < 32) t = 512 + 128 * ((i - 24) >> 1) + 4 * ((i - 24) & 1);
  else if (i < 40) t = 1024 + 128 * (i - 32);
  else t = 2048;
  return (float)t;
}

struct KTab { unsigned short v[L_]; };
constexpr KTab make_ktab() {
  KTab t{};
  for (int j = 0; j < L_; ++j) {
    int sh = (j < 256) ? 0 : (j < 512) ? 1 : (j < 1024) ? 2 : 3;
    int rr = j;
    if (sh) {
      int q = j >> sh, rem = j & ((1 << sh) - 1), half = 1 << (sh - 1);
      if (rem > half || (rem == half && (q & 1))) ++q;
      rr = q << sh;
    }
    int i = 40;
    if (rr < 256) i = (rr & 7) + 8 * (rr >> 7);
    else if (rr < 512) i = 16 + 4 * ((rr - 256) >> 7) + ((rr & 7) >> 1);
    else if (rr < 1024) i = 24 + 2 * ((rr - 512) >> 7) + ((rr & 7) >> 2);
    else if (rr < 2048) i = 32 + ((rr - 1024) >> 7);
    t.v[j] = (unsigned short)(i * 16 + ((rr >> 3) & 15));
  }
  return t;
}
__device__ __attribute__((aligned(16))) const KTab KTAB = make_ktab();

__device__ void conv_role(int d, const unsigned short* __restrict__ x, const unsigned short* __restrict__ logAr,
                          const unsigned short* __restrict__ Aim, const unsigned short* __restrict__ Cre,
                          const unsigned short* __restrict__ Cim, const unsigned short* __restrict__ logdt,
                          const unsigned short* __restrict__ Dsk, unsigned short* __restrict__ aT, char* smem) {
  const int tid = threadIdx.x, lane = tid & 63, wid = tid >> 6;

  const int pn = tid & (NS_ - 1);
  const unsigned short r_ldt = logdt[d];
  const unsigned short r_lar = logAr[d * NS_ + pn], r_aim = Aim[d * NS_ + pn];
  const unsigned short r_cre = Cre[d * NS_ + pn], r_cim = Cim[d * NS_ + pn];
  const u32x4 kidx = *reinterpret_cast<const u32x4*>(KTAB.v + 8 * tid);
  __builtin_amdgcn_sched_barrier(0);
  unsigned short ug[8];
#pragma unroll
  for (int k = 0; k < 8; ++k) ug[k] = x[(size_t)(8 * tid + k) * D_ + d];
  __builtin_amdgcn_sched_barrier(0);
  asm volatile("" :: "v"(r_ldt), "v"(r_lar), "v"(r_aim), "v"(r_cre), "v"(r_cim));

  const float dt = rbf(expf(bf2f(r_ldt)));
  if (tid < NS_) {
    float are = -rbf(expf(bf2f(r_lar)));
    float aim = bf2f(r_aim);
    float lr = are * dt, li = aim * dt;
    float cr = bf2f(r_cre) * dt, ci = bf2f(r_cim) * dt;
    float mag = sqrtf(cr * cr + ci * ci);
    f32x4 pp;
    pp[0] = lr * 1.4426950408889634f;
    pp[1] = li * 0.15915494309189535f;
    pp[2] = log2f(mag);
    pp[3] = atan2f(ci, cr) * 0.15915494309189535f;
    *reinterpret_cast<f32x4*>(smem + PR_OFF + tid * 16) = pp;
  }
  if (tid < 64) {
    u32x4 z = {0, 0, 0, 0};
    *reinterpret_cast<u32x4*>(smem + UC_OFF + (tid >> 3) * UC_STRIDE + (tid & 7) * 16) = z;
  }
  RAW_BARRIER();

  {
    const int x = lane & 15, kk = lane >> 4;
    float th[3];
#pragma unroll
    for (int T = 0; T < 3; ++T) th[T] = thi_of(16 * T + x);
    const float tlo = (float)(8 * x);
    f32x4 acc[3];
#pragma unroll
    for (int T = 0; T < 3; ++T) acc[T] = (f32x4){0.f, 0.f, 0.f, 0.f};
#pragma unroll 2
    for (int g = 0; g < 8; ++g) {
      int n = 32 * wid + 4 * g + kk;
      f32x4 pp = *reinterpret_cast<const f32x4*>(smem + PR_OFF + n * 16);
      float qe = __builtin_amdgcn_exp2f(pp[0] * tlo);
      float qa = pp[1] * tlo;
      float qr = qe * __builtin_amdgcn_cosf(qa), qi = qe * __builtin_amdgcn_sinf(qa);
#pragma unroll
      for (int T = 0; T < 3; ++T) {
        float pe = __builtin_amdgcn_exp2f(pp[0] * th[T] + pp[2]);
        float pa = pp[1] * th[T] + pp[3];
        float pr = pe * __builtin_amdgcn_cosf(pa), pi = pe * __builtin_amdgcn_sinf(pa);
        acc[T] = __builtin_amdgcn_mfma_f32_16x16x4f32(qr, pr, acc[T], 0, 0, 0);
        acc[T] = __builtin_amdgcn_mfma_f32_16x16x4f32(-qi, pi, acc[T], 0, 0, 0);
      }
    }
    float* os = reinterpret_cast<float*>(smem + OS_OFF) + wid * 768;
#pragma unroll
    for (int T = 0; T < 3; ++T) *reinterpret_cast<f32x4*>(os + (16 * T + x) * 16 + 4 * (kk ^ ((x >> 1) & 3))) = acc[T];
  }
  RAW_BARRIER();

  u32x4 uvec;
#pragma unroll
  for (int j = 0; j < 4; ++j) uvec[j] = (unsigned)ug[2 * j] | ((unsigned)ug[2 * j + 1] << 16);
  *reinterpret_cast<u32x4*>(smem + UC_OFF + 7 * UC_STRIDE + 112 + tid * 16) = uvec;
  {
    unsigned* tab = reinterpret_cast<unsigned*>(smem + TB_OFF);
#pragma unroll
    for (int k = 0; k < 3; ++k) {
      int e = tid + 256 * k;
      const float* osp = reinterpret_cast<const float*>(smem + OS_OFF) + (e ^ ((e >> 3) & 12));
      float kv = 2.0f * ((osp[0] + osp[768]) + (osp[1536] + osp[2304]));
      unsigned short hi = f2bf(kv);
      tab[e] = (unsigned)hi | ((unsigned)f2bf(kv - bf2f(hi)) << 16);
    }
  }
  __syncthreads();
  {
    u32x4 v1 = {0, 0, 0, 0};
    if (tid < 255) v1 = *reinterpret_cast<const u32x4*>(smem + UC_OFF + 7 * UC_STRIDE + 112 + (tid + 1) * 16);
    unsigned w[8] = {uvec[0], uvec[1], uvec[2], uvec[3], v1[0], v1[1], v1[2], v1[3]};
#pragma unroll
    for (int e = 1; e < 8; ++e) {
      u32x4 o;
#pragma unroll
      for (int j = 0; j < 4; ++j) {
        int base = j + (e >> 1);
        if (e & 1) o[j] = (w[base] >> 16) | (w[base + 1] << 16);
        else o[j] = w[base];
      }
      *reinterpret_cast<u32x4*>(smem + UC_OFF + (e - 1) * UC_STRIDE + 128 + tid * 16) = o;
    }
    if (tid == 0) {
#pragma unroll
      for (int e = 1; e < 8; ++e)
#pragma unroll
        for (int k = 0; k < e; ++k)
          *reinterpret_cast<unsigned short*>(smem + UC_OFF + (e - 1) * UC_STRIDE + 2 * (64 - e + k)) =
              (unsigned short)(w[k >> 1] >> (16 * (k & 1)));
    }
  }
  {
    const unsigned* tab = reinterpret_cast<const unsigned*>(smem + TB_OFF);
    unsigned w[8];
#pragma unroll
    for (int k = 0; k < 8; ++k) w[k] = tab[(kidx[k >> 1] >> (16 * (k & 1))) & 0xffff];
    u32x4 vh, vl;
#pragma unroll
    for (int j = 0; j < 4; ++j) {
      vh[j] = (w[7 - 2 * j] & 0xffffu) | (w[6 - 2 * j] << 16);
      vl[j] = (w[7 - 2 * j] >> 16) | (w[6 - 2 * j] & 0xffff0000u);
    }
    *reinterpret_cast<u32x4*>(smem + KH_OFF + (tid >> 2) * KROW + (tid & 3) * 16) = vh;
    *reinterpret_cast<u32x4*>(smem + KL_OFF + (tid >> 2) * KROW + (tid & 3) * 16) = vl;
    if (tid == 0) { u32x4 z = {0, 0, 0, 0}; *reinterpret_cast<u32x4*>(smem + KZ_OFF) = z; }
  }
  __syncthreads();

  f32x16 y0, y1;
#pragma unroll
  for (int i = 0; i < 16; ++i) { y0[i] = 0.f; y1[i] = 0.f; }
  {
    const int r = lane & 31, h = lane >> 5;
    const int e = r & 7;
    const char* ubase = smem + UC_OFF + e * UC_STRIDE + 2 * (r - e + 56 - 8 * h);
#pragma unroll 2
    for (int dl = wid; dl < 32; dl += 4) {
      bf16x8 a0 = *reinterpret_cast<const bf16x8*>(ubase + 64 * dl);
      bf16x8 a1 = *reinterpret_cast<const bf16x8*>(ubase + 64 * dl - 32);
      bf16x8 a2 = *reinterpret_cast<const bf16x8*>(ubase + 64 * dl + 2048);
      bf16x8 a3 = *reinterpret_cast<const bf16x8*>(ubase + 64 * dl + 2048 - 32);
      const int m = r - dl;
      const int off = (m >= 0) ? (m * KROW + 16 * h) : (KZ_OFF - KH_OFF);
      const int off1 = (m >= 0) ? (off + 32) : off;
      const int lo = (m >= 0) ? KL_OFF : KH_OFF;
      bf16x8 fh0 = *reinterpret_cast<const bf16x8*>(smem + KH_OFF + off);
      bf16x8 fh1 = *reinterpret_cast<const bf16x8*>(smem + KH_OFF + off1);
      bf16x8 fl0 = *reinterpret_cast<const bf16x8*>(smem + lo + off);
      bf16x8 fl1 = *reinterpret_cast<const bf16x8*>(smem + lo + off1);
      const int go = (32 + m) * KROW + 16 * h;
      bf16x8 gh0 = *reinterpret_cast<const bf16x8*>(smem + KH_OFF + go);
      bf16x8 gh1 = *reinterpret_cast<const bf16x8*>(smem + KH_OFF + go + 32);
      bf16x8 gl0 = *reinterpret_cast<const bf16x8*>(smem + KL_OFF + go);
      bf16x8 gl1 = *reinterpret_cast<const bf16x8*>(smem + KL_OFF + go + 32);
      y0 = __builtin_amdgcn_mfma_f32_32x32x16_bf16(a0, fh0, y0, 0, 0, 0);
      y1 = __builtin_amdgcn_mfma_f32_32x32x16_bf16(a2, fh0, y1, 0, 0, 0);
      y0 = __builtin_amdgcn_mfma_f32_32x32x16_bf16(a1, fh1, y0, 0, 0, 0);
      y1 = __builtin_amdgcn_mfma_f32_32x32x16_bf16(a3, fh1, y1, 0, 0, 0);
      y0 = __builtin_amdgcn_mfma_f32_32x32x16_bf16(a0, fl0, y0, 0, 0, 0);
      y1 = __builtin_amdgcn_mfma_f32_32x32x16_bf16(a2, fl0, y1, 0, 0, 0);
      y0 = __builtin_amdgcn_mfma_f32_32x32x16_bf16(a1, fl1, y0, 0, 0, 0);
      y1 = __builtin_amdgcn_mfma_f32_32x32x16_bf16(a3, fl1, y1, 0, 0, 0);
      y1 = __builtin_amdgcn_mfma_f32_32x32x16_bf16(a0, gh0, y1, 0, 0, 0);
      y1 = __builtin_amdgcn_mfma_f32_32x32x16_bf16(a1, gh1, y1, 0, 0, 0);
      y1 = __builtin_amdgcn_mfma_f32_32x32x16_bf16(a0, gl0, y1, 0, 0, 0);
      y1 = __builtin_amdgcn_mfma_f32_32x32x16_bf16(a1, gl1, y1, 0, 0, 0);
    }
  }
  __syncthreads();
  {
    const int r = lane & 31, h = lane >> 5;
    float* slab = reinterpret_cast<float*>(smem + wid * 9216);
#pragma unroll
    for (int g = 0; g < 4; ++g) {
      f32x4 v0 = {y0[4 * g], y0[4 * g + 1], y0[4 * g + 2], y0[4 * g + 3]};
      f32x4 v1 = {y1[4 * g], y1[4 * g + 1], y1[4 * g + 2], y1[4 * g + 3]};
      int b = 8 * g + 4 * h;
      *reinterpret_cast<f32x4*>(slab + 36 * r + b) = v0;
      *reinterpret_cast<f32x4*>(slab + 36 * (32 + r) + b) = v1;
    }
  }
  __syncthreads();
  {
    const float dsk = bf2f(Dsk[d]);
    const float* s = reinterpret_cast<const float*>(smem) + (tid >> 2) * 36 + (tid & 3) * 8;
    f32x4 ys[2];
#pragma unroll
    for (int hh = 0; hh < 2; ++hh) {
      f32x4 s0 = *reinterpret_cast<const f32x4*>(s + 4 * hh);
      f32x4 s1 = *reinterpret_cast<const f32x4*>(s + 2304 + 4 * hh);
      f32x4 s2 = *reinterpret_cast<const f32x4*>(s + 4608 + 4 * hh);
      f32x4 s3 = *reinterpret_cast<const f32x4*>(s + 6912 + 4 * hh);
      ys[hh] = (s0 + s1) + (s2 + s3);
    }
    unsigned short o[8];
#pragma unroll
    for (int k = 0; k < 8; ++k) {
      float yb = rbf(ys[k >> 2][k & 3]);
      float uu = bf2f((unsigned short)(uvec[k >> 1] >> (16 * (k & 1))));
      float sk = rbf(uu * dsk);
      o[k] = f2bf(yb + sk);
    }
    u32x4 v;
#pragma unroll
    for (int j = 0; j < 4; ++j) v[j] = o[2 * j] | ((unsigned)o[2 * j + 1] << 16);
    GST(aT + ((size_t)(tid >> 3) * D_ + d) * 64 + 8 * (tid & 7), v);
  }
}

__global__ __launch_bounds__(256, 3) void k12(const unsigned short* __restrict__ x, const unsigned short* __restrict__ w1,
                                              const unsigned short* __restrict__ w2, const unsigned short* __restrict__ gw,
                                              const unsigned short* __restrict__ logAr, const unsigned short* __restrict__ Aim,
                                              const unsigned short* __restrict__ Cre, const unsigned short* __restrict__ Cim,
                                              const unsigned short* __restrict__ logdt, const unsigned short* __restrict__ Dsk,
                                              unsigned short* __restrict__ hbuf, unsigned short* __restrict__ w2T,
                                              float* __restrict__ gbuf, unsigned short* __restrict__ aT) {
  extern __shared__ __attribute__((aligned(16))) char smem[];
  const int b = blockIdx.x;
  constexpr int G0 = 0;
  if (b >= G0 && b < G0 + D_) conv_role(b - G0, x, logAr, Aim, Cre, Cim, logdt, Dsk, aT, smem);
  else if (b < B_GATE) gemm1_role(b < G0 ? b : b - D_, x, w1, hbuf, smem);
  else if (b < B_W2) gate_role(b - B_GATE, x, gw, gbuf);
  else w2_role(b - B_W2, w2, w2T, smem);
}

__global__ __launch_bounds__(256, 1) void k3_gemm2(const unsigned short* __restrict__ hbuf, const unsigned short* __restrict__ w2T,
                                                   const unsigned short* __restrict__ aT, const float* __restrict__ gbuf,
                                                   unsigned short* __restrict__ out) {
  __shared__ f32x4 red[4][4][5][64];
  const int tid = threadIdx.x, lane = tid & 63, wid = tid >> 6;
  const int xc = blockIdx.x & 7, jj = blockIdx.x >> 3;
  const int tm = (xc >> 1) * 8 + (jj >> 2), tn = (xc & 1) * 4 + (jj & 3);
  const int m0 = tm * 64, n0 = tn * 80;
  const int fr = lane & 15, fq = lane >> 4;
  const int wv = __builtin_amdgcn_readfirstlane(wid);
  const int c0 = (wv * 27) >> 2;
  const int nch = (((wv + 1) * 27) >> 2) - c0;

  const unsigned short* ap = hbuf + (size_t)(m0 >> 4) * 54 * 512 + lane * 8 + c0 * 1024;
  const unsigned short* bp = w2T + (size_t)(n0 >> 4) * 54 * 512 + lane * 8 + c0 * 1024;

  f32x4 acc[4][5];
#pragma unroll
  for (int a = 0; a < 4; ++a)
#pragma unroll
    for (int c = 0; c < 5; ++c) acc[a][c] = (f32x4){0.f, 0.f, 0.f, 0.f};

  constexpr int PD = 2;
  bf16x8 A[PD][4][2], B[PD][5][2];
#define G2_LOAD(S, c)                                                                                  \
  {                                                                                                    \
    _Pragma("unroll") for (int a = 0; a < 4; ++a) {                                                    \
      A[S][a][0] = *reinterpret_cast<const bf16x8*>(ap + a * 54 * 512 + (c) * 1024);                   \
      A[S][a][1] = *reinterpret_cast<const bf16x8*>(ap + a * 54 * 512 + (c) * 1024 + 512);             \
    }                                                                                                  \
    _Pragma("unroll") for (int a = 0; a < 5; ++a) {                                                    \
      B[S][a][0] = *reinterpret_cast<const bf16x8*>(bp + a * 54 * 512 + (c) * 1024);                   \
      B[S][a][1] = *reinterpret_cast<const bf16x8*>(bp + a * 54 * 512 + (c) * 1024 + 512);             \
    }                                                                                                  \
  }
#define G2_COMP(S)                                                                                     \
  {                                                                                                    \
    _Pragma("unroll") for (int s = 0; s < 2; ++s)                                                      \
      _Pragma("unroll") for (int a = 0; a < 4; ++a)                                                    \
        _Pragma("unroll") for (int c = 0; c < 5; ++c)                                                  \
          acc[a][c] = __builtin_amdgcn_mfma_f32_16x16x32_bf16(B[S][c][s], A[S][a][s], acc[a][c], 0, 0, 0); \
  }
#pragma unroll
  for (int i = 0; i < PD - 1; ++i) {
    G2_LOAD(i, i);
    __builtin_amdgcn_sched_barrier(0);
  }
  unsigned short apre[5][4];
  {
    const unsigned short* ab = aT + ((size_t)tm * D_ + n0 + 4 * fq) * 64 + 16 * wid + fr;
#pragma unroll
    for (int c = 0; c < 5; ++c)
#pragma unroll
      for (int i = 0; i < 4; ++i) apre[c][i] = ab[(16 * c + i) * 64];
  }
  const float g = gbuf[m0 + 16 * wid + fr];
  __builtin_amdgcn_sched_barrier(0);
#pragma unroll
  for (int i = 0; i < 7; ++i) {
    if (i + PD - 1 < 7) {
      const int cn = (i + PD - 1 < 6) ? (i + PD - 1) : (nch - 1);
      G2_LOAD((i + PD - 1) % PD, cn);
    }
    __builtin_amdgcn_sched_barrier(0);
    if (i == 6 && nch != 7) {
      const bf16x8 z = {0, 0, 0, 0, 0, 0, 0, 0};
#pragma unroll
      for (int a = 0; a < 4; ++a) { A[i % PD][a][0] = z; A[i % PD][a][1] = z; }
#pragma unroll
      for (int a = 0; a < 5; ++a) { B[i % PD][a][0] = z; B[i % PD][a][1] = z; }
    }
    G2_COMP(i % PD);
    __builtin_amdgcn_sched_barrier(0);
  }
#undef G2_LOAD
#undef G2_COMP

#pragma unroll
  for (int a = 0; a < 4; ++a)
#pragma unroll
    for (int c = 0; c < 5; ++c) red[a][wid][c][lane] = acc[a][c];
  __syncthreads();

  const f32x2 g2 = {g, g};
  const float t2s = rbf(1.0f - g);
  const f32x2 t2 = {t2s, t2s};
  unsigned pk[5][2];
#pragma unroll
  for (int c = 0; c < 5; ++c) {
    const f32x4 sm = (red[wid][0][c][lane] + red[wid][1][c][lane]) + (red[wid][2][c][lane] + red[wid][3][c][lane]);
#pragma unroll
    for (int j = 0; j < 2; ++j) {
      const f32x2 sv = {sm[2 * j], sm[2 * j + 1]};
      const unsigned bb = __builtin_bit_cast(unsigned, __builtin_convertvector(sv, bf16x2n));
      const f32x2 bv = {__uint_as_float(bb << 16), __uint_as_float(bb & 0xffff0000u)};
      const f32x2 av = {bf2f(apre[c][2 * j]), bf2f(apre[c][2 * j + 1])};
      const unsigned b1 = __builtin_bit_cast(unsigned, __builtin_convertvector(g2 * av, bf16x2n));
      const unsigned b3 = __builtin_bit_cast(unsigned, __builtin_convertvector(t2 * bv, bf16x2n));
      const f32x2 t1 = {__uint_as_float(b1 << 16), __uint_as_float(b1 & 0xffff0000u)};
      const f32x2 t3 = {__uint_as_float(b3 << 16), __uint_as_float(b3 & 0xffff0000u)};
      pk[c][j] = __builtin_bit_cast(unsigned, __builtin_convertvector(t1 + t3, bf16x2n));
    }
  }
  unsigned short* op = out + (size_t)(m0 + 16 * wid + fr) * D_ + n0;
#pragma unroll
  for (int cp = 0; cp < 2; ++cp) {
    u32x2 s0 = __builtin_amdgcn_permlane16_swap(pk[2 * cp][0], pk[2 * cp + 1][0], false, false);
    u32x2 s1 = __builtin_amdgcn_permlane16_swap(pk[2 * cp][1], pk[2 * cp + 1][1], false, false);
    u32x4 v = {s0[0], s1[0], s0[1], s1[1]};
    *reinterpret_cast<u32x4*>(op + 32 * cp + 16 * (fq & 1) + 8 * (fq >> 1)) = v;
  }
  *reinterpret_cast<u32x2*>(op + 64 + 4 * fq) = (u32x2){pk[4][0], pk[4][1]};
}

extern "C" void kernel_launch(void* const* d_in, const int* in_sizes, int n_in, void* d_out, int out_size, void* d_ws,
                              size_t ws_size, hipStream_t stream) {
  const unsigned short* u = (const unsigned short*)d_in[0];
  const unsigned short* logAr = (const unsigned short*)d_in[1];
  const unsigned short* Aim = (const unsigned short*)d_in[2];
  const unsigned short* Cre = (const unsigned short*)d_in[3];
  const unsigned short* Cim = (const unsigned short*)d_in[4];
  const unsigned short* logdt = (const unsigned short*)d_in[5];
  const unsigned short* Dsk = (const unsigned short*)d_in[6];
  const unsigned short* w1 = (const unsigned short*)d_in[8];
  const unsigned short* w2 = (const unsigned short*)d_in[9];
  const unsigned short* gw = (const unsigned short*)d_in[10];
  unsigned short* out = (unsigned short*)d_out;

  char* ws = (char*)d_ws;
  unsigned short* hbuf = (unsigned short*)ws;
  unsigned short* aT = (unsigned short*)(ws + (size_t)L_ * F_ * 2);
  unsigned short* w2T = (unsigned short*)(ws + (size_t)L_ * F_ * 2 + (size_t)D_ * L_ * 2);
  float* gbuf = (float*)(ws + (size_t)L_ * F_ * 2 + (size_t)D_ * L_ * 2 + (size_t)D_ * F_ * 2);

  constexpr int K12_LDS = (CONV_LDS > 3 * G1_STAGE) ? CONV_LDS : 3 * G1_STAGE;
  hipLaunchKernelGGL(k12, dim3(K12_GRID), dim3(256), K12_LDS, stream, u, w1, w2, gw, logAr, Aim, Cre, Cim, logdt, Dsk,
                     hbuf, w2T, gbuf, aT);
  hipLaunchKernelGGL(k3_gemm2, dim3(256), dim3(256), 0, stream, hbuf, w2T, aT, gbuf, out);
}
```

```cpp
#include <hip/hip_runtime.h>
#include <hip/hip_bf16.h>
#include <stdint.h>

typedef __attribute__((ext_vector_type(8))) short bf16x8;
typedef __attribute__((ext_vector_type(4))) short bf16x4;
typedef __attribute__((ext_vector_type(16))) float f32x16;
typedef __attribute__((ext_vector_type(4))) float f32x4;
typedef __attribute__((ext_vector_type(2))) float f32x2;
typedef __attribute__((ext_vector_type(2))) __bf16 bf16x2n;
typedef __attribute__((ext_vector_type(4))) unsigned int u32x4;
typedef __attribute__((ext_vector_type(2))) unsigned int u32x2;

#define LDS_AS __attribute__((address_space(3)))
#define RAW_BARRIER() do { asm volatile("s_waitcnt lgkmcnt(0)" ::: "memory"); __builtin_amdgcn_s_barrier(); } while (0)

#define GST(P, V) asm volatile("global_store_dwordx4 %0, %1, off sc0 sc1\n\ts_nop 1" :: "v"(P), "v"(V) : "memory")
constexpr int L_ = 2048, D_ = 640, NS_ = 128, F_ = 1728;

__device__ __forceinline__ float bf2f(unsigned short v) { return __uint_as_float(((unsigned)v) << 16); }
__device__ __forceinline__ unsigned short f2bf(float f) {
  __bf16 b = (__bf16)f;
  return __builtin_bit_cast(unsigned short, b);
}
__device__ __forceinline__ float rbf(float f) { return bf2f(f2bf(f)); }

constexpr int G1_TM = L_ / 128, G1_TN = (F_ + 127) / 128;
constexpr int N_G1 = G1_TM * G1_TN;
constexpr int G1_STAGE = 16384;
constexpr int N_GATE = 32, N_W2 = 90;
constexpr int B_CONV = N_G1, B_GATE = B_CONV + D_, B_W2 = B_GATE + N_GATE, K12_GRID = B_W2 + N_W2;

__device__ __forceinline__ void tr_tiles_load(const unsigned short* __restrict__ src, int ss, int r0, int c0,
                                              u32x4 (&v)[2]) {
  const int tid = threadIdx.x;
#pragma unroll
  for (int i = 0; i < 2; ++i) {
    int id = tid + 256 * i, row = id >> 3, ch = id & 7;
    v[i] = *reinterpret_cast<const u32x4*>(src + (size_t)(r0 + row) * ss + c0 + ch * 8);
  }
}
__device__ __forceinline__ void tr_tiles_put(unsigned short (*tile)[72], const u32x4 (&v)[2]) {
  const int tid = threadIdx.x;
#pragma unroll
  for (int i = 0; i < 2; ++i) {
    int id = tid + 256 * i, row = id >> 3, ch = id & 7;
    *reinterpret_cast<u32x4*>(&tile[row][ch * 8]) = v[i];
  }
}
__device__ __forceinline__ void tr_tiles_write_blocked(unsigned short (*tile)[72], unsigned short* __restrict__ dst,
                                                       int r0, int c0) {
  const int tid = threadIdx.x;
#pragma unroll
  for (int i = 0; i < 2; ++i) {
    int id = tid + 256 * i, col = ((id >> 7) << 4) | (id & 15), ch = (id >> 4) & 7;
    unsigned short o[8];
#pragma unroll
    for (int k = 0; k < 8; ++k) o[k] = tile[ch * 8 + k][col];
    u32x4 w;
#pragma unroll
    for (int j = 0; j < 4; ++j) w[j] = o[2 * j] | ((unsigned)o[2 * j + 1] << 16);
    unsigned short* dp = dst + ((size_t)((c0 + col) >> 4) * 54 + ((r0 + ch * 8) >> 5)) * 512 + (id & 63) * 8;
    GST(dp, w);
  }
}

__device__ void gate_role(int gb, const unsigned short* __restrict__ x, const unsigned short* __restrict__ gw,
                          float* __restrict__ gbuf) {
  const int tid = threadIdx.x;
  const int r0 = gb * 64;
  u32x4 v[10][2];
#pragma unroll
  for (int t = 0; t < 10; ++t) tr_tiles_load(x, D_, r0, t * 64, v[t]);
  float s[2] = {0.f, 0.f};
#pragma unroll
  for (int t = 0; t < 10; ++t) {
    u32x4 g = *reinterpret_cast<const u32x4*>(gw + t * 64 + (tid & 7) * 8);
#pragma unroll
    for (int i = 0; i < 2; ++i)
#pragma unroll
      for (int j = 0; j < 4; ++j) {
        s[i] += rbf(__uint_as_float(v[t][i][j] << 16) * __uint_as_float(g[j] << 16));
        s[i] += rbf(__uint_as_float(v[t][i][j] & 0xffff0000u) * __uint_as_float(g[j] & 0xffff0000u));
      }
  }
#pragma unroll
  for (int i = 0; i < 2; ++i) {
    s[i] += __shfl_xor(s[i], 1); s[i] += __shfl_xor(s[i], 2); s[i] += __shfl_xor(s[i], 4);
    if ((tid & 7) == 0) gbuf[r0 + (tid >> 3) + 32 * i] = rbf(1.0f / (1.0f + expf(-rbf(s[i]))));
  }
}

__device__ void w2_role(int wb, const unsigned short* __restrict__ w2, unsigned short* __restrict__ w2T, char* smem) {
  u32x4 tv[3][2];
#pragma unroll
  for (int j = 0; j < 3; ++j) {
    int id = wb * 3 + j;
    tr_tiles_load(w2, D_, (id / 10) * 64, (id % 10) * 64, tv[j]);
  }
  typedef unsigned short tile_t[64][72];
  tile_t* tiles = reinterpret_cast<tile_t*>(smem);
#pragma unroll
  for (int j = 0; j < 3; ++j) tr_tiles_put(tiles[j], tv[j]);
  __syncthreads();
#pragma unroll
  for (int j = 0; j < 3; ++j) {
    int id = wb * 3 + j;
    tr_tiles_write_blocked(tiles[j], w2T, (id / 10) * 64, (id % 10) * 64);
  }
}

__device__ void gemm1_role(int b, const unsigned short* __restrict__ x, const unsigned short* __restrict__ w1,
                           unsigned short* __restrict__ hbuf, char* smem) {
  const int tid = threadIdx.x, lane = tid & 63, wid = tid >> 6;
  __builtin_amdgcn_s_setprio(3);
  int t = (b & 7) * (N_G1 / 8) + (b >> 3);
  const int tm = t / G1_TN, tn = t % G1_TN;
  const int m0 = tm * 128, n0 = tn * 128;
  const int wm = wid >> 1, wn = wid & 1, r = lane & 31, h = lane >> 5;

  const unsigned short* asrc[2];
  const unsigned short* bsrc[2];
#pragma unroll
  for (int i = 0; i < 2; ++i) {
    int row = (i * 4 + wid) * 16 + (lane >> 2);
    int c = (lane & 3) ^ ((row >> 2) & 3);
    asrc[i] = x + (size_t)(m0 + row) * D_ + c * 8;
    int k = (i * 4 + wid) * 4 + (lane >> 4);
    int cb = (lane & 15) ^ ((k & 3) << 2);
    int col = n0 + cb * 8;
    if (col > F_ - 8) col = F_ - 8;
    bsrc[i] = w1 + (size_t)k * F_ + col;
  }
  auto stage = [&](int kt, int buf) {
    char* sa = smem + buf * G1_STAGE;
    char* sb = sa + 8192;
#pragma unroll
    for (int i = 0; i < 2; ++i)
      __builtin_amdgcn_global_load_lds((const unsigned*)(asrc[i] + kt * 32), (LDS_AS unsigned*)(sa + (i * 4 + wid) * 1024), 16, 0, 0);
#pragma unroll
    for (int i = 0; i < 2; ++i)
      __builtin_amdgcn_global_load_lds((const unsigned*)(bsrc[i] + (size_t)kt * 32 * F_), (LDS_AS unsigned*)(sb + (i * 4 + wid) * 1024), 16, 0, 0);
  };

  f32x16 acc[2][2];
#pragma unroll
  for (int a = 0; a < 2; ++a)
#pragma unroll
    for (int c = 0; c < 2; ++c)
#pragma unroll
      for (int i = 0; i < 16; ++i) acc[a][c][i] = 0.f;

  const int q = (lane & 15) >> 2, p = lane & 3, blk = (lane >> 4) & 1;
  auto compute = [&](int buf) {
    const char* sa = smem + buf * G1_STAGE;
    const char* sb = sa + 8192;
#pragma unroll
    for (int s = 0; s < 2; ++s) {
      bf16x8 af[2], bfr[2];
#pragma unroll
      for (int mt = 0; mt < 2; ++mt) {
        int row = wm * 64 + mt * 32 + r;
        af[mt] = *reinterpret_cast<const bf16x8*>(sa + row * 64 + (((2 * s + h) ^ ((row >> 2) & 3)) << 4));
      }
#pragma unroll
      for (int nt = 0; nt < 2; ++nt)
#pragma unroll
        for (int tt = 0; tt < 2; ++tt) {
          int k = 16 * s + 8 * h + 4 * tt + q;
          int chunk = wn * 8 + nt * 4 + 2 * blk + (p >> 1);
          const char* ad = sb + k * 256 + ((chunk ^ (q << 2)) << 4) + (p & 1) * 8;
          bf16x4 v = __builtin_amdgcn_ds_read_tr16_b64_v4i16((LDS_AS bf16x4*)(ad));
          bfr[nt][4 * tt + 0] = v[0]; bfr[nt][4 * tt + 1] = v[1]; bfr[nt][4 * tt + 2] = v[2]; bfr[nt][4 * tt + 3] = v[3];
        }
#pragma unroll
      for (int mt = 0; mt < 2; ++mt)
#pragma unroll
        for (int nt = 0; nt < 2; ++nt)
          acc[mt][nt] = __builtin_amdgcn_mfma_f32_32x32x16_bf16(bfr[nt], af[mt], acc[mt][nt], 0, 0, 0);
    }
  };

  constexpr int NK = D_ / 32;
  stage(0, 0);
  stage(1, 1);
  int cur = 0, nxt = 2;
  for (int kt = 0; kt < NK - 1; ++kt) {
    asm volatile("s_waitcnt vmcnt(4)" ::: "memory");
    RAW_BARRIER();
    if (kt + 2 < NK) stage(kt + 2, nxt);
    compute(cur);
    cur = (cur == 2) ? 0 : cur + 1;
    nxt = (nxt == 2) ? 0 : nxt + 1;
  }
  asm volatile("" ::: "memory");
  __builtin_amdgcn_s_waitcnt(0x0F70);
  RAW_BARRIER();
  compute(cur);

  if (n0 + wn * 64 < F_) {
    unsigned short* hp = hbuf + ((size_t)((m0 + wm * 64 + r) >> 4) * 54 + ((n0 + wn * 64) >> 5)) * 512 + (h * 16 + (r & 15)) * 8;
#pragma unroll
    for (int mt = 0; mt < 2; ++mt)
#pragma unroll
      for (int nt = 0; nt < 2; ++nt) {
        unsigned pk[8];
#pragma unroll
        for (int j = 0; j < 8; ++j) {
          f32x2 a = {acc[mt][nt][2 * j], acc[mt][nt][2 * j + 1]};
          unsigned hb = __builtin_bit_cast(unsigned, __builtin_convertvector(a, bf16x2n));
          f32x2 hv = {__uint_as_float(hb << 16), __uint_as_float(hb & 0xffff0000u)};
          f32x2 t = hv * (f32x2){-1.4426950408889634f, -1.4426950408889634f};
          f32x2 e = {__builtin_amdgcn_exp2f(t[0]), __builtin_amdgcn_exp2f(t[1])};
          e = e + (f32x2){1.0f, 1.0f};
          f32x2 rc = {__builtin_amdgcn_rcpf(e[0]), __builtin_amdgcn_rcpf(e[1])};
          f32x2 sv = hv * rc;
          pk[j] = __builtin_bit_cast(unsigned, __builtin_convertvector(sv, bf16x2n));
        }
#pragma unroll
        for (int gp = 0; gp < 2; ++gp) {
          u32x2 s0 = __builtin_amdgcn_permlane32_swap(pk[4 * gp + 0], pk[4 * gp + 2], false, false);
          u32x2 s1 = __builtin_amdgcn_permlane32_swap(pk[4 * gp + 1], pk[4 * gp + 3], false, false);
          u32x4 v = {s0[0], s1[0], s0[1], s1[1]};
          GST(hp + (mt * 2 * 54 + nt) * 512 + gp * 256, v);
        }
      }
  }
}

constexpr int UC_STRIDE = 4416;
constexpr int UC_OFF = 0;
constexpr int KROW = 80;
constexpr int KH_OFF = 8 * UC_STRIDE;
constexpr int KL_OFF = KH_OFF + 64 * KROW + 16;
constexpr int KZ_OFF = KH_OFF + 64 * KROW;
constexpr int PR_OFF = KH_OFF + 12288;
constexpr int OS_OFF = KH_OFF;
constexpr int TB_OFF = PR_OFF + 128 * 16;
constexpr int CONV_LDS = TB_OFF + 768 * 4;

__device__ __forceinline__ float thi_of(int i) {
  int t;
  if (i < 8) t = i;
  else if (i < 16) t = 128 + (i - 8);
  else if (i < 24) t = 256 + 128 * ((i - 16) >> 2) + 2 * ((i - 16) & 3);
  else if (i < 32) t = 512 + 128 * ((i - 24) >> 1) + 4 * ((i - 24) & 1);
  else if (i < 40) t = 1024 + 128 * (i - 32);
  else t = 2048;
  return (float)t;
}

struct KTab { unsigned short v[L_]; };
constexpr KTab make_ktab() {
  KTab t{};
  for (int j = 0; j < L_; ++j) {
    int sh = (j < 256) ? 0 : (j < 512) ? 1 : (j < 1024) ? 2 : 3;
    int rr = j;
    if (sh) {
      int q = j >> sh, rem = j & ((1 << sh) - 1), half = 1 << (sh - 1);
      if (rem > half || (rem == half && (q & 1))) ++q;
      rr = q << sh;
    }
    int i = 40;
    if (rr < 256) i = (rr & 7) + 8 * (rr >> 7);
    else if (rr < 512) i = 16 + 4 * ((rr - 256) >> 7) + ((rr & 7) >> 1);
    else if (rr < 1024) i = 24 + 2 * ((rr - 512) >> 7) + ((rr & 7) >> 2);
    else if (rr < 2048) i = 32 + ((rr - 1024) >> 7);
    t.v[j] = (unsigned short)(i * 16 + ((rr >> 3) & 15));
  }
  return t;
}
__device__ __attribute__((aligned(16))) const KTab KTAB = make_ktab();

__device__ void conv_role(int d, const unsigned short* __restrict__ x, const unsigned short* __restrict__ logAr,
                          const unsigned short* __restrict__ Aim, const unsigned short* __restrict__ Cre,
                          const unsigned short* __restrict__ Cim, const unsigned short* __restrict__ logdt,
                          const unsigned short* __restrict__ Dsk, unsigned short* __restrict__ aT, char* smem) {
  const int tid = threadIdx.x, lane = tid & 63, wid = tid >> 6;

  const int pn = tid & (NS_ - 1);
  const unsigned short r_ldt = logdt[d];
  const unsigned short r_lar = logAr[d * NS_ + pn], r_aim = Aim[d * NS_ + pn];
  const unsigned short r_cre = Cre[d * NS_ + pn], r_cim = Cim[d * NS_ + pn];
  const u32x4 kidx = *reinterpret_cast<const u32x4*>(KTAB.v + 8 * tid);
  __builtin_amdgcn_sched_barrier(0);
  unsigned short ug[8];
#pragma unroll
  for (int k = 0; k < 8; ++k) ug[k] = x[(size_t)(8 * tid + k) * D_ + d];
  __builtin_amdgcn_sched_barrier(0);
  asm volatile("" :: "v"(r_ldt), "v"(r_lar), "v"(r_aim), "v"(r_cre), "v"(r_cim));

  const float dt = rbf(expf(bf2f(r_ldt)));
  if (tid < NS_) {
    float are = -rbf(expf(bf2f(r_lar)));
    float aim = bf2f(r_aim);
    float lr = are * dt, li = aim * dt;
    float cr = bf2f(r_cre) * dt, ci = bf2f(r_cim) * dt;
    float mag = sqrtf(cr * cr + ci * ci);
    f32x4 pp;
    pp[0] = lr * 1.4426950408889634f;
    pp[1] = li * 0.15915494309189535f;
    pp[2] = log2f(mag);
    pp[3] = atan2f(ci, cr) * 0.15915494309189535f;
    *reinterpret_cast<f32x4*>(smem + PR_OFF + tid * 16) = pp;
  }
  if (tid < 64) {
    u32x4 z = {0, 0, 0, 0};
    *reinterpret_cast<u32x4*>(smem + UC_OFF + (tid >> 3) * UC_STRIDE + (tid & 7) * 16) = z;
  }
  RAW_BARRIER();

  {
    const int x = lane & 15, kk = lane >> 4;
    float th[3];
#pragma unroll
    for (int T = 0; T < 3; ++T) th[T] = thi_of(16 * T + x);
    const float tlo = (float)(8 * x);
    f32x4 acc[3];
#pragma unroll
    for (int T = 0; T < 3; ++T) acc[T] = (f32x4){0.f, 0.f, 0.f, 0.f};
#pragma unroll 2
    for (int g = 0; g < 8; ++g) {
      int n = 32 * wid + 4 * g + kk;
      f32x4 pp = *reinterpret_cast<const f32x4*>(smem + PR_OFF + n * 16);
      float qe = __builtin_amdgcn_exp2f(pp[0] * tlo);
      float qa = pp[1] * tlo;
      float qr = qe * __builtin_amdgcn_cosf(qa), qi = qe * __builtin_amdgcn_sinf(qa);
#pragma unroll
      for (int T = 0; T < 3; ++T) {
        float pe = __builtin_amdgcn_exp2f(pp[0] * th[T] + pp[2]);
        float pa = pp[1] * th[T] + pp[3];
        float pr = pe * __builtin_amdgcn_cosf(pa), pi = pe * __builtin_amdgcn_sinf(pa);
        acc[T] = __builtin_amdgcn_mfma_f32_16x16x4f32(qr, pr, acc[T], 0, 0, 0);
        acc[T] = __builtin_amdgcn_mfma_f32_16x16x4f32(-qi, pi, acc[T], 0, 0, 0);
      }
    }
    float* os = reinterpret_cast<float*>(smem + OS_OFF) + wid * 768;
#pragma unroll
    for (int T = 0; T < 3; ++T) *reinterpret_cast<f32x4*>(os + (16 * T + x) * 16 + 4 * (kk ^ ((x >> 1) & 3))) = acc[T];
  }
  RAW_BARRIER();

  u32x4 uvec;
#pragma unroll
  for (int j = 0; j < 4; ++j) uvec[j] = (unsigned)ug[2 * j] | ((unsigned)ug[2 * j + 1] << 16);
  *reinterpret_cast<u32x4*>(smem + UC_OFF + 7 * UC_STRIDE + 112 + tid * 16) = uvec;
  {
    unsigned* tab = reinterpret_cast<unsigned*>(smem + TB_OFF);
#pragma unroll
    for (int k = 0; k < 3; ++k) {
      int e = tid + 256 * k;
      const float* osp = reinterpret_cast<const float*>(smem + OS_OFF) + (e ^ ((e >> 3) & 12));
      float kv = 2.0f * ((osp[0] + osp[768]) + (osp[1536] + osp[2304]));
      unsigned short hi = f2bf(kv);
      tab[e] = (unsigned)hi | ((unsigned)f2bf(kv - bf2f(hi)) << 16);
    }
  }
  __syncthreads();
  {
    u32x4 v1 = {0, 0, 0, 0};
    if (tid < 255) v1 = *reinterpret_cast<const u32x4*>(smem + UC_OFF + 7 * UC_STRIDE + 112 + (tid + 1) * 16);
    unsigned w[8] = {uvec[0], uvec[1], uvec[2], uvec[3], v1[0], v1[1], v1[2], v1[3]};
#pragma unroll
    for (int e = 1; e < 8; ++e) {
      u32x4 o;
#pragma unroll
      for (int j = 0; j < 4; ++j) {
        int base = j + (e >> 1);
        if (e & 1) o[j] = (w[base] >> 16) | (w[base + 1] << 16);
        else o[j] = w[base];
      }
      *reinterpret_cast<u32x4*>(smem + UC_OFF + (e - 1) * UC_STRIDE + 128 + tid * 16) = o;
    }
    if (tid == 0) {
#pragma unroll
      for (int e = 1; e < 8; ++e)
#pragma unroll
        for (int k = 0; k < e; ++k)
          *reinterpret_cast<unsigned short*>(smem + UC_OFF + (e - 1) * UC_STRIDE + 2 * (64 - e + k)) =
              (unsigned short)(w[k >> 1] >> (16 * (k & 1)));
    }
  }
  {
    const unsigned* tab = reinterpret_cast<const unsigned*>(smem + TB_OFF);
    unsigned w[8];
#pragma unroll
    for (int k = 0; k < 8; ++k) w[k] = tab[(kidx[k >> 1] >> (16 * (k & 1))) & 0xffff];
    u32x4 vh, vl;
#pragma unroll
    for (int j = 0; j < 4; ++j) {
      vh[j] = (w[7 - 2 * j] & 0xffffu) | (w[6 - 2 * j] << 16);
      vl[j] = (w[7 - 2 * j] >> 16) | (w[6 - 2 * j] & 0xffff0000u);
    }
    *reinterpret_cast<u32x4*>(smem + KH_OFF + (tid >> 2) * KROW + (tid & 3) * 16) = vh;
    *reinterpret_cast<u32x4*>(smem + KL_OFF + (tid >> 2) * KROW + (tid & 3) * 16) = vl;
    if (tid == 0) { u32x4 z = {0, 0, 0, 0}; *reinterpret_cast<u32x4*>(smem + KZ_OFF) = z; }
  }
  __syncthreads();

  f32x16 y0, y1;
#pragma unroll
  for (int i = 0; i < 16; ++i) { y0[i] = 0.f; y1[i] = 0.f; }
  {
    const int r = lane & 31, h = lane >> 5;
    const int e = r & 7;
    const char* ubase = smem + UC_OFF + e * UC_STRIDE + 2 * (r - e + 56 - 8 * h);
#pragma unroll 2
    for (int dl = wid; dl < 32; dl += 4) {
      bf16x8 a0 = *reinterpret_cast<const bf16x8*>(ubase + 64 * dl);
      bf16x8 a1 = *reinterpret_cast<const bf16x8*>(ubase + 64 * dl - 32);
      bf16x8 a2 = *reinterpret_cast<const bf16x8*>(ubase + 64 * dl + 2048);
      bf16x8 a3 = *reinterpret_cast<const bf16x8*>(ubase + 64 * dl + 2048 - 32);
      const int m = r - dl;
      const int off = (m >= 0) ? (m * KROW + 16 * h) : (KZ_OFF - KH_OFF);
      const int off1 = (m >= 0) ? (off + 32) : off;
      const int lo = (m >= 0) ? KL_OFF : KH_OFF;
      bf16x8 fh0 = *reinterpret_cast<const bf16x8*>(smem + KH_OFF + off);
      bf16x8 fh1 = *reinterpret_cast<const bf16x8*>(smem + KH_OFF + off1);
      bf16x8 fl0 = *reinterpret_cast<const bf16x8*>(smem + lo + off);
      bf16x8 fl1 = *reinterpret_cast<const bf16x8*>(smem + lo + off1);
      const int go = (32 + m) * KROW + 16 * h;
      bf16x8 gh0 = *reinterpret_cast<const bf16x8*>(smem + KH_OFF + go);
      bf16x8 gh1 = *reinterpret_cast<const bf16x8*>(smem + KH_OFF + go + 32);
      bf16x8 gl0 = *reinterpret_cast<const bf16x8*>(smem + KL_OFF + go);
      bf16x8 gl1 = *reinterpret_cast<const bf16x8*>(smem + KL_OFF + go + 32);
      y0 = __builtin_amdgcn_mfma_f32_32x32x16_bf16(a0, fh0, y0, 0, 0, 0);
      y1 = __builtin_amdgcn_mfma_f32_32x32x16_bf16(a2, fh0, y1, 0, 0, 0);
      y0 = __builtin_amdgcn_mfma_f32_32x32x16_bf16(a1, fh1, y0, 0, 0, 0);
      y1 = __builtin_amdgcn_mfma_f32_32x32x16_bf16(a3, fh1, y1, 0, 0, 0);
      y0 = __builtin_amdgcn_mfma_f32_32x32x16_bf16(a0, fl0, y0, 0, 0, 0);
      y1 = __builtin_amdgcn_mfma_f32_32x32x16_bf16(a2, fl0, y1, 0, 0, 0);
      y0 = __builtin_amdgcn_mfma_f32_32x32x16_bf16(a1, fl1, y0, 0, 0, 0);
      y1 = __builtin_amdgcn_mfma_f32_32x32x16_bf16(a3, fl1, y1, 0, 0, 0);
      y1 = __builtin_amdgcn_mfma_f32_32x32x16_bf16(a0, gh0, y1, 0, 0, 0);
      y1 = __builtin_amdgcn_mfma_f32_32x32x16_bf16(a1, gh1, y1, 0, 0, 0);
      y1 = __builtin_amdgcn_mfma_f32_32x32x16_bf16(a0, gl0, y1, 0, 0, 0);
      y1 = __builtin_amdgcn_mfma_f32_32x32x16_bf16(a1, gl1, y1, 0, 0, 0);
    }
  }
  __syncthreads();
  {
    const int r = lane & 31, h = lane >> 5;
    float* slab = reinterpret_cast<float*>(smem + wid * 9216);
#pragma unroll
    for (int g = 0; g < 4; ++g) {
      f32x4 v0 = {y0[4 * g], y0[4 * g + 1], y0[4 * g + 2], y0[4 * g + 3]};
      f32x4 v1 = {y1[4 * g], y1[4 * g + 1], y1[4 * g + 2], y1[4 * g + 3]};
      int b = 8 * g + 4 * h;
      *reinterpret_cast<f32x4*>(slab + 36 * r + b) = v0;
      *reinterpret_cast<f32x4*>(slab + 36 * (32 + r) + b) = v1;
    }
  }
  __syncthreads();
  {
    const float dsk = bf2f(Dsk[d]);
    const float* s = reinterpret_cast<const float*>(smem) + (tid >> 2) * 36 + (tid & 3) * 8;
    f32x4 ys[2];
#pragma unroll
    for (int hh = 0; hh < 2; ++hh) {
      f32x4 s0 = *reinterpret_cast<const f32x4*>(s + 4 * hh);
      f32x4 s1 = *reinterpret_cast<const f32x4*>(s + 2304 + 4 * hh);
      f32x4 s2 = *reinterpret_cast<const f32x4*>(s + 4608 + 4 * hh);
      f32x4 s3 = *reinterpret_cast<const f32x4*>(s + 6912 + 4 * hh);
      ys[hh] = (s0 + s1) + (s2 + s3);
    }
    unsigned short o[8];
#pragma unroll
    for (int k = 0; k < 8; ++k) {
      float yb = rbf(ys[k >> 2][k & 3]);
      float uu = bf2f((unsigned short)(uvec[k >> 1] >> (16 * (k & 1))));
      float sk = rbf(uu * dsk);
      o[k] = f2bf(yb + sk);
    }
    u32x4 v;
#pragma unroll
    for (int j = 0; j < 4; ++j) v[j] = o[2 * j] | ((unsigned)o[2 * j + 1] << 16);
    GST(aT + ((size_t)(tid >> 3) * D_ + d) * 64 + 8 * (tid & 7), v);
  }
}

__global__ __launch_bounds__(256, 3) void k12(const unsigned short* __restrict__ x, const unsigned short* __restrict__ w1,
                                              const unsigned short* __restrict__ w2, const unsigned short* __restrict__ gw,
                                              const unsigned short* __restrict__ logAr, const unsigned short* __restrict__ Aim,
                                              const unsigned short* __restrict__ Cre, const unsigned short* __restrict__ Cim,
                                              const unsigned short* __restrict__ logdt, const unsigned short* __restrict__ Dsk,
                                              unsigned short* __restrict__ hbuf, unsigned short* __restrict__ w2T,
                                              float* __restrict__ gbuf, unsigned short* __restrict__ aT) {
  extern __shared__ __attribute__((aligned(16))) char smem[];
  const int b = blockIdx.x;
  constexpr int G0 = 0;
  if (b >= G0 && b < G0 + D_) conv_role(b - G0, x, logAr, Aim, Cre, Cim, logdt, Dsk, aT, smem);
  else if (b < B_GATE) gemm1_role(b < G0 ? b : b - D_, x, w1, hbuf, smem);
  else if (b < B_W2) gate_role(b - B_GATE, x, gw, gbuf);
  else w2_role(b - B_W2, w2, w2T, smem);
}

__global__ __launch_bounds__(256, 1) void k3_gemm2(const unsigned short* __restrict__ hbuf, const unsigned short* __restrict__ w2T,
                                                   const unsigned short* __restrict__ aT, const float* __restrict__ gbuf,
                                                   unsigned short* __restrict__ out) {
  __shared__ f32x4 red[4][4][5][64];
  const int tid = threadIdx.x, lane = tid & 63, wid = tid >> 6;
  const int xc = blockIdx.x & 7, jj = blockIdx.x >> 3;
  const int tm = (xc >> 1) * 8 + (jj >> 2), tn = (xc & 1) * 4 + (jj & 3);
  const int m0 = tm * 64, n0 = tn * 80;
  const int fr = lane & 15, fq = lane >> 4;
  const int wv = __builtin_amdgcn_readfirstlane(wid);
  const int c0 = (wv * 27) >> 2;
  const int nch = (((wv + 1) * 27) >> 2) - c0;

  const unsigned short* ap = hbuf + (size_t)(m0 >> 4) * 54 * 512 + lane * 8 + c0 * 1024;
  const unsigned short* bp = w2T + (size_t)(n0 >> 4) * 54 * 512 + lane * 8 + c0 * 1024;

  f32x4 acc[4][5];
#pragma unroll
  for (int a = 0; a < 4; ++a)
#pragma unroll
    for (int c = 0; c < 5; ++c) acc[a][c] = (f32x4){0.f, 0.f, 0.f, 0.f};

  constexpr int PD = 2;
  bf16x8 A[PD][4][2], B[PD][5][2];
#define G2_LOAD(S, c)                                                                                  \
  {                                                                                                    \
    _Pragma("unroll") for (int a = 0; a < 4; ++a) {                                                    \
      A[S][a][0] = *reinterpret_cast<const bf16x8*>(ap + a * 54 * 512 + (c) * 1024);                   \
      A[S][a][1] = *reinterpret_cast<const bf16x8*>(ap + a * 54 * 512 + (c) * 1024 + 512);             \
    }                                                                                                  \
    _Pragma("unroll") for (int a = 0; a < 5; ++a) {                                                    \
      B[S][a][0] = *reinterpret_cast<const bf16x8*>(bp + a * 54 * 512 + (c) * 1024);                   \
      B[S][a][1] = *reinterpret_cast<const bf16x8*>(bp + a * 54 * 512 + (c) * 1024 + 512);             \
    }                                                                                                  \
  }
#define G2_COMP(S)                                                                                     \
  {                                                                                                    \
    _Pragma("unroll") for (int s = 0; s < 2; ++s)                                                      \
      _Pragma("unroll") for (int a = 0; a < 4; ++a)                                                    \
        _Pragma("unroll") for (int c = 0; c < 5; ++c)                                                  \
          acc[a][c] = __builtin_amdgcn_mfma_f32_16x16x32_bf16(B[S][c][s], A[S][a][s], acc[a][c], 0, 0, 0); \
  }
#pragma unroll
  for (int i = 0; i < PD - 1; ++i) {
    G2_LOAD(i, i);
    __builtin_amdgcn_sched_barrier(0);
  }
  unsigned short apre[5][4];
  {
    const unsigned short* ab = aT + ((size_t)tm * D_ + n0 + 4 * fq) * 64 + 16 * wid + fr;
#pragma unroll
    for (int c = 0; c < 5; ++c)
#pragma unroll
      for (int i = 0; i < 4; ++i) apre[c][i] = ab[(16 * c + i) * 64];
  }
  const float g = gbuf[m0 + 16 * wid + fr];
  __builtin_amdgcn_sched_barrier(0);
#pragma unroll
  for (int i = 0; i < 7; ++i) {
    if (i + PD - 1 < 7) {
      if (i + PD - 1 == 6 && nch != 7) { ap -= lane * 8 + 6 * 1024; bp -= lane * 8 + 6 * 1024; }
      G2_LOAD((i + PD - 1) % PD, i + PD - 1);
    }
    __builtin_amdgcn_sched_barrier(0);
    if (i == 6 && nch != 7) {
#pragma unroll
      for (int a = 0; a < 4; ++a) asm volatile("" :: "v"(A[i % PD][a][0]), "v"(A[i % PD][a][1]));
#pragma unroll
      for (int a = 0; a < 5; ++a) asm volatile("" :: "v"(B[i % PD][a][0]), "v"(B[i % PD][a][1]));
    } else {
      G2_COMP(i % PD);
    }
    __builtin_amdgcn_sched_barrier(0);
  }
#undef G2_LOAD
#undef G2_COMP

#pragma unroll
  for (int a = 0; a < 4; ++a)
#pragma unroll
    for (int c = 0; c < 5; ++c) red[a][wid][c][lane] = acc[a][c];
  __syncthreads();

  const f32x2 g2 = {g, g};
  const float t2s = rbf(1.0f - g);
  const f32x2 t2 = {t2s, t2s};
  unsigned pk[5][2];
#pragma unroll
  for (int c = 0; c < 5; ++c) {
    const f32x4 sm = (red[wid][0][c][lane] + red[wid][1][c][lane]) + (red[wid][2][c][lane] + red[wid][3][c][lane]);
#pragma unroll
    for (int j = 0; j < 2; ++j) {
      const f32x2 sv = {sm[2 * j], sm[2 * j + 1]};
      const unsigned bb = __builtin_bit_cast(unsigned, __builtin_convertvector(sv, bf16x2n));
      const f32x2 bv = {__uint_as_float(bb << 16), __uint_as_float(bb & 0xffff0000u)};
      const f32x2 av = {bf2f(apre[c][2 * j]), bf2f(apre[c][2 * j + 1])};
      const unsigned b1 = __builtin_bit_cast(unsigned, __builtin_convertvector(g2 * av, bf16x2n));
      const unsigned b3 = __builtin_bit_cast(unsigned, __builtin_convertvector(t2 * bv, bf16x2n));
      const f32x2 t1 = {__uint_as_float(b1 << 16), __uint_as_float(b1 & 0xffff0000u)};
      const f32x2 t3 = {__uint_as_float(b3 << 16), __uint_as_float(b3 & 0xffff0000u)};
      pk[c][j] = __builtin_bit_cast(unsigned, __builtin_convertvector(t1 + t3, bf16x2n));
    }
  }
  unsigned short* op = out + (size_t)(m0 + 16 * wid + fr) * D_ + n0;
#pragma unroll
  for (int cp = 0; cp < 2; ++cp) {
    u32x2 s0 = __builtin_amdgcn_permlane16_swap(pk[2 * cp][0], pk[2 * cp + 1][0], false, false);
    u32x2 s1 = __builtin_amdgcn_permlane16_swap(pk[2 * cp][1], pk[2 * cp + 1][1], false, false);
    u32x4 v = {s0[0], s1[0], s0[1], s1[1]};
    *reinterpret_cast<u32x4*>(op + 32 * cp + 16 * (fq & 1) + 8 * (fq >> 1)) = v;
  }
  *reinterpret_cast<u32x2*>(op + 64 + 4 * fq) = (u32x2){pk[4][0], pk[4][1]};
}

extern "C" void kernel_launch(void* const* d_in, const int* in_sizes, int n_in, void* d_out, int out_size, void* d_ws,
                              size_t ws_size, hipStream_t stream) {
  const unsigned short* u = (const unsigned short*)d_in[0];
  const unsigned short* logAr = (const unsigned short*)d_in[1];
  const unsigned short* Aim = (const unsigned short*)d_in[2];
  const unsigned short* Cre = (const unsigned short*)d_in[3];
  const unsigned short* Cim = (const unsigned short*)d_in[4];
  const unsigned short* logdt = (const unsigned short*)d_in[5];
  const unsigned short* Dsk = (const unsigned short*)d_in[6];
  const unsigned short* w1 = (const unsigned short*)d_in[8];
  const unsigned short* w2 = (const unsigned short*)d_in[9];
  const unsigned short* gw = (const unsigned short*)d_in[10];
  unsigned short* out = (unsigned short*)d_out;

  char* ws = (char*)d_ws;
  unsigned short* hbuf = (unsigned short*)ws;
  unsigned short* aT = (unsigned short*)(ws + (size_t)L_ * F_ * 2);
  unsigned short* w2T = (unsigned short*)(ws + (size_t)L_ * F_ * 2 + (size_t)D_ * L_ * 2);
  float* gbuf = (float*)(ws + (size_t)L_ * F_ * 2 + (size_t)D_ * L_ * 2 + (size_t)D_ * F_ * 2);

  constexpr int K12_LDS = (CONV_LDS > 3 * G1_STAGE) ? CONV_LDS : 3 * G1_STAGE;
  hipLaunchKernelGGL(k12, dim3(K12_GRID), dim3(256), K12_LDS, stream, u, w1, w2, gw, logAr, Aim, Cre, Cim, logdt, Dsk,
                     hbuf, w2T, gbuf, aT);
  hipLaunchKernelGGL(k3_gemm2, dim3(256), dim3(256), 0, stream, hbuf, w2T, aT, gbuf, out);
}
```
